```python
import torch

def _softmax(x, axis=-1):
    return torch.softmax(x, dim=axis)

def _gelu(x):
    return torch.nn.functional.gelu(x, approximate='tanh')

def _silu(x):
    return torch.nn.functional.silu(x)

def _sigmoid(x):
    return torch.sigmoid(x)

def _layernorm(x, axis=-1, eps=1e-05):
    m = x.mean(dim=axis, keepdim=True)
    v = x.var(dim=axis, keepdim=True, unbiased=False)
    return (x - m) / torch.sqrt(v + eps)

def _scatter_add(tgt, idx, src, axis=0):
    return tgt.clone().index_add_(axis, idx, src)

def _segment_sum(src, seg, n):
    out = torch.zeros((n,) + src.shape[1:], dtype=src.dtype)
    return out.index_add_(0, seg, src)

def _pad(x, pw):
    flat = tuple((v for pair in reversed(pw) for v in pair))
    return torch.nn.functional.pad(x, flat)
kernel_name = 'adaLN_cond__compressed_kv_cla__swiglu_ffn__decode'

def setup_inputs():

    _w = lambda a: (lambda t: t.to(torch.bfloat16) if t.is_floating_point() else t)(torch.as_tensor(a))

    def _A_setup():
        B, H, Sq, Sk, D, M = (2, 32, 1, 8192, 128, 640)
        Dc = 4 * D
        return {'x': _w((lambda *s: torch.randn(*s, dtype=torch.bfloat16) * 0.05)(B, Sq, M)), 'c_kv': _w((lambda *s: torch.randn(*s, dtype=torch.bfloat16) * 1.6)(B, Sk, Dc)), 'w_q': _w((lambda *s: torch.randn(*s, dtype=torch.bfloat16) * 1.6)(M, H, D) * M ** (-0.5)), 'w_uk': _w((lambda *s: torch.randn(*s, dtype=torch.bfloat16) * 1.6)(Dc, H, D) * Dc ** (-0.5)), 'w_uv': _w((lambda *s: torch.randn(*s, dtype=torch.bfloat16) * 1.6)(Dc, H, D) * Dc ** (-0.5)), 'w_o': _w((lambda *s: torch.randn(*s, dtype=torch.bfloat16) * 1.6)(H, D, M) * (H * D) ** (-0.5))}
    _A_in = _A_setup()

    def _B_setup():
        B, S, D, F = (2, 1, 640, 1728)
        return {'x': _w((lambda *s: torch.randn(*s, dtype=torch.bfloat16) * 0.05)(B, S, D)), 'w_gate': _w((lambda *s: torch.randn(*s, dtype=torch.bfloat16) * 0.05)(D, F)), 'w_up': _w((lambda *s: torch.randn(*s, dtype=torch.bfloat16) * 0.05)(D, F)), 'w_down': _w((lambda *s: torch.randn(*s, dtype=torch.bfloat16) * 0.05)(F, D))}
    _B_in = _B_setup()
    return {'A_x': _A_in['x'], 'A_c_kv': _A_in['c_kv'], 'A_w_q': _A_in['w_q'], 'A_w_uk': _A_in['w_uk'], 'A_w_uv': _A_in['w_uv'], 'A_w_o': _A_in['w_o'], 'B_x': _B_in['x'], 'B_w_gate': _B_in['w_gate'], 'B_w_up': _B_in['w_up'], 'B_w_down': _B_in['w_down'], 'c': _w((lambda *s: torch.randn(*s, dtype=torch.bfloat16) * 0.2)(2, 640) * 8.0), 'w_mod': _w((lambda *s: torch.randn(*s, dtype=torch.bfloat16) * 0.2)(640, 6 * 640) * 640 ** (-0.5) * 8.0)}

def reference(A_x, A_c_kv, A_w_q, A_w_uk, A_w_uv, A_w_o, B_x, B_w_gate, B_w_up, B_w_down, c, w_mod):

    def _A_ref(x, c_kv, w_q, w_uk, w_uv, w_o):
        B, Sq, M = (x.shape[0], x.shape[1], x.shape[2])
        Sk = c_kv.shape[1]
        H, D = (w_q.shape[1], w_q.shape[2])
        q = torch.einsum('bqm,mhd->bhqd', x, w_q)
        k = torch.einsum('bkc,chd->bhkd', c_kv, w_uk)
        v = torch.einsum('bkc,chd->bhkd', c_kv, w_uv)
        scores = torch.einsum('bhqd,bhkd->bhqk', q, k) * D ** (-0.5)
        off = Sk - Sq
        qi = torch.arange(Sq)[:, None]
        kj = torch.arange(Sk)[None, :]
        cmask = kj <= qi + off
        scores = torch.where(cmask[None, None], scores, -1000000000.0)
        p = _softmax(scores, axis=-1)
        ctx = torch.einsum('bhqk,bhkd->bhqd', p, v)
        return torch.einsum('bhqd,hdm->bqm', ctx, w_o)

    def _B_ref(x, w_gate, w_up, w_down):
        g = _silu(x @ w_gate)
        u = x @ w_up
        return g * u @ w_down
    x = A_x
    mod = (_silu(c) @ w_mod).reshape(c.shape[0], 6, 1, -1)
    sa, ca, ga, sb, cb, gb = (mod[:, 0].clone(), mod[:, 1].clone(), mod[:, 2].clone(), mod[:, 3].clone(), mod[:, 4].clone(), mod[:, 5].clone())
    h = _layernorm(x, axis=-1) * (1 + ca) + sa
    x = x + ga * _A_ref(h, A_c_kv, A_w_q, A_w_uk, A_w_uv, A_w_o)
    h = _layernorm(x, axis=-1) * (1 + cb) + sb
    x = x + gb * _B_ref(h, B_w_gate, B_w_up, B_w_down)
    return x
```

```cpp
#include <hip/hip_runtime.h>
#include <hip/hip_bf16.h>
#include <stdint.h>

typedef unsigned short u16;
__device__ __forceinline__ float bf2f(u16 b) { return __uint_as_float(((unsigned)b) << 16); }
__device__ __forceinline__ u16 f2bf(float f) { unsigned u = __float_as_uint(f); u += 0x7FFFu + ((u >> 16) & 1u); return (u16)(u >> 16); }
__device__ __forceinline__ float rbf(float f) { return bf2f(f2bf(f)); }

constexpr int B = 2, H = 32, SK = 8192, D = 128, M = 640, DC = 512, F = 1728;

template <int MODE>
__global__ void gemv_cols(const float* __restrict__ in, const u16* __restrict__ W, float* __restrict__ out, int K, int N) {
  __shared__ float red[4][2][64];
  const int lane = threadIdx.x & 63, w = threadIdx.x >> 6;
  const int n = blockIdx.x * 64 + lane;
  float a0 = 0.f, a1 = 0.f;
  if (n < N) {
    for (int k = w; k < K; k += 4) {
      float wv = bf2f(W[(size_t)k * N + n]);
      a0 += in[k] * wv; a1 += in[K + k] * wv;
    }
  }
  red[w][0][lane] = a0; red[w][1][lane] = a1;
  __syncthreads();
  if (w == 0 && n < N) {
    out[n] = red[0][0][lane] + red[1][0][lane] + red[2][0][lane] + red[3][0][lane];
    out[N + n] = red[0][1][lane] + red[1][1][lane] + red[2][1][lane] + red[3][1][lane];
  }
}

__global__ void k_silu_c(const u16* c, float* sc) {
  int i = blockIdx.x * blockDim.x + threadIdx.x;
  if (i < B * M) { float v = bf2f(c[i]); sc[i] = rbf(v / (1.f + __expf(-v))); }
}

__global__ void k_ln_mod(const float* x, const float* mod, int s_idx, int c_idx, float* h) {
  __shared__ float sred[16];
  const int b = blockIdx.x, t = threadIdx.x;
  float v = x[b * M + t];
  float s = v;
  for (int o = 32; o > 0; o >>= 1) s += __shfl_xor(s, o);
  if ((t & 63) == 0) sred[t >> 6] = s;
  __syncthreads();
  float tot = 0.f; for (int i = 0; i < 10; ++i) tot += sred[i];
  float mean = tot / M;
  __syncthreads();
  float d = v - mean; float q = d * d;
  for (int o = 32; o > 0; o >>= 1) q += __shfl_xor(q, o);
  if ((t & 63) == 0) sred[t >> 6] = q;
  __syncthreads();
  tot = 0.f; for (int i = 0; i < 10; ++i) tot += sred[i];
  float var = tot / M;
  float n = d / sqrtf(var + 1e-5f);
  h[b * M + t] = n * (1.f + mod[b * 6 * M + c_idx * M + t]) + mod[b * 6 * M + s_idx * M + t];
}

__global__ void k_cvt(const u16* x, float* o, int n) { int i = blockIdx.x * blockDim.x + threadIdx.x; if (i < n) o[i] = bf2f(x[i]); }

__global__ void __launch_bounds__(512) k_qabs(const float* q, const u16* wuk, float* qabs) {
  const int h = blockIdx.x, c = threadIdx.x;
  float a0 = 0.f, a1 = 0.f;
  const u16* wp = wuk + ((size_t)c * H + h) * D;
  for (int d = 0; d < D; ++d) { float wv = bf2f(wp[d]); a0 += q[h * D + d] * wv; a1 += q[H * D + h * D + d] * wv; }
  const float sc = 0.08838834764831845f;
  qabs[(0 * H + h) * DC + c] = a0 * sc; qabs[(1 * H + h) * DC + c] = a1 * sc;
}

constexpr int KC = 64, NCH = SK / KC;
__global__ void __launch_bounds__(256) k_attn(const float* qabs, const u16* ckv, float* part, float* ml) {
  __shared__ float s[H][KC + 1];
  __shared__ u16 kv[KC][DC + 8];
  const int b = blockIdx.x / NCH, ch = blockIdx.x % NCH, t = threadIdx.x;
  const u16* src = ckv + ((size_t)b * SK + ch * KC) * DC;
  for (int i = t; i < KC * DC; i += 256) kv[i / DC][i % DC] = src[i];
  __syncthreads();
  for (int i = t; i < H * KC; i += 256) {
    int h = i / KC, k = i % KC; float a = 0.f;
    const float* qp = qabs + (b * H + h) * DC;
    for (int c = 0; c < DC; ++c) a += qp[c] * bf2f(kv[k][c]);
    s[h][k] = a;
  }
  __syncthreads();
  if (t < H) {
    float m = -INFINITY; for (int k = 0; k < KC; ++k) m = fmaxf(m, s[t][k]);
    float l = 0.f; for (int k = 0; k < KC; ++k) { float p = __expf(s[t][k] - m); s[t][k] = p; l += p; }
    ml[((b * H + t) * NCH + ch) * 2] = m; ml[((b * H + t) * NCH + ch) * 2 + 1] = l;
  }
  __syncthreads();
  for (int i = t; i < H * DC; i += 256) {
    int h = i / DC, c = i % DC; float a = 0.f;
    for (int k = 0; k < KC; ++k) a += s[h][k] * bf2f(kv[k][c]);
    part[((size_t)(b * H + h) * NCH + ch) * DC + c] = a;
  }
}

__global__ void __launch_bounds__(512) k_comb(const float* part, const float* ml, const u16* wuv, float* ctx) {
  __shared__ float cc[DC]; __shared__ float wgt[NCH];
  const int bh = blockIdx.x, h = bh % H, t = threadIdx.x;
  float m = -INFINITY; for (int j = 0; j < NCH; ++j) m = fmaxf(m, ml[(bh * NCH + j) * 2]);
  float l = 0.f; for (int j = 0; j < NCH; ++j) l += ml[(bh * NCH + j) * 2 + 1] * __expf(ml[(bh * NCH + j) * 2] - m);
  if (t < NCH) wgt[t] = __expf(ml[(bh * NCH + t) * 2] - m) / l;
  __syncthreads();
  float a = 0.f; for (int j = 0; j < NCH; ++j) a += wgt[j] * part[((size_t)bh * NCH + j) * DC + t];
  cc[t] = a;
  __syncthreads();
  if (t < D) { float o = 0.f; for (int c = 0; c < DC; ++c) o += cc[c] * bf2f(wuv[((size_t)c * H + h) * D + t]); ctx[bh * D + t] = o; }
}

__global__ void k_res(const float* x, const float* mod, int g_idx, const float* y, float* xo, u16* obf) {
  int i = blockIdx.x * blockDim.x + threadIdx.x; if (i >= B * M) return;
  int b = i / M, m = i % M;
  float v = x[i] + mod[b * 6 * M + g_idx * M + m] * y[i];
  if (xo) xo[i] = v; if (obf) obf[i] = f2bf(v);
}
__global__ void k_act(const float* g, const float* u, float* a) {
  int i = blockIdx.x * blockDim.x + threadIdx.x; if (i >= B * F) return;
  float gv = g[i]; a[i] = gv / (1.f + __expf(-gv)) * u[i];
}

extern "C" void kernel_launch(void* const* d_in, const int* in_sizes, int n_in, void* d_out, int out_size, void* d_ws, size_t ws_size, hipStream_t stream) {
  const u16* A_x = (const u16*)d_in[0]; const u16* ckv = (const u16*)d_in[1]; const u16* wq = (const u16*)d_in[2];
  const u16* wuk = (const u16*)d_in[3]; const u16* wuv = (const u16*)d_in[4]; const u16* wo = (const u16*)d_in[5];
  const u16* wg = (const u16*)d_in[7]; const u16* wu = (const u16*)d_in[8]; const u16* wd = (const u16*)d_in[9];
  const u16* c = (const u16*)d_in[10]; const u16* wmod = (const u16*)d_in[11];
  float* ws = (float*)d_ws;
  float* sc = ws; float* mod = sc + 2048; float* x0 = mod + 8192; float* h = x0 + 2048; float* q = h + 2048; float* qabs = q + 8192;
  float* ctx = qabs + 32768; float* ao = ctx + 8192; float* x1 = ao + 2048; float* g = x1 + 2048; float* u = g + 4096; float* act = u + 4096;
  float* dn = act + 4096; float* ml = dn + 2048; float* part = ml + 64 * NCH * 2;
  k_silu_c<<<5, 256, 0, stream>>>(c, sc);
  gemv_cols<0><<<60, 256, 0, stream>>>(sc, wmod, mod, M, 6 * M);
  k_cvt<<<5, 256, 0, stream>>>(A_x, x0, B * M);
  k_ln_mod<<<2, 640, 0, stream>>>(x0, mod, 0, 1, h);
  gemv_cols<0><<<64, 256, 0, stream>>>(h, wq, q, M, H * D);
  k_qabs<<<32, 512, 0, stream>>>(q, wuk, qabs);
  k_attn<<<B * NCH, 256, 0, stream>>>(qabs, ckv, part, ml);
  k_comb<<<64, 512, 0, stream>>>(part, ml, wuv, ctx);
  gemv_cols<0><<<10, 256, 0, stream>>>(ctx, wo, ao, H * D, M);
  k_res<<<5, 256, 0, stream>>>(x0, mod, 2, ao, x1, nullptr);
  k_ln_mod<<<2, 640, 0, stream>>>(x1, mod, 3, 4, h);
  gemv_cols<0><<<27, 256, 0, stream>>>(h, wg, g, M, F);
  gemv_cols<0><<<27, 256, 0, stream>>>(h, wu, u, M, F);
  k_act<<<14, 256, 0, stream>>>(g, u, act);
  gemv_cols<0><<<10, 256, 0, stream>>>(act, wd, dn, F, M);
  k_res<<<5, 256, 0, stream>>>(x1, mod, 5, dn, nullptr, (u16*)d_out);
}
```

```cpp
#include <hip/hip_runtime.h>
#include <stdint.h>

#define GAS __attribute__((address_space(1)))
#define LAS __attribute__((address_space(3)))
typedef unsigned v4u __attribute__((ext_vector_type(4)));
typedef unsigned v2u __attribute__((ext_vector_type(2)));
typedef float f32x4 __attribute__((ext_vector_type(4)));
typedef float f32x2 __attribute__((ext_vector_type(2)));
typedef __bf16 bf16x8 __attribute__((ext_vector_type(8)));
typedef __bf16 bf16x2 __attribute__((ext_vector_type(2)));
typedef short s16x4 __attribute__((ext_vector_type(4)));
typedef unsigned short u16;
typedef unsigned long long u64;

constexpr int NB = 2, NH = 32, SK = 8192, DH = 128, DM = 640, DC = 512, DF = 1728;
constexpr int OFF_EPOCH = 0;
constexpr int OFF_FLAGS = 4096;
constexpr int OFF_EFLAGS = OFF_FLAGS + 256 * 128;
constexpr int OFF_X1 = OFF_EFLAGS + 128 * 128;
constexpr int OFF_R = 81920;
constexpr int X2 = OFF_R;
constexpr int X3 = X2 + 65536;
constexpr int X4 = X3 + 65536;
constexpr int X4S = X4 + 4194304;
constexpr int X5 = X4S + 32768;
constexpr int X6 = X5 + 65536;
constexpr int X7 = X6 + 20480;
constexpr int XM = X7 + 138240;
constexpr int R_END = XM + 10240;
static_assert(OFF_X1 + 20480 <= OFF_R, "map");
constexpr int WS_USED = R_END;
#define CKV_A 0
#define CKV_B 6
constexpr unsigned SENT = 0xFFFFFFFFu;
constexpr int AUX_SC1 = 16;
constexpr unsigned SPIN_MAX = 1u << 18;
constexpr int L_CKV = 0;
constexpr int L_QIMG = 131072;
constexpr int L_PIMG = L_QIMG + 16384;
constexpr int L_RED = L_PIMG + 4096;
constexpr int L_MISC = L_RED + 8192;
constexpr int LDS_BYTES = L_MISC + 2048;
static_assert(LDS_BYTES <= 163840, "LDS");

#define STAMP(k) do { } while (0)
#define STAMPW(k, wv) do { } while (0)

struct Params {
  const u16 *x, *ckv, *wq, *wuk, *wuv, *wo, *wg, *wu, *wd, *c, *wmod;
  u16* out;
  unsigned char* ws;
};

#define BAR() do { asm volatile("s_waitcnt lgkmcnt(0)" ::: "memory"); __builtin_amdgcn_s_barrier(); asm volatile("" ::: "memory"); } while (0)
#define DRAIN() asm volatile("s_waitcnt vmcnt(0)" ::: "memory")
#define PIN() __builtin_amdgcn_sched_barrier(0)

__device__ __forceinline__ float bflo(unsigned d) { return __uint_as_float(d << 16); }
__device__ __forceinline__ float bfhi(unsigned d) { return __uint_as_float(d & 0xffff0000u); }
__device__ __forceinline__ unsigned pk2(float lo, float hi) { f32x2 f = {lo, hi}; return __builtin_bit_cast(unsigned, __builtin_convertvector(f, bf16x2)); }
__device__ __forceinline__ unsigned pk2s(float lo, float hi) { unsigned d = pk2(lo, hi); return d < 0xFFFFFFFEu ? d : 0xFFFFFFFEu; }
__device__ __forceinline__ f32x4 mfma16(v4u a, v4u b, f32x4 c) { return __builtin_amdgcn_mfma_f32_16x16x32_bf16(__builtin_bit_cast(bf16x8, a), __builtin_bit_cast(bf16x8, b), c, 0, 0, 0); }
__device__ __forceinline__ float dot2(unsigned a, unsigned b, float c) { return __builtin_amdgcn_fdot2_f32_bf16(__builtin_bit_cast(bf16x2, a), __builtin_bit_cast(bf16x2, b), c, false); }
__device__ __forceinline__ v4u ldnt(const u16* p) { return __builtin_nontemporal_load((const GAS v4u*)p); }
__device__ __forceinline__ unsigned umax4(v4u v) { unsigned a = v.x > v.y ? v.x : v.y, b = v.z > v.w ? v.z : v.w; return a > b ? a : b; }
__device__ __forceinline__ float x16_max(float x) {
  auto s = __builtin_amdgcn_permlane16_swap(__float_as_uint(x), __float_as_uint(x), false, false);
  x = fmaxf(__uint_as_float(s[0]), __uint_as_float(s[1]));
  auto t = __builtin_amdgcn_permlane32_swap(__float_as_uint(x), __float_as_uint(x), false, false);
  return fmaxf(__uint_as_float(t[0]), __uint_as_float(t[1]));
}
__device__ __forceinline__ float x16_sum(float x) {
  auto s = __builtin_amdgcn_permlane16_swap(__float_as_uint(x), __float_as_uint(x), false, false);
  x = __uint_as_float(s[0]) + __uint_as_float(s[1]);
  auto t = __builtin_amdgcn_permlane32_swap(__float_as_uint(x), __float_as_uint(x), false, false);
  return __uint_as_float(t[0]) + __uint_as_float(t[1]);
}
__device__ __forceinline__ float rs32(float a, float b) { auto t = __builtin_amdgcn_permlane32_swap(__float_as_uint(a), __float_as_uint(b), false, false); return __uint_as_float(t[0]) + __uint_as_float(t[1]); }
__device__ __forceinline__ float rs16(float a, float b) { auto t = __builtin_amdgcn_permlane16_swap(__float_as_uint(a), __float_as_uint(b), false, false); return __uint_as_float(t[0]) + __uint_as_float(t[1]); }
template <int CTRL> __device__ __forceinline__ float dppf(float x) { return __builtin_bit_cast(float, __builtin_amdgcn_mov_dpp(__builtin_bit_cast(int, x), CTRL, 0xf, 0xf, true)); }
__device__ __forceinline__ float row16_sum(float x) {
  x += dppf<0xB1>(x); x += dppf<0x4E>(x); x += dppf<0x141>(x); x += dppf<0x128>(x); return x;
}
__device__ __forceinline__ float wave_sum(float x) { return x16_sum(row16_sum(x)); }
__device__ __forceinline__ float silu(float v) { return v / (1.f + __expf(-v)); }

__device__ __forceinline__ void tr8(const v4u (&w)[8], v4u (&f)[8]) {
#pragma unroll
  for (int i2 = 0; i2 < 4; ++i2) {
#pragma unroll
    for (int d = 0; d < 4; ++d) {
      f[2 * i2][d] = __builtin_amdgcn_perm(w[2 * d + 1][i2], w[2 * d][i2], 0x05040100u);
      f[2 * i2 + 1][d] = __builtin_amdgcn_perm(w[2 * d + 1][i2], w[2 * d][i2], 0x07060302u);
    }
  }
}
__device__ __forceinline__ v4u silu8(v4u c) {
  v4u r;
#pragma unroll
  for (int d = 0; d < 4; ++d) r[d] = pk2(silu(bflo(c[d])), silu(bfhi(c[d])));
  return r;
}

__global__ void __launch_bounds__(512, 2) mega(Params p) {
  __shared__ __attribute__((aligned(16))) unsigned char smem[LDS_BYTES];
  LAS unsigned char* L = (LAS unsigned char*)smem;
  const int tid = threadIdx.x, l = tid & 63, w = __builtin_amdgcn_readfirstlane(tid >> 6);
  const int bid = blockIdx.x;
  const int g = l >> 4, r16 = l & 15;
  STAMP(0);
  const __amdgpu_buffer_rsrc_t rs = __builtin_amdgcn_make_buffer_rsrc((void*)p.ws, 0, WS_USED, 0x00020000);
  const bool Ltype = bid >= 128;
  const int idx = bid & 127;
  const int ab = idx >> 6, aj = idx & 63, hg = bid >> 7;
  const unsigned epoch_raw = __builtin_amdgcn_raw_buffer_load_b32(rs, OFF_EPOCH, 0, AUX_SC1);
  auto TAG = [&]() { unsigned t = epoch_raw; asm volatile("" : "+v"(t)); return t + 1u; };

  auto ld16 = [&](int off) { return __builtin_amdgcn_raw_buffer_load_b128(rs, off, 0, AUX_SC1); };
  auto st16 = [&](v4u v, int off) { __builtin_amdgcn_raw_buffer_store_b128(v, rs, off, 0, AUX_SC1); };
  auto st8 = [&](v2u v, int off) { __builtin_amdgcn_raw_buffer_store_b64(v, rs, off, 0, AUX_SC1); };
  auto st4 = [&](unsigned v, int off) { __builtin_amdgcn_raw_buffer_store_b32(v, rs, off, 0, AUX_SC1); };
  auto dma_ckv = [&](int row0, int nrows) {
    const u16* base = p.ckv + ((size_t)ab * SK + aj * 128) * DC;
    for (int rr = 0; rr < nrows; ++rr) {
      const int row = row0 + rr;
      const int sw = ((row & 3) << 2) | ((row >> 2) & 3);
      const u16* src = base + row * DC + ((l ^ sw) * 8);
      unsigned keep; const unsigned dst = (unsigned)(L_CKV + row * 1024) + (unsigned)(size_t)L;
      asm volatile("s_mov_b32 %0, m0\n\ts_mov_b32 m0, %2\n\ts_nop 0\n\tglobal_load_lds_dwordx4 %1, off nt\n\ts_mov_b32 m0, %0" : "=&s"(keep) : "v"(src), "s"(dst) : "memory");
    }
  };
  const v4u SENT4 = {SENT, SENT, SENT, SENT};
  auto fill_x4 = [&]() {
#pragma unroll
    for (int k = 0; k < 2; ++k) st16(SENT4, X4 + ((ab * NH + 16 * hg + 2 * w + k) * 64 + aj) * 1024 + l * 16);
    if (w == 0 && l < 8) st16(SENT4, X4S + ((ab * 64 + aj) * NH + 16 * hg) * 8 + l * 16);
  };
  auto check_flags = [&](int off, int nper) {
    const unsigned tag = TAG();
    for (unsigned sp = 0; sp < SPIN_MAX; ++sp) {
      asm volatile("" ::: "memory");
      bool ok = true;
      for (int k = 0; k < nper; ++k) ok &= __builtin_amdgcn_raw_buffer_load_b32(rs, off + (k * 64 + l) * 128, 0, AUX_SC1) == tag;
      if (__all(ok)) break;
      __builtin_amdgcn_s_sleep(1);
    }
  };

  auto attention = [&](auto&& hook_post) {
    DRAIN();
    BAR();
    STAMP(6);
    if (w == 7 && l == 0) st4(TAG(), OFF_FLAGS + bid * 128);
    v4u qk[16], pv[16];
    {
      const int key = 16 * w + r16;
      const int sw = ((key & 3) << 2) | ((key >> 2) & 3);
#pragma unroll
      for (int s = 0; s < 16; ++s) qk[s] = *(LAS v4u*)(L + L_CKV + key * 1024 + (((4 * s + g) ^ sw) * 16));
      const int q4 = r16 >> 2, p4 = l & 3;
#pragma unroll
      for (int s = 0; s < 4; ++s)
#pragma unroll
        for (int t = 0; t < 4; ++t) {
          v2u h2[2];
#pragma unroll
          for (int hf = 0; hf < 2; ++hf) {
            const int ky = 32 * s + 16 * hf + 4 * g + q4;
            const int sw2 = ((ky & 3) << 2) | ((ky >> 2) & 3);
            const int ch = 8 * w + 4 * (t >> 1) + p4;
            const s16x4 v = __builtin_amdgcn_ds_read_tr16_b64_v4i16((LAS s16x4*)(L + L_CKV + ky * 1024 + ((ch ^ sw2) * 16) + 8 * (t & 1)));
            h2[hf] = __builtin_bit_cast(v2u, v);
          }
          pv[s * 4 + t] = v4u{h2[0].x, h2[0].y, h2[1].x, h2[1].y};
        }
    }
    PIN();
    {
      v4u a[2];
      const int base = X3 + (ab * NH + 16 * hg) * 1024;
      for (unsigned sp = 0; sp < SPIN_MAX; ++sp) {
        asm volatile("" ::: "memory");
        a[0] = ld16(base + tid * 16); a[1] = ld16(base + 8192 + tid * 16);
        const unsigned m0 = umax4(a[0]), m1 = umax4(a[1]);
        if (__all((m0 > m1 ? m0 : m1) != SENT)) break;
      }
#pragma unroll
      for (int k = 0; k < 2; ++k) { const int u = k * 512 + tid, hd = u >> 6, ch = u & 63; *(LAS v4u*)(L + L_QIMG + hd * 1024 + ((ch ^ hd) * 16)) = a[k]; }
    }
    STAMP(7);
    unsigned lf[4] = {0u, 0u, 0u, 0u};
    if (w == 6) {
#pragma unroll
      for (int k = 0; k < 4; ++k) lf[k] = __builtin_amdgcn_raw_buffer_load_b32(rs, OFF_FLAGS + (k * 64 + l) * 128, 0, AUX_SC1);
    }
    PIN();
    BAR();
    f32x4 sacc = {0.f, 0.f, 0.f, 0.f};
#pragma unroll
    for (int s = 0; s < 16; ++s) {
      const v4u qf = *(LAS v4u*)(L + L_QIMG + r16 * 1024 + (((4 * s + g) ^ r16) * 16));
      sacc = mfma16(qk[s], qf, sacc);
    }
    STAMP(8);
    PIN();
    PIN();
    LAS float* mx = (LAS float*)(L + L_MISC);
    LAS float* ls = mx + 128;
    const float mloc = x16_max(fmaxf(fmaxf(sacc[0], sacc[1]), fmaxf(sacc[2], sacc[3])));
    if (g == 0) mx[w * 16 + r16] = mloc;
    BAR();
    float m = mx[r16];
#pragma unroll
    for (int k = 1; k < 8; ++k) m = fmaxf(m, mx[k * 16 + r16]);
    const float p0 = __builtin_amdgcn_exp2f(sacc[0] - m), p1 = __builtin_amdgcn_exp2f(sacc[1] - m), p2 = __builtin_amdgcn_exp2f(sacc[2] - m), p3 = __builtin_amdgcn_exp2f(sacc[3] - m);
    const unsigned d0 = pk2(p0, p1), d1 = pk2(p2, p3);
    *(LAS v2u*)(L + L_PIMG + (w >> 1) * 1024 + l * 16 + (w & 1) * 8) = v2u{d0, d1};
    const float lsum = x16_sum((bflo(d0) + bfhi(d0)) + (bflo(d1) + bfhi(d1)));
    if (g == 0) ls[w * 16 + r16] = lsum;
    BAR();
    f32x4 o[4];
#pragma unroll
    for (int t = 0; t < 4; ++t) o[t] = f32x4{0.f, 0.f, 0.f, 0.f};
#pragma unroll
    for (int s = 0; s < 4; ++s) {
      const v4u pf = *(LAS v4u*)(L + L_PIMG + s * 1024 + l * 16);
#pragma unroll
      for (int t = 0; t < 4; ++t) o[t] = mfma16(pv[s * 4 + t], pf, o[t]);
    }
    const int hd = 16 * hg + r16;
    const int ob = X4 + (((ab * NH + hd) * 64 + aj) * 512 + 64 * w + 8 * g) * 2;
    st16(v4u{pk2s(o[0][0], o[0][1]), pk2s(o[0][2], o[0][3]), pk2s(o[1][0], o[1][1]), pk2s(o[1][2], o[1][3])}, ob);
    st16(v4u{pk2s(o[2][0], o[2][1]), pk2s(o[2][2], o[2][3]), pk2s(o[3][0], o[3][1]), pk2s(o[3][2], o[3][3])}, ob + 64);
    if (w == 0 && g == 0) {
      float lt = 0.f;
#pragma unroll
      for (int k = 0; k < 8; ++k) lt += ls[k * 16 + r16];
      st8(v2u{__float_as_uint(m), __float_as_uint(lt)}, X4S + ((ab * 64 + aj) * NH + hd) * 8);
    }
    STAMP(9);
    PIN();
    hook_post();
    PIN();
    if (w == 6) {
      const unsigned tag = TAG();
      if (!__all((lf[0] == tag) & (lf[1] == tag) & (lf[2] == tag) & (lf[3] == tag))) check_flags(OFF_FLAGS, 4);
      STAMPW(18, 6);
    }
    BAR();
  };

  if (Ltype) {
    const int hh = idx >> 2, sub = idx & 3;
    if (w == 7) {
      if (l < 32) st16(SENT4, X2 + (hh * 4 + sub) * 512 + l * 16);
      else st16(SENT4, X3 + (((l - 32) >> 4) * NH + hh) * 1024 + sub * 256 + (l & 15) * 16);
      DRAIN();
      if (l == 0) st4(TAG(), OFF_EFLAGS + idx * 128);
    }
    if (idx < 40) {
      const int mg = idx % 10, pp = idx / 10;
      LAS float* red = (LAS float*)(L + L_RED);
      LAS float* st = (LAS float*)(L + L_MISC);
      f32x4 acc[8];
      if (w < 5) {
        v4u wv[8];
        const int col = (r16 < 8) ? (64 * mg + 8 * r16) : (DM + 64 * mg + 8 * (r16 - 8));
        const int k0 = 160 * pp + 32 * w + 8 * g;
#pragma unroll
        for (int j = 0; j < 8; ++j) wv[j] = ldnt(p.wmod + (size_t)(k0 + j) * (6 * DM) + col);
        const v4u cv = *(const GAS v4u*)(p.c + (r16 & 1) * DM + k0);
        PIN();
        const v4u a = silu8(cv);
        v4u f[8]; tr8(wv, f);
#pragma unroll
        for (int i = 0; i < 8; ++i) acc[i] = mfma16(a, f[i], f32x4{0.f, 0.f, 0.f, 0.f});
        if (g == 0) {
#pragma unroll
          for (int b = 0; b < 2; ++b) {
            *(LAS f32x4*)(red + (w * 2 + b) * 128 + 8 * r16) = f32x4{acc[0][b], acc[1][b], acc[2][b], acc[3][b]};
            *(LAS f32x4*)(red + (w * 2 + b) * 128 + 8 * r16 + 4) = f32x4{acc[4][b], acc[5][b], acc[6][b], acc[7][b]};
          }
        }
      } else if (w < 7) {
        const int b = w - 5;
        float v[10]; float s = 0.f;
#pragma unroll
        for (int k = 0; k < 5; ++k) { const unsigned d = *(const GAS unsigned*)(p.x + b * DM + 2 * (l + 64 * k)); v[2 * k] = bflo(d); v[2 * k + 1] = bfhi(d); s += v[2 * k] + v[2 * k + 1]; }
        const float mean = wave_sum(s) * (1.f / DM);
        float q = 0.f;
#pragma unroll
        for (int k = 0; k < 10; ++k) { const float d = v[k] - mean; q += d * d; }
        const float var = wave_sum(q) * (1.f / DM);
        if (l == 0) { st[2 * b] = mean; st[2 * b + 1] = 1.f / sqrtf(var + 1e-5f); }
      }
      unsigned xd = 0;
      if (w == 0) xd = *(const GAS unsigned*)(p.x + (l >> 5) * DM + 64 * mg + 2 * (l & 31));
      BAR();
      if (w == 0) {
        const int b = l >> 5, pr = l & 31;
        float sa0 = 0.f, sa1 = 0.f, ca0 = 0.f, ca1 = 0.f;
#pragma unroll
        for (int ww = 0; ww < 5; ++ww) {
          const f32x2 s = *(LAS f32x2*)(red + (ww * 2 + b) * 128 + 2 * pr), c2 = *(LAS f32x2*)(red + (ww * 2 + b) * 128 + 64 + 2 * pr);
          sa0 += s[0]; sa1 += s[1]; ca0 += c2[0]; ca1 += c2[1];
        }
        const float mean = st[2 * b], rstd = st[2 * b + 1];
        const float xn0 = (bflo(xd) - mean) * rstd, xn1 = (bfhi(xd) - mean) * rstd;
        const float o0 = xn0 * ca0 + sa0 + (pp == 0 ? xn0 : 0.f), o1 = xn1 * ca1 + sa1 + (pp == 0 ? xn1 : 0.f);
        st8(v2u{pk2(o0, o1), TAG()}, OFF_X1 + (((pp * 2 + b) * 320) + 32 * mg + pr) * 8);
        STAMP(1);
      }
    }
    {
      LAS float* red = (LAS float*)(L + L_QIMG);
      v4u wv[8], uk[4];
      if (w < 5) {
        const int k0 = 160 * sub + 32 * w + 8 * g;
#pragma unroll
        for (int j = 0; j < 8; ++j) wv[j] = ldnt(p.wq + (size_t)(k0 + j) * (NH * DH) + hh * DH + 8 * r16);
      }
#pragma unroll
      for (int s = 0; s < 4; ++s) uk[s] = ldnt(p.wuk + ((size_t)(128 * sub + 16 * w + r16) * NH + hh) * DH + 32 * s + 8 * g);
      fill_x4();
      if (w == 5) {
        if (idx < 80) { if (l < 16) st16(SENT4, X6 + (((idx / 10) * 2 + (l >> 3)) * DM + 64 * (idx % 10)) * 2 + (l & 7) * 16); }
        else if (idx < 120) { if (l < 16) st16(SENT4, XM + ((l >> 3) * 2560 + 64 * (idx - 80)) * 2 + (l & 7) * 16); }
      }
      if (CKV_A > 0 && idx >= 40) dma_ckv(16 * w, CKV_A);
      PIN();
      if (w == 6) check_flags(OFF_EFLAGS, 2);
      if (w < 5) {
        const unsigned tag = TAG();
        v4u f[8]; tr8(wv, f);
        const int pp = (r16 & 7) >> 1, b = r16 & 1;
        const int off = OFF_X1 + ((pp * 2 + b) * 320 + (160 * sub + 32 * w + 8 * g) / 2) * 8;
        v4u a;
        for (unsigned sp = 0; sp < SPIN_MAX; ++sp) {
          asm volatile("" ::: "memory");
          const v4u u0 = ld16(off), u1 = ld16(off + 16);
          a = v4u{u0.x, u0.z, u1.x, u1.z};
          if (__all((u0.y == tag) & (u0.w == tag) & (u1.y == tag) & (u1.w == tag))) break;
        }
        STAMP(2);
        f32x4 acc[8];
#pragma unroll
        for (int i = 0; i < 8; ++i) acc[i] = mfma16(a, f[i], f32x4{0.f, 0.f, 0.f, 0.f});
        if (g < 2) {
#pragma unroll
          for (int b2 = 0; b2 < 2; ++b2) {
            LAS float* d = red + ((w * 2 + g) * 2 + b2) * 128 + 8 * r16;
            *(LAS f32x4*)d = f32x4{acc[0][b2] + acc[0][b2 + 2], acc[1][b2] + acc[1][b2 + 2], acc[2][b2] + acc[2][b2 + 2], acc[3][b2] + acc[3][b2 + 2]};
            *(LAS f32x4*)(d + 4) = f32x4{acc[4][b2] + acc[4][b2 + 2], acc[5][b2] + acc[5][b2 + 2], acc[6][b2] + acc[6][b2 + 2], acc[7][b2] + acc[7][b2 + 2]};
          }
        }
      }
      BAR();
      if (w < 2) {
        const int b = w;
        float s0 = 0.f, s1 = 0.f;
#pragma unroll
        for (int k = 0; k < 10; ++k) { const f32x2 v = *(LAS f32x2*)(red + (k * 2 + b) * 128 + 2 * l); s0 += v[0]; s1 += v[1]; }
        st4(pk2s(s0, s1), X2 + ((hh * 4 + sub) * 2 + b) * 256 + l * 4);
        STAMP(3);
      }
      if (CKV_B > 0) { if (CKV_A > 0 && idx < 40) dma_ckv(16 * w, CKV_A + CKV_B); else dma_ckv(16 * w + CKV_A, CKV_B); }
      {
        LAS u16* qa = (LAS u16*)(L + L_RED);
        const int kq = (r16 & 7) >> 1, b = r16 & 1;
        v4u a[4];
        for (unsigned sp = 0; sp < SPIN_MAX; ++sp) {
          asm volatile("" ::: "memory");
          unsigned m = 0;
#pragma unroll
          for (int s = 0; s < 4; ++s) { a[s] = ld16(X2 + ((hh * 4 + kq) * 2 + b) * 256 + (32 * s + 8 * g) * 2); const unsigned t = umax4(a[s]); m = m > t ? m : t; }
          if (__all(m != SENT)) break;
        }
        STAMP(4);
        f32x4 acc = {0.f, 0.f, 0.f, 0.f};
#pragma unroll
        for (int s = 0; s < 4; ++s) acc = mfma16(a[s], uk[s], acc);
        const float sc = 0.08838834764831845f * 1.4426950408889634f;
        const float tot = rs16(acc[0] + acc[2], acc[1] + acc[3]) * sc;
        if (g < 2) qa[g * 128 + 16 * w + r16] = (u16)(pk2(tot, 0.f) & 0xffffu);
        BAR();
        if (w == 0 && l < 32) {
          v4u v = *(LAS v4u*)(L + L_RED + l * 16);
          v.x = v.x < 0xFFFFFFFEu ? v.x : 0xFFFFFFFEu; v.y = v.y < 0xFFFFFFFEu ? v.y : 0xFFFFFFFEu; v.z = v.z < 0xFFFFFFFEu ? v.z : 0xFFFFFFFEu; v.w = v.w < 0xFFFFFFFEu ? v.w : 0xFFFFFFFEu;
          st16(v, X3 + ((l >> 4) * NH + hh) * 1024 + sub * 256 + (l & 15) * 16);
          STAMP(5);
        }
      }
    }
    dma_ckv(16 * w + CKV_A + CKV_B, 16 - CKV_A - CKV_B);
    v4u wpre[3][8];
    attention([&]() {
      if (w < 4) {
        const int kk = (l >> 3) & 1, ch = l & 7;
        if (idx < 80) {
          const int pp = idx / 10, n = idx % 10;
#pragma unroll
          for (int k = 0; k < 2; ++k)
#pragma unroll
            for (int j = 0; j < 8; ++j) wpre[k][j] = ldnt(p.wo + (size_t)(512 * pp + 64 * (w + 4 * k) + 32 * kk + 8 * g + j) * DM + 64 * n + 8 * ch);
        } else if (idx < 120) {
          const int tb = idx - 80;
#pragma unroll
          for (int k = 0; k < 2; ++k)
#pragma unroll
            for (int j = 0; j < 8; ++j) wpre[k][j] = ldnt(p.wmod + (size_t)(64 * (w + 4 * k) + 32 * kk + 8 * g + j) * (6 * DM) + 2 * DM + 64 * tb + 8 * ch);
          if (w < 2) {
#pragma unroll
            for (int j = 0; j < 8; ++j) wpre[2][j] = ldnt(p.wmod + (size_t)(64 * (w + 8) + 32 * kk + 8 * g + j) * (6 * DM) + 2 * DM + 64 * tb + 8 * ch);
          }
        }
      }
    });
    LAS float* red = (LAS float*)(L + L_RED);
    if (idx < 80) {
      const int pp = idx / 10, n = idx % 10;
      if (w < 4) {
        v4u f0[8], f1[8]; tr8(wpre[0], f0); tr8(wpre[1], f1);
        const int rho = r16 & 7, cq = rho >> 1, b = rho & 1, kk2 = r16 >> 3;
        v4u a[2];
        for (unsigned sp = 0; sp < SPIN_MAX; ++sp) {
          asm volatile("" ::: "memory");
#pragma unroll
          for (int k = 0; k < 2; ++k) { const int bk = w + 4 * k, h = 4 * pp + (bk >> 1), d0 = 64 * (bk & 1) + 32 * kk2 + 8 * g; a[k] = ld16(X5 + ((h * 4 + cq) * 2 + b) * 256 + d0 * 2); }
          const unsigned m0 = umax4(a[0]), m1 = umax4(a[1]);
          if (__all((m0 > m1 ? m0 : m1) != SENT)) break;
        }
        STAMP(12);
        float s[8];
#pragma unroll
        for (int i = 0; i < 8; ++i) {
          f32x4 d = mfma16(a[0], f0[i], f32x4{0.f, 0.f, 0.f, 0.f});
          d = mfma16(a[1], f1[i], d);
          s[i] = rs16(d[0] + d[2], d[1] + d[3]);
        }
        if ((g >> 1) == (r16 >> 3)) {
          LAS float* d = red + ((w * 2 + (g >> 1)) * 2 + (g & 1)) * 64 + 8 * (r16 & 7);
          *(LAS f32x4*)d = f32x4{s[0], s[1], s[2], s[3]}; *(LAS f32x4*)(d + 4) = f32x4{s[4], s[5], s[6], s[7]};
        }
      }
      BAR();
      if (w == 0) {
        const int b = l >> 5, pr = l & 31;
        float s0 = 0.f, s1 = 0.f;
#pragma unroll
        for (int k = 0; k < 8; ++k) { const f32x2 v = *(LAS f32x2*)(red + (k * 2 + b) * 64 + 2 * pr); s0 += v[0]; s1 += v[1]; }
        st4(pk2s(s0, s1), X6 + ((pp * 2 + b) * DM + 64 * n + 2 * pr) * 2);
        STAMP(13);
      }
    } else if (idx < 120) {
      const int tb = idx - 80;
      if (w < 4) {
        f32x4 acc[8];
#pragma unroll
        for (int i = 0; i < 8; ++i) acc[i] = f32x4{0.f, 0.f, 0.f, 0.f};
#pragma unroll
        for (int k = 0; k < 3; ++k) {
          if (k < 2 || w < 2) {
            const v4u cv = *(const GAS v4u*)(p.c + (r16 & 1) * DM + 64 * (w + 4 * k) + 32 * (r16 >> 3) + 8 * g);
            const v4u a = silu8(cv);
            v4u f[8];
            tr8(wpre[k], f);
#pragma unroll
            for (int i = 0; i < 8; ++i) acc[i] = mfma16(a, f[i], acc[i]);
          }
        }
        if ((g == 0 && r16 < 8) || (g == 2 && r16 >= 8)) {
#pragma unroll
          for (int b2 = 0; b2 < 2; ++b2) {
            LAS float* d = red + ((w * 2 + (g >> 1)) * 2 + b2) * 64 + 8 * (r16 & 7);
            *(LAS f32x4*)d = f32x4{acc[0][b2], acc[1][b2], acc[2][b2], acc[3][b2]}; *(LAS f32x4*)(d + 4) = f32x4{acc[4][b2], acc[5][b2], acc[6][b2], acc[7][b2]};
          }
        }
      }
      BAR();
      if (w == 0) {
        const int b = l >> 5, pr = l & 31;
        float s0 = 0.f, s1 = 0.f;
#pragma unroll
        for (int k = 0; k < 8; ++k) { const f32x2 v = *(LAS f32x2*)(red + (k * 2 + b) * 64 + 2 * pr); s0 += v[0]; s1 += v[1]; }
        st4(pk2s(s0, s1), XM + (b * 2560 + 64 * tb + 2 * pr) * 2);
        STAMP(19);
      }
    }
  } else {
    fill_x4();
    if (w == 1 && l < 32) st16(SENT4, X5 + idx * 512 + l * 16);
    if (idx < 54 && w >= 2 && w < 5) { const int u = (w - 2) * 64 + l; if (u < 160) st16(SENT4, X7 + idx * 2560 + u * 16); }
    dma_ckv(16 * w, 16);
    if (w == 6) check_flags(OFF_EFLAGS, 2);
    v4u wpre[1][8];
    attention([&]() {
      if (w < 4) {
        const int h5 = idx >> 2, cq = idx & 3;
#pragma unroll
        for (int j = 0; j < 8; ++j) wpre[0][j] = ldnt(p.wuv + ((size_t)(128 * cq + 32 * w + 8 * g + j) * NH + h5) * DH + 8 * r16);
      }
    });
    const int h5 = idx >> 2, cq = idx & 3;
    LAS float* st5 = (LAS float*)(L + L_MISC + 1024);
    {
      const int b = w >> 2;
      v4u a[4]; v2u sv[4];
      for (unsigned sp = 0; sp < SPIN_MAX; ++sp) {
        asm volatile("" ::: "memory");
        unsigned m = 0;
#pragma unroll
        for (int k = 0; k < 4; ++k) {
          const int j = 16 * (w & 3) + 4 * k + g;
          a[k] = ld16(X4 + (((b * NH + h5) * 64 + j) * 512 + 128 * cq + 8 * r16) * 2);
          sv[k] = __builtin_amdgcn_raw_buffer_load_b64(rs, X4S + ((b * 64 + j) * NH + h5) * 8, 0, AUX_SC1);
        }
#pragma unroll
        for (int k = 0; k < 4; ++k) { unsigned t = umax4(a[k]); t = t > sv[k].x ? t : sv[k].x; t = t > sv[k].y ? t : sv[k].y; m = m > t ? m : t; }
        if (__all(m != SENT)) break;
      }
      STAMP(10);
      float mw = fmaxf(fmaxf(__uint_as_float(sv[0].x), __uint_as_float(sv[1].x)), fmaxf(__uint_as_float(sv[2].x), __uint_as_float(sv[3].x)));
      mw = x16_max(mw);
      float acc[8] = {0.f, 0.f, 0.f, 0.f, 0.f, 0.f, 0.f, 0.f}; float lw = 0.f;
#pragma unroll
      for (int k = 0; k < 4; ++k) {
        const unsigned wb = pk2(__builtin_amdgcn_exp2f(__uint_as_float(sv[k].x) - mw), 0.f) & 0xffffu;
        lw += bflo(wb) * __uint_as_float(sv[k].y);
        const unsigned wlo = wb, whi = wb << 16;
#pragma unroll
        for (int d = 0; d < 4; ++d) { acc[2 * d] = dot2(a[k][d], wlo, acc[2 * d]); acc[2 * d + 1] = dot2(a[k][d], whi, acc[2 * d + 1]); }
      }
      lw = x16_sum(lw);
      float t4[4];
#pragma unroll
      for (int e = 0; e < 4; ++e) t4[e] = rs32(acc[e], acc[e + 4]);
      const float u0 = rs16(t4[0], t4[2]), u1 = rs16(t4[1], t4[3]);
      const int cofs = 8 * r16 + 4 * (l >> 5) + 2 * ((l >> 4) & 1);
      const int chn = cofs >> 3;
      *(LAS unsigned*)(L + L_RED + w * 256 + ((chn ^ w) * 16) + (cofs & 7) * 2) = pk2(u0, u1);
      if (l == 0) { st5[2 * w] = mw; st5[2 * w + 1] = lw; }
    }
    BAR();
    LAS float* red = (LAS float*)(L + L_QIMG);
    if (w < 4) {
      v4u f[8]; tr8(wpre[0], f);
      const int row = r16 & 7;
      const v4u a = *(LAS v4u*)(L + L_RED + row * 256 + (((4 * w + g) ^ row) * 16));
      float fs[4];
      {
        const int b = g & 1;
        float mm[4], ll[4];
#pragma unroll
        for (int k = 0; k < 4; ++k) { mm[k] = st5[2 * (4 * b + k)]; ll[k] = st5[2 * (4 * b + k) + 1]; }
        const float mb = fmaxf(fmaxf(mm[0], mm[1]), fmaxf(mm[2], mm[3]));
        float Lb = 0.f;
#pragma unroll
        for (int k = 0; k < 4; ++k) { fs[k] = __builtin_amdgcn_exp2f(mm[k] - mb); Lb += fs[k] * ll[k]; }
        const float inv = 1.f / Lb;
#pragma unroll
        for (int k = 0; k < 4; ++k) fs[k] *= inv;
      }
      float v[8];
#pragma unroll
      for (int i = 0; i < 8; ++i) { const f32x4 d = mfma16(a, f[i], f32x4{0.f, 0.f, 0.f, 0.f}); v[i] = (d[0] * fs[0] + d[1] * fs[1]) + (d[2] * fs[2] + d[3] * fs[3]); }
      if (g < 2) {
        LAS float* d = red + (w * 2 + g) * 128 + 8 * r16;
        *(LAS f32x4*)d = f32x4{v[0], v[1], v[2], v[3]}; *(LAS f32x4*)(d + 4) = f32x4{v[4], v[5], v[6], v[7]};
      }
    }
    BAR();
    if (w < 2) {
      const int b = w;
      float s0 = 0.f, s1 = 0.f;
#pragma unroll
      for (int k = 0; k < 4; ++k) { const f32x2 v = *(LAS f32x2*)(red + (k * 2 + b) * 128 + 2 * l); s0 += v[0]; s1 += v[1]; }
      st4(pk2s(s0, s1), X5 + ((h5 * 4 + cq) * 2 + b) * 256 + l * 4);
      STAMP(11);
    }
    if (idx < 54) {
      const int kk = (l >> 3) & 1, ch = l & 7;
      v4u wa[3][8], wdn[8];
      if (w < 4) {
        const u16* wsrc = (ch < 4 ? p.wg : p.wu) + 32 * idx + 8 * (ch & 3);
#pragma unroll
        for (int k = 0; k < 2; ++k)
#pragma unroll
          for (int j = 0; j < 8; ++j) wa[k][j] = ldnt(wsrc + (size_t)(64 * (w + 4 * k) + 32 * kk + 8 * g + j) * DF);
        if (w < 2) {
#pragma unroll
          for (int j = 0; j < 8; ++j) wa[2][j] = ldnt(wsrc + (size_t)(64 * (w + 8) + 32 * kk + 8 * g + j) * DF);
        }
      }
      if (w < 5) {
#pragma unroll
        for (int j = 0; j < 8; ++j) wdn[j] = ldnt(p.wd + (size_t)(32 * idx + 8 * g + j) * DM + 128 * w + 8 * r16);
      }
      PIN();
      LAS float* stt = (LAS float*)(L + L_MISC);
      const int b = w / 3, cc = 32 * (w % 3) + (l & 31), half = l >> 5;
      const bool act_lane = (w < 6) && (cc < 80);
      const int m0 = 8 * cc + 4 * half;
      float x1[4] = {0.f, 0.f, 0.f, 0.f}, cb4[4], sb4[4];
      if (w < 6) {
        const int ccc = cc < 80 ? cc : 79;
        const int mo = 8 * ccc + 4 * half;
        v2u gav, sbv, cbv;
        for (unsigned sp = 0; sp < SPIN_MAX; ++sp) {
          asm volatile("" ::: "memory");
          gav = __builtin_amdgcn_raw_buffer_load_b64(rs, XM + (b * 2560 + mo) * 2, 0, AUX_SC1);
          sbv = __builtin_amdgcn_raw_buffer_load_b64(rs, XM + (b * 2560 + 640 + mo) * 2, 0, AUX_SC1);
          cbv = __builtin_amdgcn_raw_buffer_load_b64(rs, XM + (b * 2560 + 1280 + mo) * 2, 0, AUX_SC1);
          if (__all((gav.x != SENT) & (gav.y != SENT) & (sbv.x != SENT) & (sbv.y != SENT) & (cbv.x != SENT) & (cbv.y != SENT))) break;
        }
        const v2u xv = *(const GAS v2u*)(p.x + b * DM + mo);
        v4u a[4];
        for (unsigned sp = 0; sp < SPIN_MAX; ++sp) {
          asm volatile("" ::: "memory");
          unsigned m = 0;
#pragma unroll
          for (int k = 0; k < 4; ++k) { a[k] = ld16(X6 + (((4 * half + k) * 2 + b) * DM + 8 * ccc) * 2); const unsigned t = umax4(a[k]); m = m > t ? m : t; }
          if (__all(m != SENT)) break;
        }
        STAMP(14);
        float acc[8] = {0.f, 0.f, 0.f, 0.f, 0.f, 0.f, 0.f, 0.f};
#pragma unroll
        for (int k = 0; k < 4; ++k)
#pragma unroll
          for (int d = 0; d < 4; ++d) { acc[2 * d] += bflo(a[k][d]); acc[2 * d + 1] += bfhi(a[k][d]); }
        float s4[4];
#pragma unroll
        for (int e = 0; e < 4; ++e) s4[e] = rs32(acc[e], acc[e + 4]);
        x1[0] = bflo(xv.x) + bflo(gav.x) * s4[0]; x1[1] = bfhi(xv.x) + bfhi(gav.x) * s4[1];
        x1[2] = bflo(xv.y) + bflo(gav.y) * s4[2]; x1[3] = bfhi(xv.y) + bfhi(gav.y) * s4[3];
        cb4[0] = bflo(cbv.x); cb4[1] = bfhi(cbv.x); cb4[2] = bflo(cbv.y); cb4[3] = bfhi(cbv.y);
        sb4[0] = bflo(sbv.x); sb4[1] = bfhi(sbv.x); sb4[2] = bflo(sbv.y); sb4[3] = bfhi(sbv.y);
        float s1 = act_lane ? (x1[0] + x1[1]) + (x1[2] + x1[3]) : 0.f;
        float s2 = act_lane ? (x1[0] * x1[0] + x1[1] * x1[1]) + (x1[2] * x1[2] + x1[3] * x1[3]) : 0.f;
        s1 = wave_sum(s1); s2 = wave_sum(s2);
        if (l == 0) { stt[2 * w] = s1; stt[2 * w + 1] = s2; }
      }
      BAR();
      LAS u16* h2 = (LAS u16*)(L + L_RED);
      if (act_lane) {
        const float s1 = stt[6 * b] + stt[6 * b + 2] + stt[6 * b + 4], s2 = stt[6 * b + 1] + stt[6 * b + 3] + stt[6 * b + 5];
        const float mean = s1 * (1.f / DM);
        const float var = fmaxf(s2 * (1.f / DM) - mean * mean, 0.f);
        const float rstd = 1.f / sqrtf(var + 1e-5f);
        float hv[4];
#pragma unroll
        for (int e = 0; e < 4; ++e) hv[e] = (x1[e] - mean) * rstd * (1.f + cb4[e]) + sb4[e];
        *(LAS v2u*)(h2 + b * DM + m0) = v2u{pk2(hv[0], hv[1]), pk2(hv[2], hv[3])};
      }
      BAR();
      LAS float* red2 = (LAS float*)(L + L_QIMG);
      if (w < 4) {
        f32x4 acc[8];
#pragma unroll
        for (int i = 0; i < 8; ++i) acc[i] = f32x4{0.f, 0.f, 0.f, 0.f};
#pragma unroll
        for (int k = 0; k < 3; ++k) {
          if (k < 2 || w < 2) {
            v4u f[8]; tr8(wa[k], f);
            const v4u a = *(LAS v4u*)(h2 + (r16 & 1) * DM + 64 * (w + 4 * k) + 32 * (r16 >> 3) + 8 * g);
#pragma unroll
            for (int i = 0; i < 8; ++i) acc[i] = mfma16(a, f[i], acc[i]);
          }
        }
        if ((g == 0 && r16 < 8) || (g == 2 && r16 >= 8)) {
#pragma unroll
          for (int b2 = 0; b2 < 2; ++b2) {
            LAS float* d = red2 + ((w * 2 + (g >> 1)) * 2 + b2) * 64 + 8 * (r16 & 7);
            *(LAS f32x4*)d = f32x4{acc[0][b2], acc[1][b2], acc[2][b2], acc[3][b2]}; *(LAS f32x4*)(d + 4) = f32x4{acc[4][b2], acc[5][b2], acc[6][b2], acc[7][b2]};
          }
        }
      }
      BAR();
      LAS u16* actl = (LAS u16*)(L + L_MISC + 512);
      if (w == 0) {
        const int b2 = l >> 5, f = l & 31;
        float gs = 0.f, us = 0.f;
#pragma unroll
        for (int k = 0; k < 8; ++k) { gs += red2[(k * 2 + b2) * 64 + f]; us += red2[(k * 2 + b2) * 64 + 32 + f]; }
        actl[b2 * 32 + f] = (u16)(pk2(silu(gs) * us, 0.f) & 0xffffu);
      }
      BAR();
      if (w < 5) {
        v4u f[8]; tr8(wdn, f);
        const v4u a = *(LAS v4u*)(actl + (r16 & 1) * 32 + 8 * g);
        f32x4 d[8];
#pragma unroll
        for (int i = 0; i < 8; ++i) d[i] = mfma16(a, f[i], f32x4{0.f, 0.f, 0.f, 0.f});
        if (g == 0) {
#pragma unroll
          for (int b2 = 0; b2 < 2; ++b2)
            st16(v4u{pk2s(d[0][b2], d[1][b2]), pk2s(d[2][b2], d[3][b2]), pk2s(d[4][b2], d[5][b2]), pk2s(d[6][b2], d[7][b2])}, X7 + ((idx * 2 + b2) * DM + 128 * w + 8 * r16) * 2);
        }
        STAMP(15);
      }
    } else if (idx >= 64 && idx < 84) {
      const int n = idx - 64;
      LAS float* red = (LAS float*)(L + L_RED);
      float base0 = 0.f, base1 = 0.f, gb0 = 0.f, gb1 = 0.f;
      if (w == 7 && l < 32) {
        const int b = l >> 4, pr = l & 15, m = 32 * n + 2 * pr;
        unsigned ga = 0, gb = 0, ap[8];
        for (unsigned sp = 0; sp < SPIN_MAX; ++sp) {
          asm volatile("" ::: "memory");
          ga = __builtin_amdgcn_raw_buffer_load_b32(rs, XM + (b * 2560 + m) * 2, 0, AUX_SC1);
          gb = __builtin_amdgcn_raw_buffer_load_b32(rs, XM + (b * 2560 + 1920 + m) * 2, 0, AUX_SC1);
          bool ok = (ga != SENT) & (gb != SENT);
#pragma unroll
          for (int k = 0; k < 8; ++k) { ap[k] = __builtin_amdgcn_raw_buffer_load_b32(rs, X6 + ((k * 2 + b) * DM + m) * 2, 0, AUX_SC1); ok &= ap[k] != SENT; }
          if (__all(ok)) break;
        }
        const unsigned xd = *(const GAS unsigned*)(p.x + b * DM + m);
        float s0 = 0.f, s1 = 0.f;
#pragma unroll
        for (int k = 0; k < 8; ++k) { s0 += bflo(ap[k]); s1 += bfhi(ap[k]); }
        base0 = bflo(xd) + bflo(ga) * s0; base1 = bfhi(xd) + bfhi(ga) * s1; gb0 = bflo(gb); gb1 = bfhi(gb);
      }
      if (w < 7) {
        const int task = 8 * w + (l >> 3), b = (l >> 2) & 1, ch = l & 3;
        const bool valid = task < 54;
        v4u a = {0u, 0u, 0u, 0u};
        for (unsigned sp = 0; sp < SPIN_MAX; ++sp) {
          asm volatile("" ::: "memory");
          a = ld16(X7 + (((valid ? task : 0) * 2 + b) * DM + 32 * n + 8 * ch) * 2);
          if (__all(umax4(a) != SENT)) break;
        }
        STAMP(16);
        float v[8];
#pragma unroll
        for (int d = 0; d < 4; ++d) { v[2 * d] = valid ? bflo(a[d]) : 0.f; v[2 * d + 1] = valid ? bfhi(a[d]) : 0.f; }
        float t4[4];
#pragma unroll
        for (int e = 0; e < 4; ++e) t4[e] = rs32(v[e], v[e + 4]);
        float t2[2];
        t2[0] = rs16(t4[0], t4[2]); t2[1] = rs16(t4[1], t4[3]);
        t2[0] += dppf<0x128>(t2[0]); t2[1] += dppf<0x128>(t2[1]);
        if (((l >> 3) & 1) == 0) {
          const int e = 4 * (l >> 5) + 2 * ((l >> 4) & 1);
          *(LAS f32x2*)(red + w * 64 + b * 32 + 8 * ch + e) = f32x2{t2[0], t2[1]};
        }
      }
      BAR();
      if (w == 7 && l < 32) {
        const int b = l >> 4, pr = l & 15;
        float s0 = 0.f, s1 = 0.f;
#pragma unroll
        for (int k = 0; k < 7; ++k) { const f32x2 v = *(LAS f32x2*)(red + k * 64 + b * 32 + 2 * pr); s0 += v[0]; s1 += v[1]; }
        *(GAS unsigned*)(p.out + b * DM + 32 * n + 2 * pr) = pk2(base0 + gb0 * s0, base1 + gb1 * s1);
        if (n == 0 && l == 0) st4(TAG(), OFF_EPOCH);
        STAMPW(17, 7);
      }
    }
  }
}

extern "C" void kernel_launch(void* const* d_in, const int* in_sizes, int n_in, void* d_out, int out_size, void* d_ws, size_t ws_size, hipStream_t stream) {
  Params p;
  p.x = (const u16*)d_in[0]; p.ckv = (const u16*)d_in[1]; p.wq = (const u16*)d_in[2]; p.wuk = (const u16*)d_in[3]; p.wuv = (const u16*)d_in[4]; p.wo = (const u16*)d_in[5];
  p.wg = (const u16*)d_in[7]; p.wu = (const u16*)d_in[8]; p.wd = (const u16*)d_in[9]; p.c = (const u16*)d_in[10]; p.wmod = (const u16*)d_in[11];
  p.out = (u16*)d_out; p.ws = (unsigned char*)d_ws;
  mega<<<256, 512, 0, stream>>>(p);
}
```

```cpp
#include <hip/hip_runtime.h>
#include <stdint.h>
#define CODE_PF 7

#define GAS __attribute__((address_space(1)))
#define LAS __attribute__((address_space(3)))
typedef unsigned v4u __attribute__((ext_vector_type(4)));
typedef unsigned v2u __attribute__((ext_vector_type(2)));
typedef float f32x4 __attribute__((ext_vector_type(4)));
typedef float f32x2 __attribute__((ext_vector_type(2)));
typedef __bf16 bf16x8 __attribute__((ext_vector_type(8)));
typedef __bf16 bf16x2 __attribute__((ext_vector_type(2)));
typedef short s16x4 __attribute__((ext_vector_type(4)));
typedef unsigned short u16;
typedef unsigned long long u64;

constexpr int NB = 2, NH = 32, SK = 8192, DH = 128, DM = 640, DC = 512, DF = 1728;
constexpr int OFF_EPOCH = 0;
constexpr int OFF_FLAGS = 4096;
constexpr int OFF_EFLAGS = OFF_FLAGS + 256 * 128;
constexpr int OFF_X1 = OFF_EFLAGS + 128 * 128;
constexpr int OFF_R = 81920;
constexpr int X2 = OFF_R;
constexpr int X3 = X2 + 65536;
constexpr int X4 = X3 + 65536;
constexpr int X4S = X4 + 4194304;
constexpr int X5 = X4S + 32768;
constexpr int X6 = X5 + 65536;
constexpr int X7 = X6 + 20480;
constexpr int XM = X7 + 138240;
constexpr int R_END = XM + 10240;
static_assert(OFF_X1 + 20480 <= OFF_R, "map");
constexpr int WS_USED = R_END;
#define CKV_A 0
#define CKV_B 6
constexpr unsigned SENT = 0xFFFFFFFFu;
constexpr int AUX_SC1 = 16;
constexpr unsigned SPIN_MAX = 1u << 18;
constexpr int L_CKV = 0;
constexpr int L_QIMG = 131072;
constexpr int L_PIMG = L_QIMG + 16384;
constexpr int L_RED = L_PIMG + 4096;
constexpr int L_MISC = L_RED + 8192;
constexpr int LDS_BYTES = L_MISC + 2048;
static_assert(LDS_BYTES <= 163840, "LDS");

#define TS(k) do { } while (0)

struct Params {
  const u16 *x, *ckv, *wq, *wuk, *wuv, *wo, *wg, *wu, *wd, *c, *wmod;
  u16* out;
  unsigned char* ws;
};

#define BAR() do { asm volatile("s_waitcnt lgkmcnt(0)" ::: "memory"); __builtin_amdgcn_s_barrier(); asm volatile("" ::: "memory"); } while (0)
#define DRAIN() asm volatile("s_waitcnt vmcnt(0)" ::: "memory")
#define PIN() __builtin_amdgcn_sched_barrier(0)
#define KEEP(x) asm volatile("" : "+v"(x))
#define KEEP4(a) do { KEEP(a[0]); KEEP(a[1]); KEEP(a[2]); KEEP(a[3]); } while (0)
#define KEEP8(a) do { KEEP(a[0]); KEEP(a[1]); KEEP(a[2]); KEEP(a[3]); KEEP(a[4]); KEEP(a[5]); KEEP(a[6]); KEEP(a[7]); } while (0)

__device__ __forceinline__ float bflo(unsigned d) { return __uint_as_float(d << 16); }
__device__ __forceinline__ float bfhi(unsigned d) { return __uint_as_float(d & 0xffff0000u); }
__device__ __forceinline__ unsigned pk2(float lo, float hi) { f32x2 f = {lo, hi}; return __builtin_bit_cast(unsigned, __builtin_convertvector(f, bf16x2)); }
__device__ __forceinline__ f32x4 mfma16(v4u a, v4u b, f32x4 c) { return __builtin_amdgcn_mfma_f32_16x16x32_bf16(__builtin_bit_cast(bf16x8, a), __builtin_bit_cast(bf16x8, b), c, 0, 0, 0); }
__device__ __forceinline__ float dot2(unsigned a, unsigned b, float c) { return __builtin_amdgcn_fdot2_f32_bf16(__builtin_bit_cast(bf16x2, a), __builtin_bit_cast(bf16x2, b), c, false); }
__device__ __forceinline__ v4u ldnt(const u16* p) { return __builtin_nontemporal_load((const GAS v4u*)p); }
__device__ __forceinline__ unsigned umax3(unsigned a, unsigned b, unsigned c) { unsigned t = a > b ? a : b; return t > c ? t : c; }
__device__ __forceinline__ unsigned umax4(v4u v) { return umax3(umax3(v.x, v.y, v.z), v.w, 0u); }
__device__ __forceinline__ unsigned umax4a(v4u v, unsigned m) { return umax3(umax3(v.x, v.y, v.z), v.w, m); }
__device__ __forceinline__ float x16_max(float x) {
  auto s = __builtin_amdgcn_permlane16_swap(__float_as_uint(x), __float_as_uint(x), false, false);
  x = fmaxf(__uint_as_float(s[0]), __uint_as_float(s[1]));
  auto t = __builtin_amdgcn_permlane32_swap(__float_as_uint(x), __float_as_uint(x), false, false);
  return fmaxf(__uint_as_float(t[0]), __uint_as_float(t[1]));
}
__device__ __forceinline__ float x16_sum(float x) {
  auto s = __builtin_amdgcn_permlane16_swap(__float_as_uint(x), __float_as_uint(x), false, false);
  x = __uint_as_float(s[0]) + __uint_as_float(s[1]);
  auto t = __builtin_amdgcn_permlane32_swap(__float_as_uint(x), __float_as_uint(x), false, false);
  return __uint_as_float(t[0]) + __uint_as_float(t[1]);
}
__device__ __forceinline__ float rs32(float a, float b) { auto t = __builtin_amdgcn_permlane32_swap(__float_as_uint(a), __float_as_uint(b), false, false); return __uint_as_float(t[0]) + __uint_as_float(t[1]); }
__device__ __forceinline__ float rs16(float a, float b) { auto t = __builtin_amdgcn_permlane16_swap(__float_as_uint(a), __float_as_uint(b), false, false); return __uint_as_float(t[0]) + __uint_as_float(t[1]); }
template <int CTRL> __device__ __forceinline__ float dppf(float x) { return __builtin_bit_cast(float, __builtin_amdgcn_mov_dpp(__builtin_bit_cast(int, x), CTRL, 0xf, 0xf, true)); }
__device__ __forceinline__ float row16_sum(float x) { x += dppf<0xB1>(x); x += dppf<0x4E>(x); x += dppf<0x141>(x); x += dppf<0x128>(x); return x; }
__device__ __forceinline__ float wave_sum(float x) { return x16_sum(row16_sum(x)); }
__device__ __forceinline__ float silu(float v) { return v * __builtin_amdgcn_rcpf(1.f + __builtin_amdgcn_exp2f(v * -1.4426950408889634f)); }

__device__ __forceinline__ void tr8(const v4u (&w)[8], v4u (&f)[8]) {
#pragma unroll
  for (int d = 0; d < 4; ++d) {
#pragma unroll
    for (int i2 = 0; i2 < 4; ++i2) {
      f[2 * i2][d] = __builtin_amdgcn_perm(w[2 * d + 1][i2], w[2 * d][i2], 0x05040100u);
      f[2 * i2 + 1][d] = __builtin_amdgcn_perm(w[2 * d + 1][i2], w[2 * d][i2], 0x07060302u);
    }
  }
}
__device__ __forceinline__ v4u silu8(v4u c) {
  v4u r;
#pragma unroll
  for (int d = 0; d < 4; ++d) r[d] = pk2(silu(bflo(c[d])), silu(bfhi(c[d])));
  return r;
}
__device__ __forceinline__ float sum5(float a, float b, float c, float d, float e) { return ((a + b) + (c + d)) + e; }

__global__ void __launch_bounds__(512, 2) mega(Params p) {
  __shared__ __attribute__((aligned(16))) unsigned char smem[LDS_BYTES];
  LAS unsigned char* L = (LAS unsigned char*)smem;
  const int tid = threadIdx.x, l = tid & 63, w = __builtin_amdgcn_readfirstlane(tid >> 6);
  const int bid = blockIdx.x;
  const int g = l >> 4, r16 = l & 15;
  TS(0);
  if (w == CODE_PF) {
    u64 pc; asm volatile("s_getpc_b64 %0" : "=s"(pc));
    const GAS unsigned char* cp = (const GAS unsigned char*)((pc & ~127ull) + (u64)l * 128);
    const unsigned dst = (unsigned)(L_MISC + 1792) + (unsigned)(size_t)L;
#pragma unroll
    for (int k = 0; k < 4; ++k) {
      const GAS unsigned char* q = cp + (k < 3 || l < 44 ? k * 8192 : 0);
      unsigned keep;
      asm volatile("s_mov_b32 %0, m0\n\ts_mov_b32 m0, %2\n\ts_nop 0\n\tglobal_load_lds_dword %1, off\n\ts_mov_b32 m0, %0" : "=&s"(keep) : "v"(q), "s"(dst) : "memory");
    }
  }
  const __amdgpu_buffer_rsrc_t rs = __builtin_amdgcn_make_buffer_rsrc((void*)p.ws, 0, WS_USED, 0x00020000);
  const bool Ltype = bid >= 128;
  const int idx = bid & 127;
  const int ab = idx >> 6, aj = idx & 63, hg = bid >> 7;
  const unsigned epoch_raw = __builtin_amdgcn_raw_buffer_load_b32(rs, OFF_EPOCH, 0, AUX_SC1);
  auto TAG = [&]() { unsigned t = epoch_raw; asm volatile("" : "+v"(t)); return t + 1u; };

  auto ld16 = [&](int off) { return __builtin_amdgcn_raw_buffer_load_b128(rs, off, 0, AUX_SC1); };
  auto ld8 = [&](int off) { return __builtin_amdgcn_raw_buffer_load_b64(rs, off, 0, AUX_SC1); };
  auto ld4 = [&](int off) { return __builtin_amdgcn_raw_buffer_load_b32(rs, off, 0, AUX_SC1); };
  auto st16 = [&](v4u v, int off) { __builtin_amdgcn_raw_buffer_store_b128(v, rs, off, 0, AUX_SC1); };
  auto st8 = [&](v2u v, int off) { __builtin_amdgcn_raw_buffer_store_b64(v, rs, off, 0, AUX_SC1); };
  auto st4 = [&](unsigned v, int off) { __builtin_amdgcn_raw_buffer_store_b32(v, rs, off, 0, AUX_SC1); };
  auto dma_ckv = [&](int row0, int nrows) {
    const u16* base = p.ckv + ((size_t)ab * SK + aj * 128) * DC;
    for (int rr = 0; rr < nrows; ++rr) {
      const int row = row0 + rr;
      const int sw = ((row & 3) << 2) | ((row >> 2) & 3);
      const u16* src = base + row * DC + ((l ^ sw) * 8);
      unsigned keep; const unsigned dst = (unsigned)(L_CKV + row * 1024) + (unsigned)(size_t)L;
      asm volatile("s_mov_b32 %0, m0\n\ts_mov_b32 m0, %2\n\ts_nop 0\n\tglobal_load_lds_dwordx4 %1, off nt\n\ts_mov_b32 m0, %0" : "=&s"(keep) : "v"(src), "s"(dst) : "memory");
    }
  };
  const v4u SENT4 = {SENT, SENT, SENT, SENT};
  auto fill_x4 = [&]() {
#pragma unroll
    for (int k = 0; k < 2; ++k) st16(SENT4, X4 + ((ab * NH + 16 * hg + 2 * w + k) * 64 + aj) * 1024 + l * 16);
    if (w == 0 && l < 8) st16(SENT4, X4S + ((ab * 64 + aj) * NH + 16 * hg) * 8 + l * 16);
  };
  auto check_flags = [&](int off, int nper) {
    const unsigned tag = TAG();
    for (unsigned sp = 0; sp < SPIN_MAX; ++sp) {
      asm volatile("" ::: "memory");
      bool ok = true;
      for (int k = 0; k < nper; ++k) ok &= ld4(off + (k * 64 + l) * 128) == tag;
      if (__all(ok)) break;
      __builtin_amdgcn_s_sleep(1);
    }
  };

  auto attention = [&](auto&& hook_post) {
    DRAIN();
    BAR();
    TS(11);
    if (w == 7 && l == 0) st4(TAG(), OFF_FLAGS + bid * 128);
    v4u qk[16], pv[16];
    {
      const int key = 16 * w + r16;
      const int sw = ((key & 3) << 2) | ((key >> 2) & 3);
#pragma unroll
      for (int s = 0; s < 16; ++s) qk[s] = *(LAS v4u*)(L + L_CKV + key * 1024 + (s >> 2) * 256 + (((4 * (s & 3) + g) ^ sw) * 16));
      const int q4 = r16 >> 2, p4 = l & 3;
#pragma unroll
      for (int s = 0; s < 4; ++s)
#pragma unroll
        for (int t = 0; t < 4; ++t) {
          v2u h2[2];
#pragma unroll
          for (int hf = 0; hf < 2; ++hf) {
            const int ky = 32 * s + 16 * hf + 4 * g + q4;
            const int sw2 = (q4 << 2) | g;
            const int ch = 8 * w + 4 * (t >> 1) + p4;
            const s16x4 v = __builtin_amdgcn_ds_read_tr16_b64_v4i16((LAS s16x4*)(L + L_CKV + ky * 1024 + ((ch ^ sw2) * 16) + 8 * (t & 1)));
            h2[hf] = __builtin_bit_cast(v2u, v);
          }
          pv[s * 4 + t] = v4u{h2[0].x, h2[0].y, h2[1].x, h2[1].y};
        }
    }
    int a_qw0 = L_QIMG + w * 1024 + ((l ^ w) * 16), a_qw1 = L_QIMG + (8 + w) * 1024 + ((l ^ (8 + w)) * 16);
    int a_qr[4];
#pragma unroll
    for (int t = 0; t < 4; ++t) a_qr[t] = L_QIMG + r16 * 1024 + (((4 * t + g) ^ r16) * 16);
    int a_mxw = L_MISC + r16 * 32 + w * 4, a_mxr = L_MISC + r16 * 32;
    int a_pw = L_PIMG + (w >> 1) * 1024 + l * 16 + (w & 1) * 8, a_pr = L_PIMG + l * 16;
    const int hd = 16 * hg + r16;
    int o_st = X4 + (((ab * NH + hd) * 64 + aj) * 512 + 64 * w + 8 * g) * 2, o_ss = X4S + ((ab * 64 + aj) * NH + hd) * 8;
    int o_q = X3 + (ab * NH + 16 * hg) * 1024 + tid * 16;
    KEEP(a_qw0); KEEP(a_qw1); KEEP4(a_qr); KEEP(a_mxw); KEEP(a_mxr); KEEP(a_pw); KEEP(a_pr); KEEP(o_st); KEEP(o_ss); KEEP(o_q);
#pragma unroll
    for (int s = 0; s < 16; ++s) { KEEP(qk[s]); KEEP(pv[s]); }
    TS(12);
    PIN();
    {
      v4u a0, a1;
      for (unsigned sp = 0; sp < SPIN_MAX; ++sp) {
        asm volatile("" ::: "memory");
        a0 = ld16(o_q); a1 = ld16(o_q + 8192);
        if (__all(umax4a(a1, umax4(a0)) != SENT)) break;
      }
      *(LAS v4u*)(L + a_qw0) = a0; *(LAS v4u*)(L + a_qw1) = a1;
    }
    TS(13);
    unsigned lf[4] = {0u, 0u, 0u, 0u};
    if (w == 6) {
#pragma unroll
      for (int k = 0; k < 4; ++k) lf[k] = ld4(OFF_FLAGS + (k * 64 + l) * 128);
    }
    PIN();
    BAR();
    TS(14);
    f32x4 sacc = {0.f, 0.f, 0.f, 0.f}, sacc2 = {0.f, 0.f, 0.f, 0.f};
    {
      v4u qf[16];
#pragma unroll
      for (int s = 0; s < 16; ++s) qf[s] = *(LAS v4u*)(L + a_qr[s & 3] + (s >> 2) * 256);
      PIN();
#pragma unroll
      for (int s = 0; s < 16; s += 2) { sacc = mfma16(qk[s], qf[s], sacc); sacc2 = mfma16(qk[s + 1], qf[s + 1], sacc2); }
    }
    sacc += sacc2;
    const float mloc = x16_max(fmaxf(fmaxf(sacc[0], sacc[1]), fmaxf(sacc[2], sacc[3])));
    *(LAS float*)(L + a_mxw) = mloc;
    TS(15);
    BAR();
    TS(16);
    const f32x4 m0 = *(LAS f32x4*)(L + a_mxr), m1 = *(LAS f32x4*)(L + a_mxr + 16);
    const float m = fmaxf(fmaxf(fmaxf(m0[0], m0[1]), fmaxf(m0[2], m0[3])), fmaxf(fmaxf(m1[0], m1[1]), fmaxf(m1[2], m1[3])));
    const unsigned d0 = pk2(__builtin_amdgcn_exp2f(sacc[0] - m), __builtin_amdgcn_exp2f(sacc[1] - m)), d1 = pk2(__builtin_amdgcn_exp2f(sacc[2] - m), __builtin_amdgcn_exp2f(sacc[3] - m));
    *(LAS v2u*)(L + a_pw) = v2u{d0, d1};
    BAR();
    TS(17);
    f32x4 o[4], lacc = {0.f, 0.f, 0.f, 0.f};
#pragma unroll
    for (int t = 0; t < 4; ++t) o[t] = f32x4{0.f, 0.f, 0.f, 0.f};
    v4u pf[4];
#pragma unroll
    for (int s = 0; s < 4; ++s) pf[s] = *(LAS v4u*)(L + a_pr + s * 1024);
    PIN();
#pragma unroll
    for (int s = 0; s < 4; ++s)
#pragma unroll
      for (int t = 0; t < 4; ++t) o[t] = mfma16(pv[s * 4 + t], pf[s], o[t]);
    st16(v4u{pk2(o[0][0], o[0][1]), pk2(o[0][2], o[0][3]), pk2(o[1][0], o[1][1]), pk2(o[1][2], o[1][3])}, o_st);
    st16(v4u{pk2(o[2][0], o[2][1]), pk2(o[2][2], o[2][3]), pk2(o[3][0], o[3][1]), pk2(o[3][2], o[3][3])}, o_st + 64);
    if (w == 0) {
      const v4u ones = {0x3F803F80u, 0x3F803F80u, 0x3F803F80u, 0x3F803F80u};
#pragma unroll
      for (int s = 0; s < 4; ++s) lacc = mfma16(ones, pf[s], lacc);
      if (g == 0) st8(v2u{__float_as_uint(m), __float_as_uint(lacc[0])}, o_ss);
    }
    TS(18);
    PIN();
    hook_post();
    PIN();
    if (w == 6) {
      const unsigned tag = TAG();
      if (!__all((lf[0] == tag) & (lf[1] == tag) & (lf[2] == tag) & (lf[3] == tag))) check_flags(OFF_FLAGS, 4);
    }
    BAR();
    TS(19);
  };

  if (Ltype) {
    const int hh = idx >> 2, sub = idx & 3;
    const bool t1role = idx < 40;
    const int mg = idx % 10, pp1 = idx / 10;
    v4u uk[4];
    auto load_t3 = [&]() {
      const u16* src = p.wuk + ((size_t)(128 * sub + 16 * w + r16) * NH + hh) * DH + 8 * g;
#pragma unroll
      for (int s = 0; s < 4; ++s) uk[s] = ldnt(src + 32 * s);
    };
    auto stage_t3 = [&]() {
      if (CKV_B > 0) dma_ckv(16 * w, CKV_B);
      const int kq = (r16 & 7) >> 1, b = r16 & 1;
      int off = X2 + ((hh * 4 + kq) * 2 + b) * 256 + 16 * g;
      int a_w = L_RED + ((g & 1) * 128 + 16 * w + r16) * 2;
      int o_x3 = X3 + ((l >> 4) * NH + hh) * 1024 + sub * 256 + (l & 15) * 16;
      KEEP(off); KEEP(a_w); KEEP(o_x3);
      PIN();
      v4u a[4];
      for (unsigned sp = 0; sp < SPIN_MAX; ++sp) {
        asm volatile("" ::: "memory");
#pragma unroll
        for (int s = 0; s < 4; ++s) a[s] = ld16(off + 64 * s);
        if (__all(umax4a(a[3], umax4a(a[2], umax4a(a[1], umax4(a[0])))) != SENT)) break;
      }
      TS(9);
      f32x4 acc = mfma16(a[0], uk[0], f32x4{0.f, 0.f, 0.f, 0.f}), acc2 = mfma16(a[1], uk[1], f32x4{0.f, 0.f, 0.f, 0.f});
      acc = mfma16(a[2], uk[2], acc); acc2 = mfma16(a[3], uk[3], acc2);
      acc += acc2;
      const float sc = 0.08838834764831845f * 1.4426950408889634f;
      const float tot = rs16(acc[0] + acc[2], acc[1] + acc[3]) * sc;
      if (g < 2) *(LAS u16*)(L + a_w) = (u16)pk2(tot, tot);
      BAR();
      if (w == 0 && l < 32) {
        st16(*(LAS v4u*)(L + L_RED + l * 16), o_x3);
        TS(10);
      }
    };
    if (w < 5) {
      v4u wv2[8];
      const u16* src2 = p.wq + (size_t)(160 * sub + 32 * w + 8 * g) * (NH * DH) + hh * DH + 8 * r16;
      if (t1role) {
        v4u wv[8];
        const int col = (r16 < 8) ? (64 * mg + 8 * r16) : (DM + 64 * mg + 8 * (r16 - 8));
        const int k0 = 160 * pp1 + 32 * w + 8 * g;
        const v4u cv = *(const GAS v4u*)(p.c + (r16 & 1) * DM + k0);
        const unsigned xd = *(const GAS unsigned*)(p.x + (l >> 5) * DM + 64 * mg + 2 * (l & 31));
        const u16* src = p.wmod + (size_t)k0 * (6 * DM) + col;
#pragma unroll
        for (int j = 0; j < 8; ++j) wv[j] = ldnt(src + (size_t)j * (6 * DM));
        int a_w = L_RED + ((w * 2) * 128 + 8 * r16) * 4;
        int a_r = L_RED + ((l >> 5) * 128 + 2 * (l & 31)) * 4;
        int o_x1 = OFF_X1 + (((pp1 * 2 + (l >> 5)) * 320) + 32 * mg + (l & 31)) * 8;
        KEEP(a_w); KEEP(a_r); KEEP(o_x1);
        PIN();
        v4u a = silu8(cv);
        float xlo = bflo(xd), xhi = bfhi(xd);
        unsigned tag = TAG();
        KEEP(a); KEEP(xlo); KEEP(xhi); KEEP(tag);
        PIN();
        v4u f[8]; tr8(wv, f);
        KEEP8(f);
        TS(1);
        PIN();
#pragma unroll
        for (int j = 0; j < 8; ++j) wv2[j] = ldnt(src2 + (size_t)j * (NH * DH));
        PIN();
        f32x4 acc[8];
#pragma unroll
        for (int i = 0; i < 8; ++i) acc[i] = mfma16(a, f[i], f32x4{0.f, 0.f, 0.f, 0.f});
        if (g == 0) {
#pragma unroll
          for (int b = 0; b < 2; ++b) {
            *(LAS f32x4*)(L + a_w + b * 512) = f32x4{acc[0][b], acc[1][b], acc[2][b], acc[3][b]};
            *(LAS f32x4*)(L + a_w + b * 512 + 16) = f32x4{acc[4][b], acc[5][b], acc[6][b], acc[7][b]};
          }
        }
        TS(2);
        BAR();
        TS(3);
        if (w == 0) {
          f32x2 s[5], c2[5];
#pragma unroll
          for (int ww = 0; ww < 5; ++ww) { s[ww] = *(LAS f32x2*)(L + a_r + ww * 1024); c2[ww] = *(LAS f32x2*)(L + a_r + ww * 1024 + 256); }
          const f32x2 mr = *(LAS f32x2*)(L + L_MISC + (l >> 5) * 8);
          const float xn0 = (xlo - mr[0]) * mr[1], xn1 = (xhi - mr[0]) * mr[1];
          const float one = pp1 == 0 ? 1.f : 0.f;
          const float o0 = xn0 * (sum5(c2[0][0], c2[1][0], c2[2][0], c2[3][0], c2[4][0]) + one) + sum5(s[0][0], s[1][0], s[2][0], s[3][0], s[4][0]);
          const float o1 = xn1 * (sum5(c2[0][1], c2[1][1], c2[2][1], c2[3][1], c2[4][1]) + one) + sum5(s[0][1], s[1][1], s[2][1], s[3][1], s[4][1]);
          st8(v2u{pk2(o0, o1), tag}, o_x1);
          TS(4);
        }
      } else {
#pragma unroll
        for (int j = 0; j < 8; ++j) wv2[j] = ldnt(src2 + (size_t)j * (NH * DH));
      }
      load_t3();
      PIN();
      {
        unsigned tag = TAG();
        v4u f[8]; tr8(wv2, f);
        const int pp = (r16 & 7) >> 1, b = r16 & 1;
        int off = OFF_X1 + ((pp * 2 + b) * 320 + (160 * sub + 32 * w + 8 * g) / 2) * 8;
        int a_w = L_QIMG + (((w * 2 + (g & 1)) * 2) * 128 + 8 * r16) * 4;
        int a_r = L_QIMG + ((w & 1) * 128 + 2 * l) * 4, o_x2 = X2 + ((hh * 4 + sub) * 2 + (w & 1)) * 256 + l * 4;
        KEEP8(f); KEEP(off); KEEP(a_w); KEEP(tag); KEEP(a_r); KEEP(o_x2);
        TS(5);
        PIN();
        v4u a;
        for (unsigned sp = 0; sp < SPIN_MAX; ++sp) {
          asm volatile("" ::: "memory");
          const v4u u0 = ld16(off), u1 = ld16(off + 16);
          a = v4u{u0.x, u0.z, u1.x, u1.z};
          if (__all(((u0.y ^ tag) | (u0.w ^ tag) | (u1.y ^ tag) | (u1.w ^ tag)) == 0u)) break;
        }
        TS(6);
        f32x4 acc[8];
#pragma unroll
        for (int i = 0; i < 8; ++i) acc[i] = mfma16(a, f[i], f32x4{0.f, 0.f, 0.f, 0.f});
        if (g < 2) {
#pragma unroll
          for (int b2 = 0; b2 < 2; ++b2) {
            *(LAS f32x4*)(L + a_w + b2 * 512) = f32x4{acc[0][b2] + acc[0][b2 + 2], acc[1][b2] + acc[1][b2 + 2], acc[2][b2] + acc[2][b2 + 2], acc[3][b2] + acc[3][b2 + 2]};
            *(LAS f32x4*)(L + a_w + b2 * 512 + 16) = f32x4{acc[4][b2] + acc[4][b2 + 2], acc[5][b2] + acc[5][b2 + 2], acc[6][b2] + acc[6][b2 + 2], acc[7][b2] + acc[7][b2 + 2]};
          }
        }
        BAR();
        TS(7);
        if (w < 2) {
          f32x2 v[10];
#pragma unroll
          for (int k = 0; k < 10; ++k) v[k] = *(LAS f32x2*)(L + a_r + k * 1024);
          const float s0 = sum5(v[0][0], v[1][0], v[2][0], v[3][0], v[4][0]) + sum5(v[5][0], v[6][0], v[7][0], v[8][0], v[9][0]);
          const float s1 = sum5(v[0][1], v[1][1], v[2][1], v[3][1], v[4][1]) + sum5(v[5][1], v[6][1], v[7][1], v[8][1], v[9][1]);
          st4(pk2(s0, s1), o_x2);
          TS(8);
        }
      }
      stage_t3();
    } else {
      if (w == 7) {
        if (l < 32) st16(SENT4, X2 + (hh * 4 + sub) * 512 + l * 16);
        else st16(SENT4, X3 + (((l - 32) >> 4) * NH + hh) * 1024 + sub * 256 + (l & 15) * 16);
        DRAIN();
        if (l == 0) st4(TAG(), OFF_EFLAGS + idx * 128);
      } else if (t1role) {
        const int b = w - 5;
        float v[10]; float s = 0.f;
#pragma unroll
        for (int k = 0; k < 5; ++k) { const unsigned d = *(const GAS unsigned*)(p.x + b * DM + 2 * (l + 64 * k)); v[2 * k] = bflo(d); v[2 * k + 1] = bfhi(d); s += v[2 * k] + v[2 * k + 1]; }
        const float mean = wave_sum(s) * (1.f / DM);
        float q = 0.f;
#pragma unroll
        for (int k = 0; k < 10; ++k) { const float d = v[k] - mean; q += d * d; }
        const float var = wave_sum(q) * (1.f / DM);
        *(LAS f32x2*)(L + L_MISC + b * 8) = f32x2{mean, __builtin_amdgcn_rsqf(var + 1e-5f)};
      }
      if (t1role) BAR();
      {
        const int h0 = (w - 5) * 6, nh = w == 7 ? 4 : 6;
        for (int k = 0; k < nh; ++k) st16(SENT4, X4 + ((ab * NH + 16 * hg + h0 + k) * 64 + aj) * 1024 + l * 16);
        if (w == 7) {
          if (l < 8) st16(SENT4, X4S + ((ab * 64 + aj) * NH + 16 * hg) * 8 + l * 16);
          else if (l >= 16 && l < 32) {
            const int u = l - 16;
            if (idx < 80) st16(SENT4, X6 + (((idx / 10) * 2 + (u >> 3)) * DM + 64 * (idx % 10)) * 2 + (u & 7) * 16);
            else if (idx < 120) st16(SENT4, XM + ((u >> 3) * 2560 + 64 * (idx - 80)) * 2 + (u & 7) * 16);
          }
        }
      }
      load_t3();
      if (w == 6) check_flags(OFF_EFLAGS, 2);
      BAR();
      stage_t3();
    }
    dma_ckv(16 * w + CKV_B, 16 - CKV_B);
    v4u wpre[3][8];
    attention([&]() {
      if (w < 4) {
        const int kk = (l >> 3) & 1, ch = l & 7;
        if (idx < 80) {
          const int pp = idx / 10, n = idx % 10;
          const u16* src = p.wo + (size_t)(512 * pp + 64 * w + 32 * kk + 8 * g) * DM + 64 * n + 8 * ch;
#pragma unroll
          for (int k = 0; k < 2; ++k)
#pragma unroll
            for (int j = 0; j < 8; ++j) wpre[k][j] = ldnt(src + (size_t)(256 * k + j) * DM);
        } else if (idx < 120) {
          const int tb = idx - 80;
          const u16* src = p.wmod + (size_t)(64 * w + 32 * kk + 8 * g) * (6 * DM) + 2 * DM + 64 * tb + 8 * ch;
#pragma unroll
          for (int k = 0; k < 2; ++k)
#pragma unroll
            for (int j = 0; j < 8; ++j) wpre[k][j] = ldnt(src + (size_t)(256 * k + j) * (6 * DM));
          if (w < 2) {
#pragma unroll
            for (int j = 0; j < 8; ++j) wpre[2][j] = ldnt(src + (size_t)(512 + j) * (6 * DM));
          }
        }
      }
    });
    LAS float* red = (LAS float*)(L + L_RED);
    if (idx < 80) {
      const int pp = idx / 10, n = idx % 10;
      if (w < 4) {
        v4u f0[8], f1[8]; tr8(wpre[0], f0); tr8(wpre[1], f1);
        const int rho = r16 & 7, cq = rho >> 1, b = rho & 1, kk2 = r16 >> 3;
        int off = X5 + (((4 * pp + (w >> 1)) * 4 + cq) * 2 + b) * 256 + (64 * (w & 1) + 32 * kk2 + 8 * g) * 2;
        int a_w = L_RED + (((w * 2 + (g >> 1)) * 2 + (g & 1)) * 64 + 8 * (r16 & 7)) * 4;
        KEEP8(f0); KEEP8(f1); KEEP(off); KEEP(a_w);
        PIN();
        v4u a0, a1;
        for (unsigned sp = 0; sp < SPIN_MAX; ++sp) {
          asm volatile("" ::: "memory");
          a0 = ld16(off); a1 = ld16(off + 2 * 4 * 2 * 256);
          if (__all(umax4a(a1, umax4(a0)) != SENT)) break;
        }
        TS(24);
        f32x4 d[8];
#pragma unroll
        for (int i = 0; i < 8; ++i) d[i] = mfma16(a0, f0[i], f32x4{0.f, 0.f, 0.f, 0.f});
#pragma unroll
        for (int i = 0; i < 8; ++i) d[i] = mfma16(a1, f1[i], d[i]);
        float s[8];
#pragma unroll
        for (int i = 0; i < 8; ++i) s[i] = rs16(d[i][0] + d[i][2], d[i][1] + d[i][3]);
        if ((g >> 1) == (r16 >> 3)) {
          *(LAS f32x4*)(L + a_w) = f32x4{s[0], s[1], s[2], s[3]}; *(LAS f32x4*)(L + a_w + 16) = f32x4{s[4], s[5], s[6], s[7]};
        }
      }
      int a_r = L_RED + ((l >> 5) * 64 + 2 * (l & 31)) * 4, o_x6 = X6 + ((pp * 2 + (l >> 5)) * DM + 64 * n + 2 * (l & 31)) * 2;
      KEEP(a_r); KEEP(o_x6);
      BAR();
      TS(25);
      if (w == 0) {
        f32x2 v[8];
#pragma unroll
        for (int k = 0; k < 8; ++k) v[k] = *(LAS f32x2*)(L + a_r + k * 512);
        const float s0 = ((v[0][0] + v[1][0]) + (v[2][0] + v[3][0])) + ((v[4][0] + v[5][0]) + (v[6][0] + v[7][0]));
        const float s1 = ((v[0][1] + v[1][1]) + (v[2][1] + v[3][1])) + ((v[4][1] + v[5][1]) + (v[6][1] + v[7][1]));
        st4(pk2(s0, s1), o_x6);
        TS(29);
      }
    } else if (idx < 120) {
      const int tb = idx - 80;
      if (w < 4) {
        f32x4 acc[8];
#pragma unroll
        for (int i = 0; i < 8; ++i) acc[i] = f32x4{0.f, 0.f, 0.f, 0.f};
#pragma unroll
        for (int k = 0; k < 3; ++k) {
          if (k < 2 || w < 2) {
            const v4u cv = *(const GAS v4u*)(p.c + (r16 & 1) * DM + 64 * (w + 4 * k) + 32 * (r16 >> 3) + 8 * g);
            const v4u a = silu8(cv);
            v4u f[8];
            tr8(wpre[k], f);
#pragma unroll
            for (int i = 0; i < 8; ++i) acc[i] = mfma16(a, f[i], acc[i]);
          }
        }
        if ((g == 0 && r16 < 8) || (g == 2 && r16 >= 8)) {
#pragma unroll
          for (int b2 = 0; b2 < 2; ++b2) {
            LAS float* d = red + ((w * 2 + (g >> 1)) * 2 + b2) * 64 + 8 * (r16 & 7);
            *(LAS f32x4*)d = f32x4{acc[0][b2], acc[1][b2], acc[2][b2], acc[3][b2]}; *(LAS f32x4*)(d + 4) = f32x4{acc[4][b2], acc[5][b2], acc[6][b2], acc[7][b2]};
          }
        }
      }
      BAR();
      if (w == 0) {
        const int b = l >> 5, pr = l & 31;
        float s0 = 0.f, s1 = 0.f;
#pragma unroll
        for (int k = 0; k < 8; ++k) { const f32x2 v = *(LAS f32x2*)(red + (k * 2 + b) * 64 + 2 * pr); s0 += v[0]; s1 += v[1]; }
        st4(pk2(s0, s1), XM + (b * 2560 + 64 * tb + 2 * pr) * 2);
        TS(29);
      }
    }
  } else {
    fill_x4();
    if (w == 1 && l < 32) st16(SENT4, X5 + idx * 512 + l * 16);
    if (idx < 54 && w >= 2 && w < 5) { const int u = (w - 2) * 64 + l; if (u < 160) st16(SENT4, X7 + idx * 2560 + u * 16); }
    dma_ckv(16 * w, 16);
    if (w == 6) check_flags(OFF_EFLAGS, 2);
    const int h5 = idx >> 2, cq = idx & 3;
    v4u wpre[8];
    attention([&]() {
      if (w < 4) {
        const u16* src = p.wuv + ((size_t)(128 * cq + 32 * w + 8 * g) * NH + h5) * DH + 8 * r16;
#pragma unroll
        for (int j = 0; j < 8; ++j) wpre[j] = ldnt(src + (size_t)j * (NH * DH));
      }
    });
    {
      const int b = w >> 2;
      int off = X4 + (((b * NH + h5) * 64 + 16 * (w & 3) + g) * 512 + 128 * cq + 8 * r16) * 2;
      int offs = X4S + ((b * 64 + 16 * (w & 3) + g) * NH + h5) * 8;
      const int cofs = 8 * r16 + 4 * (l >> 5) + 2 * ((l >> 4) & 1);
      int a_w = L_RED + w * 256 + (((cofs >> 3) ^ w) * 16) + (cofs & 7) * 2;
      int a_s = L_MISC + 1024 + w * 8;
      KEEP(off); KEEP(offs); KEEP(a_w); KEEP(a_s);
      PIN();
      v4u a[4]; v2u sv[4];
      for (unsigned sp = 0; sp < SPIN_MAX; ++sp) {
        asm volatile("" ::: "memory");
#pragma unroll
        for (int k = 0; k < 4; ++k) { sv[k] = ld8(offs + k * 1024); a[k] = ld16(off + k * 4096); }
        unsigned m = umax3(umax3(sv[0].x, sv[0].y, sv[1].x), umax3(sv[1].y, sv[2].x, sv[2].y), umax3(sv[3].x, sv[3].y, 0u));
        m = umax4a(a[1], umax4a(a[0], m)); m = umax4a(a[3], umax4a(a[2], m));
        if (__all(m != SENT)) break;
      }
      TS(20);
      float mw = fmaxf(fmaxf(__uint_as_float(sv[0].x), __uint_as_float(sv[1].x)), fmaxf(__uint_as_float(sv[2].x), __uint_as_float(sv[3].x)));
      mw = x16_max(mw);
      float acc[8] = {0.f, 0.f, 0.f, 0.f, 0.f, 0.f, 0.f, 0.f}; float lw = 0.f;
#pragma unroll
      for (int k = 0; k < 4; ++k) {
        const float e = __builtin_amdgcn_exp2f(__uint_as_float(sv[k].x) - mw);
        const unsigned wlo = pk2(e, 0.f), whi = pk2(0.f, e);
        lw += __uint_as_float(whi) * __uint_as_float(sv[k].y);
#pragma unroll
        for (int d = 0; d < 4; ++d) { acc[2 * d] = dot2(a[k][d], wlo, acc[2 * d]); acc[2 * d + 1] = dot2(a[k][d], whi, acc[2 * d + 1]); }
      }
      const float u0 = rs16(rs32(acc[0], acc[4]), rs32(acc[2], acc[6])), u1 = rs16(rs32(acc[1], acc[5]), rs32(acc[3], acc[7]));
      *(LAS unsigned*)(L + a_w) = pk2(u0, u1);
      lw = x16_sum(lw);
      *(LAS f32x2*)(L + a_s) = f32x2{mw, lw};
    }
    {
      v4u f[8];
      if (w < 4) { tr8(wpre, f); KEEP8(f); }
      const int row = r16 & 7;
      int a_a = L_RED + row * 256 + (((4 * w + g) ^ row) * 16);
      int a_st = L_MISC + 1024 + (g & 1) * 32;
      int a_w2 = L_QIMG + ((w * 2 + (g & 1)) * 128 + 8 * r16) * 4;
      int a_r2 = L_QIMG + ((w & 1) * 128 + 2 * l) * 4, o_x5 = X5 + (idx * 2 + (w & 1)) * 256 + l * 4;
      KEEP(a_a); KEEP(a_st); KEEP(a_w2); KEEP(a_r2); KEEP(o_x5);
      BAR();
      TS(21);
      if (w < 4) {
        const v4u a = *(LAS v4u*)(L + a_a);
        const f32x4 s01 = *(LAS f32x4*)(L + a_st), s23 = *(LAS f32x4*)(L + a_st + 16);
        f32x4 d[8];
#pragma unroll
        for (int i = 0; i < 8; ++i) d[i] = mfma16(a, f[i], f32x4{0.f, 0.f, 0.f, 0.f});
        const float mb = fmaxf(fmaxf(s01[0], s01[2]), fmaxf(s23[0], s23[2]));
        float f0 = __builtin_amdgcn_exp2f(s01[0] - mb), f1 = __builtin_amdgcn_exp2f(s01[2] - mb), f2 = __builtin_amdgcn_exp2f(s23[0] - mb), f3 = __builtin_amdgcn_exp2f(s23[2] - mb);
        const float inv = __builtin_amdgcn_rcpf((f0 * s01[1] + f1 * s01[3]) + (f2 * s23[1] + f3 * s23[3]));
        f0 *= inv; f1 *= inv; f2 *= inv; f3 *= inv;
        float v[8];
#pragma unroll
        for (int i = 0; i < 8; ++i) v[i] = (d[i][0] * f0 + d[i][1] * f1) + (d[i][2] * f2 + d[i][3] * f3);
        if (g < 2) { *(LAS f32x4*)(L + a_w2) = f32x4{v[0], v[1], v[2], v[3]}; *(LAS f32x4*)(L + a_w2 + 16) = f32x4{v[4], v[5], v[6], v[7]}; }
      }
      BAR();
      TS(22);
      if (w < 2) {
        f32x2 v[4];
#pragma unroll
        for (int k = 0; k < 4; ++k) v[k] = *(LAS f32x2*)(L + a_r2 + k * 1024);
        st4(pk2((v[0][0] + v[1][0]) + (v[2][0] + v[3][0]), (v[0][1] + v[1][1]) + (v[2][1] + v[3][1])), o_x5);
        TS(23);
      }
    }
    if (idx < 54) {
      const int kk = (l >> 3) & 1, ch = l & 7;
      v4u wa[3][8], wdn[8];
      if (w < 4) {
        const u16* wsrc = (ch < 4 ? p.wg : p.wu) + 32 * idx + 8 * (ch & 3) + (size_t)(64 * w + 32 * kk + 8 * g) * DF;
#pragma unroll
        for (int k = 0; k < 2; ++k)
#pragma unroll
          for (int j = 0; j < 8; ++j) wa[k][j] = ldnt(wsrc + (size_t)(256 * k + j) * DF);
        if (w < 2) {
#pragma unroll
          for (int j = 0; j < 8; ++j) wa[2][j] = ldnt(wsrc + (size_t)(512 + j) * DF);
        }
      }
      if (w < 5) {
        const u16* src = p.wd + (size_t)(32 * idx + 8 * g) * DM + 128 * w + 8 * r16;
#pragma unroll
        for (int j = 0; j < 8; ++j) wdn[j] = ldnt(src + (size_t)j * DM);
      }
      PIN();
      const int b = w / 3, cc = 32 * (w % 3) + (l & 31), half = l >> 5;
      const bool act_lane = (w < 6) && (cc < 80);
      float x1[4] = {0.f, 0.f, 0.f, 0.f}, cb4[4], sb4[4];
      int a_h2w = L_RED + (b * DM + 8 * cc + 4 * half) * 2;
      if (w < 6) {
        const int ccc = cc < 80 ? cc : 79;
        const int mo = 8 * ccc + 4 * half;
        v2u gav, sbv, cbv;
        for (unsigned sp = 0; sp < SPIN_MAX; ++sp) {
          asm volatile("" ::: "memory");
          gav = ld8(XM + (b * 2560 + mo) * 2); sbv = ld8(XM + (b * 2560 + 640 + mo) * 2); cbv = ld8(XM + (b * 2560 + 1280 + mo) * 2);
          if (__all(umax3(umax3(gav.x, gav.y, sbv.x), umax3(sbv.y, cbv.x, cbv.y), 0u) != SENT)) break;
        }
        const v2u xv = *(const GAS v2u*)(p.x + b * DM + mo);
        float xf[4] = {bflo(xv.x), bfhi(xv.x), bflo(xv.y), bfhi(xv.y)}, ga4[4] = {bflo(gav.x), bfhi(gav.x), bflo(gav.y), bfhi(gav.y)};
        cb4[0] = 1.f + bflo(cbv.x); cb4[1] = 1.f + bfhi(cbv.x); cb4[2] = 1.f + bflo(cbv.y); cb4[3] = 1.f + bfhi(cbv.y);
        sb4[0] = bflo(sbv.x); sb4[1] = bfhi(sbv.x); sb4[2] = bflo(sbv.y); sb4[3] = bfhi(sbv.y);
        if (!act_lane) { ga4[0] = ga4[1] = ga4[2] = ga4[3] = 0.f; xf[0] = xf[1] = xf[2] = xf[3] = 0.f; }
        int off = X6 + ((4 * half * 2 + b) * DM + 8 * ccc) * 2;
        int a_st = L_MISC + w * 8;
        unsigned one_lo = 0x00003F80u, one_hi = 0x3F800000u;
        KEEP(one_lo); KEEP(one_hi);
        KEEP4(xf); KEEP4(ga4); KEEP4(cb4); KEEP4(sb4); KEEP(off); KEEP(a_st); KEEP(a_h2w);
        PIN();
        v4u a[4];
        for (unsigned sp = 0; sp < SPIN_MAX; ++sp) {
          asm volatile("" ::: "memory");
#pragma unroll
          for (int k = 0; k < 4; ++k) a[k] = ld16(off + k * 2560);
          if (__all(umax4a(a[3], umax4a(a[2], umax4a(a[1], umax4(a[0])))) != SENT)) break;
        }
        TS(24);
        float acc[8];
#pragma unroll
        for (int d = 0; d < 4; ++d) {
          acc[2 * d] = dot2(a[3][d], one_lo, dot2(a[2][d], one_lo, dot2(a[1][d], one_lo, bflo(a[0][d]))));
          acc[2 * d + 1] = dot2(a[3][d], one_hi, dot2(a[2][d], one_hi, dot2(a[1][d], one_hi, bfhi(a[0][d]))));
        }
#pragma unroll
        for (int e = 0; e < 4; ++e) x1[e] = xf[e] + ga4[e] * rs32(acc[e], acc[e + 4]);
        const float s1 = wave_sum((x1[0] + x1[1]) + (x1[2] + x1[3]));
        const float s2 = wave_sum((x1[0] * x1[0] + x1[1] * x1[1]) + (x1[2] * x1[2] + x1[3] * x1[3]));
        *(LAS f32x2*)(L + a_st) = f32x2{s1, s2};
      }
      v4u fa[3][8], fd[8];
      if (w < 4) { tr8(wa[0], fa[0]); tr8(wa[1], fa[1]); if (w < 2) tr8(wa[2], fa[2]); }
      int a_st2 = L_MISC + b * 24;
      int a_ar = L_RED + ((r16 & 1) * DM + 64 * w + 32 * (r16 >> 3) + 8 * g) * 2;
      int a_rw = L_QIMG + (((w * 2 + (g >> 1)) * 2) * 64 + 8 * (r16 & 7)) * 4;
      KEEP(a_st2); KEEP(a_ar); KEEP(a_rw);
      BAR();
      TS(25);
      if (act_lane) {
        const f32x2 t0 = *(LAS f32x2*)(L + a_st2), t1 = *(LAS f32x2*)(L + a_st2 + 8), t2 = *(LAS f32x2*)(L + a_st2 + 16);
        const float mean = (t0[0] + t1[0] + t2[0]) * (1.f / DM);
        const float var = fmaxf((t0[1] + t1[1] + t2[1]) * (1.f / DM) - mean * mean, 0.f);
        const float rstd = __builtin_amdgcn_rsqf(var + 1e-5f);
        float hv[4];
#pragma unroll
        for (int e = 0; e < 4; ++e) hv[e] = (x1[e] - mean) * rstd * cb4[e] + sb4[e];
        *(LAS v2u*)(L + a_h2w) = v2u{pk2(hv[0], hv[1]), pk2(hv[2], hv[3])};
      }
      BAR();
      TS(26);
      if (w < 4) {
        const v4u a0 = *(LAS v4u*)(L + a_ar), a1 = *(LAS v4u*)(L + a_ar + 512);
        v4u a2 = a0;
        if (w < 2) a2 = *(LAS v4u*)(L + a_ar + 1024);
        f32x4 acc[8];
#pragma unroll
        for (int i = 0; i < 8; ++i) acc[i] = mfma16(a0, fa[0][i], f32x4{0.f, 0.f, 0.f, 0.f});
#pragma unroll
        for (int i = 0; i < 8; ++i) acc[i] = mfma16(a1, fa[1][i], acc[i]);
        if (w < 2) {
#pragma unroll
          for (int i = 0; i < 8; ++i) acc[i] = mfma16(a2, fa[2][i], acc[i]);
        }
        if ((g == 0 && r16 < 8) || (g == 2 && r16 >= 8)) {
#pragma unroll
          for (int b2 = 0; b2 < 2; ++b2) {
            *(LAS f32x4*)(L + a_rw + b2 * 256) = f32x4{acc[0][b2], acc[1][b2], acc[2][b2], acc[3][b2]};
            *(LAS f32x4*)(L + a_rw + b2 * 256 + 16) = f32x4{acc[4][b2], acc[5][b2], acc[6][b2], acc[7][b2]};
          }
        }
      }
      if (w < 5) { tr8(wdn, fd); KEEP8(fd); }
      int a_gr = L_QIMG + ((l >> 5) * 64 + (l & 31)) * 4, a_aw = L_MISC + 512 + l * 2, a_acr = L_MISC + 512 + ((r16 & 1) * 32 + 8 * g) * 2;
      int o_x7 = X7 + (idx * 2 * DM + 128 * w + 8 * r16) * 2;
      KEEP(a_gr); KEEP(a_aw); KEEP(a_acr); KEEP(o_x7);
      BAR();
      TS(27);
      if (w == 0) {
        float gv[8], uv[8];
#pragma unroll
        for (int k = 0; k < 8; ++k) { gv[k] = *(LAS float*)(L + a_gr + k * 512); uv[k] = *(LAS float*)(L + a_gr + k * 512 + 128); }
        const float gs = ((gv[0] + gv[1]) + (gv[2] + gv[3])) + ((gv[4] + gv[5]) + (gv[6] + gv[7]));
        const float us = ((uv[0] + uv[1]) + (uv[2] + uv[3])) + ((uv[4] + uv[5]) + (uv[6] + uv[7]));
        *(LAS u16*)(L + a_aw) = (u16)pk2(silu(gs) * us, 0.f);
      }
      BAR();
      TS(28);
      if (w < 5) {
        const v4u a = *(LAS v4u*)(L + a_acr);
        f32x4 d[8];
#pragma unroll
        for (int i = 0; i < 8; ++i) d[i] = mfma16(a, fd[i], f32x4{0.f, 0.f, 0.f, 0.f});
        if (g == 0) {
#pragma unroll
          for (int b2 = 0; b2 < 2; ++b2)
            st16(v4u{pk2(d[0][b2], d[1][b2]), pk2(d[2][b2], d[3][b2]), pk2(d[4][b2], d[5][b2]), pk2(d[6][b2], d[7][b2])}, o_x7 + b2 * DM * 2);
        }
        TS(29);
      }
    } else if (idx >= 64 && idx < 84) {
      const int n = idx - 64;
      float base0 = 0.f, base1 = 0.f, gb0 = 0.f, gb1 = 0.f;
      if (w == 7 && l < 32) {
        const int b = l >> 4, pr = l & 15, m = 32 * n + 2 * pr;
        unsigned ga = 0, gb = 0, ap[8];
        for (unsigned sp = 0; sp < SPIN_MAX; ++sp) {
          asm volatile("" ::: "memory");
          ga = ld4(XM + (b * 2560 + m) * 2); gb = ld4(XM + (b * 2560 + 1920 + m) * 2);
#pragma unroll
          for (int k = 0; k < 8; ++k) ap[k] = ld4(X6 + ((k * 2 + b) * DM + m) * 2);
          if (__all(umax3(umax3(umax3(ap[0], ap[1], ap[2]), umax3(ap[3], ap[4], ap[5]), umax3(ap[6], ap[7], ga)), gb, 0u) != SENT)) break;
        }
        const unsigned xd = *(const GAS unsigned*)(p.x + b * DM + m);
        float s0 = 0.f, s1 = 0.f;
#pragma unroll
        for (int k = 0; k < 8; ++k) { s0 += bflo(ap[k]); s1 += bfhi(ap[k]); }
        base0 = bflo(xd) + bflo(ga) * s0; base1 = bfhi(xd) + bfhi(ga) * s1; gb0 = bflo(gb); gb1 = bfhi(gb);
        KEEP(base0); KEEP(base1); KEEP(gb0); KEEP(gb1);
      }
      int a_fr = L_RED + ((l >> 4) * 32 + 2 * (l & 15)) * 4;
      KEEP(a_fr);
      if (w < 7) {
        const int task = 8 * w + (l >> 3), b = (l >> 2) & 1, ch = l & 3;
        const bool valid = task < 54;
        int off = X7 + (((valid ? task : 0) * 2 + b) * DM + 32 * n + 8 * ch) * 2;
        int a_w = L_RED + (w * 64 + b * 32 + 8 * ch + 4 * (l >> 5) + 2 * ((l >> 4) & 1)) * 4;
        unsigned msk = valid ? 0xFFFFFFFFu : 0u;
        KEEP(off); KEEP(a_w); KEEP(msk);
        PIN();
        v4u a;
        for (unsigned sp = 0; sp < SPIN_MAX; ++sp) {
          asm volatile("" ::: "memory");
          a = ld16(off);
          if (__all(umax4(a) != SENT)) break;
        }
        TS(29);
        float v[8];
#pragma unroll
        for (int d = 0; d < 4; ++d) { const unsigned x = a[d] & msk; v[2 * d] = bflo(x); v[2 * d + 1] = bfhi(x); }
        float t0 = rs16(rs32(v[0], v[4]), rs32(v[2], v[6])), t1 = rs16(rs32(v[1], v[5]), rs32(v[3], v[7]));
        t0 += dppf<0x128>(t0); t1 += dppf<0x128>(t1);
        *(LAS f32x2*)(L + a_w) = f32x2{t0, t1};
      }
      BAR();
      if (w == 7 && l < 32) {
        f32x2 v[7];
#pragma unroll
        for (int k = 0; k < 7; ++k) v[k] = *(LAS f32x2*)(L + a_fr + k * 256);
        const float s0 = ((v[0][0] + v[1][0]) + (v[2][0] + v[3][0])) + ((v[4][0] + v[5][0]) + v[6][0]);
        const float s1 = ((v[0][1] + v[1][1]) + (v[2][1] + v[3][1])) + ((v[4][1] + v[5][1]) + v[6][1]);
        *(GAS unsigned*)(p.out + (l >> 4) * DM + 32 * n + 2 * (l & 15)) = pk2(base0 + gb0 * s0, base1 + gb1 * s1);
        if (n == 0 && l == 0) st4(TAG(), OFF_EPOCH);
      }
    }
  }
done: ;
}

extern "C" void kernel_launch(void* const* d_in, const int* in_sizes, int n_in, void* d_out, int out_size, void* d_ws, size_t ws_size, hipStream_t stream) {
  Params p;
  p.x = (const u16*)d_in[0]; p.ckv = (const u16*)d_in[1]; p.wq = (const u16*)d_in[2]; p.wuk = (const u16*)d_in[3]; p.wuv = (const u16*)d_in[4]; p.wo = (const u16*)d_in[5];
  p.wg = (const u16*)d_in[7]; p.wu = (const u16*)d_in[8]; p.wd = (const u16*)d_in[9]; p.c = (const u16*)d_in[10]; p.wmod = (const u16*)d_in[11];
  p.out = (u16*)d_out; p.ws = (unsigned char*)d_ws;
  mega<<<256, 512, 0, stream>>>(p);
}
```

```cpp
#include <hip/hip_runtime.h>
#include <stdint.h>
#define TLB_PF 65536u

#define GAS __attribute__((address_space(1)))
#define LAS __attribute__((address_space(3)))
typedef unsigned v4u __attribute__((ext_vector_type(4)));
typedef unsigned v2u __attribute__((ext_vector_type(2)));
typedef float f32x4 __attribute__((ext_vector_type(4)));
typedef float f32x2 __attribute__((ext_vector_type(2)));
typedef __bf16 bf16x8 __attribute__((ext_vector_type(8)));
typedef __bf16 bf16x2 __attribute__((ext_vector_type(2)));
typedef short s16x4 __attribute__((ext_vector_type(4)));
typedef unsigned short u16;
typedef unsigned long long u64;

constexpr int NB = 2, NH = 32, SK = 8192, DH = 128, DM = 640, DC = 512, DF = 1728;
constexpr int OFF_EPOCH = 0;
constexpr int OFF_FLAGS = 4096;
constexpr int OFF_EFLAGS = OFF_FLAGS + 256 * 128;
constexpr int OFF_X1 = OFF_EFLAGS + 128 * 128;
constexpr int OFF_R = 81920;
constexpr int X2 = OFF_R;
constexpr int X3 = X2 + 65536;
constexpr int X4 = X3 + 65536;
constexpr int X4S = X4 + 4194304;
constexpr int X5 = X4S + 32768;
constexpr int X6 = X5 + 65536;
constexpr int X7 = X6 + 20480;
constexpr int XM = X7 + 138240;
constexpr int R_END = XM + 10240;
static_assert(OFF_X1 + 20480 <= OFF_R, "map");
constexpr int WS_USED = R_END;
#define CKV_A 0
#define CKV_B 6
constexpr unsigned SENT = 0xFFFFFFFFu;
constexpr int AUX_SC1 = 16;
constexpr unsigned SPIN_MAX = 1u << 18;
constexpr int L_CKV = 0;
constexpr int L_QIMG = 131072;
constexpr int L_PIMG = L_QIMG + 16384;
constexpr int L_RED = L_PIMG + 4096;
constexpr int L_MISC = L_RED + 8192;
constexpr int LDS_BYTES = L_MISC + 2048;
static_assert(LDS_BYTES <= 163840, "LDS");

#define TS(k) do { } while (0)

struct Params {
  const u16 *x, *ckv, *wq, *wuk, *wuv, *wo, *wg, *wu, *wd, *c, *wmod;
  u16* out;
  unsigned char* ws;
};

#define BAR() do { asm volatile("s_waitcnt lgkmcnt(0)" ::: "memory"); __builtin_amdgcn_s_barrier(); asm volatile("" ::: "memory"); } while (0)
#define DRAIN() asm volatile("s_waitcnt vmcnt(0)" ::: "memory")
#define PIN() __builtin_amdgcn_sched_barrier(0)
#define KEEP(x) asm volatile("" : "+v"(x))
#define KEEP4(a) do { KEEP(a[0]); KEEP(a[1]); KEEP(a[2]); KEEP(a[3]); } while (0)
#define KEEP8(a) do { KEEP(a[0]); KEEP(a[1]); KEEP(a[2]); KEEP(a[3]); KEEP(a[4]); KEEP(a[5]); KEEP(a[6]); KEEP(a[7]); } while (0)

__device__ __forceinline__ float bflo(unsigned d) { return __uint_as_float(d << 16); }
__device__ __forceinline__ float bfhi(unsigned d) { return __uint_as_float(d & 0xffff0000u); }
__device__ __forceinline__ unsigned pk2(float lo, float hi) { f32x2 f = {lo, hi}; return __builtin_bit_cast(unsigned, __builtin_convertvector(f, bf16x2)); }
__device__ __forceinline__ f32x4 mfma16(v4u a, v4u b, f32x4 c) { return __builtin_amdgcn_mfma_f32_16x16x32_bf16(__builtin_bit_cast(bf16x8, a), __builtin_bit_cast(bf16x8, b), c, 0, 0, 0); }
__device__ __forceinline__ float dot2(unsigned a, unsigned b, float c) { return __builtin_amdgcn_fdot2_f32_bf16(__builtin_bit_cast(bf16x2, a), __builtin_bit_cast(bf16x2, b), c, false); }
__device__ __forceinline__ v4u ldnt(const u16* p) { return __builtin_nontemporal_load((const GAS v4u*)p); }
__device__ __forceinline__ unsigned umax3(unsigned a, unsigned b, unsigned c) { unsigned t = a > b ? a : b; return t > c ? t : c; }
__device__ __forceinline__ unsigned umax4(v4u v) { return umax3(umax3(v.x, v.y, v.z), v.w, 0u); }
__device__ __forceinline__ unsigned umax4a(v4u v, unsigned m) { return umax3(umax3(v.x, v.y, v.z), v.w, m); }
__device__ __forceinline__ float x16_max(float x) {
  auto s = __builtin_amdgcn_permlane16_swap(__float_as_uint(x), __float_as_uint(x), false, false);
  x = fmaxf(__uint_as_float(s[0]), __uint_as_float(s[1]));
  auto t = __builtin_amdgcn_permlane32_swap(__float_as_uint(x), __float_as_uint(x), false, false);
  return fmaxf(__uint_as_float(t[0]), __uint_as_float(t[1]));
}
__device__ __forceinline__ float x16_sum(float x) {
  auto s = __builtin_amdgcn_permlane16_swap(__float_as_uint(x), __float_as_uint(x), false, false);
  x = __uint_as_float(s[0]) + __uint_as_float(s[1]);
  auto t = __builtin_amdgcn_permlane32_swap(__float_as_uint(x), __float_as_uint(x), false, false);
  return __uint_as_float(t[0]) + __uint_as_float(t[1]);
}
__device__ __forceinline__ float rs32(float a, float b) { auto t = __builtin_amdgcn_permlane32_swap(__float_as_uint(a), __float_as_uint(b), false, false); return __uint_as_float(t[0]) + __uint_as_float(t[1]); }
__device__ __forceinline__ float rs16(float a, float b) { auto t = __builtin_amdgcn_permlane16_swap(__float_as_uint(a), __float_as_uint(b), false, false); return __uint_as_float(t[0]) + __uint_as_float(t[1]); }
template <int CTRL> __device__ __forceinline__ float dppf(float x) { return __builtin_bit_cast(float, __builtin_amdgcn_mov_dpp(__builtin_bit_cast(int, x), CTRL, 0xf, 0xf, true)); }
__device__ __forceinline__ float row16_sum(float x) { x += dppf<0xB1>(x); x += dppf<0x4E>(x); x += dppf<0x141>(x); x += dppf<0x128>(x); return x; }
__device__ __forceinline__ float wave_sum(float x) { return x16_sum(row16_sum(x)); }
__device__ __forceinline__ float silu(float v) { return v * __builtin_amdgcn_rcpf(1.f + __builtin_amdgcn_exp2f(v * -1.4426950408889634f)); }

__device__ __forceinline__ void tr8(const v4u (&w)[8], v4u (&f)[8]) {
#pragma unroll
  for (int d = 0; d < 4; ++d) {
#pragma unroll
    for (int i2 = 0; i2 < 4; ++i2) {
      f[2 * i2][d] = __builtin_amdgcn_perm(w[2 * d + 1][i2], w[2 * d][i2], 0x05040100u);
      f[2 * i2 + 1][d] = __builtin_amdgcn_perm(w[2 * d + 1][i2], w[2 * d][i2], 0x07060302u);
    }
  }
}
__device__ __forceinline__ v4u silu8(v4u c) {
  v4u r;
#pragma unroll
  for (int d = 0; d < 4; ++d) r[d] = pk2(silu(bflo(c[d])), silu(bfhi(c[d])));
  return r;
}
__device__ __forceinline__ float sum5(float a, float b, float c, float d, float e) { return ((a + b) + (c + d)) + e; }

__global__ void __launch_bounds__(512, 2) mega(Params p) {
  __shared__ __attribute__((aligned(16))) unsigned char smem[LDS_BYTES];
  LAS unsigned char* L = (LAS unsigned char*)smem;
  const int tid = threadIdx.x, l = tid & 63, w = __builtin_amdgcn_readfirstlane(tid >> 6);
  const int bid = blockIdx.x;
  const int g = l >> 4, r16 = l & 15;
  TS(0);
  if (w == 6) {
    const unsigned dst = (unsigned)(L_MISC + 1792) + (unsigned)(size_t)L;
    auto touch = [&](const void* base, unsigned bytes) {
      for (unsigned o = (unsigned)l * TLB_PF; __any(o < bytes); o += 64u * TLB_PF) {
        const GAS unsigned char* q = (const GAS unsigned char*)base + (o < bytes ? o : 0u);
        unsigned keep;
        asm volatile("s_mov_b32 %0, m0\n\ts_mov_b32 m0, %2\n\ts_nop 0\n\tglobal_load_lds_dword %1, off\n\ts_mov_b32 m0, %0" : "=&s"(keep) : "v"(q), "s"(dst) : "memory");
      }
    };
    touch(p.wq, DM * NH * DH * 2); touch(p.wuk, DC * NH * DH * 2); touch(p.wuv, DC * NH * DH * 2); touch(p.wo, NH * DH * DM * 2);
    touch(p.wg, DM * DF * 2); touch(p.wu, DM * DF * 2); touch(p.wd, DM * DF * 2); touch(p.wmod, DM * 6 * DM * 2); touch(p.ws, WS_USED); touch(p.out, 2560);
  }
  const __amdgpu_buffer_rsrc_t rs = __builtin_amdgcn_make_buffer_rsrc((void*)p.ws, 0, WS_USED, 0x00020000);
  const bool Ltype = bid >= 128;
  const int idx = bid & 127;
  const int ab = idx >> 6, aj = idx & 63, hg = bid >> 7;
  const unsigned epoch_raw = __builtin_amdgcn_raw_buffer_load_b32(rs, OFF_EPOCH, 0, AUX_SC1);
  auto TAG = [&]() { unsigned t = epoch_raw; asm volatile("" : "+v"(t)); return t + 1u; };

  auto ld16 = [&](int off) { return __builtin_amdgcn_raw_buffer_load_b128(rs, off, 0, AUX_SC1); };
  auto ld8 = [&](int off) { return __builtin_amdgcn_raw_buffer_load_b64(rs, off, 0, AUX_SC1); };
  auto ld4 = [&](int off) { return __builtin_amdgcn_raw_buffer_load_b32(rs, off, 0, AUX_SC1); };
  auto st16 = [&](v4u v, int off) { __builtin_amdgcn_raw_buffer_store_b128(v, rs, off, 0, AUX_SC1); };
  auto st8 = [&](v2u v, int off) { __builtin_amdgcn_raw_buffer_store_b64(v, rs, off, 0, AUX_SC1); };
  auto st4 = [&](unsigned v, int off) { __builtin_amdgcn_raw_buffer_store_b32(v, rs, off, 0, AUX_SC1); };
  auto dma_ckv = [&](int row0, int nrows) {
    const u16* base = p.ckv + ((size_t)ab * SK + aj * 128) * DC;
    for (int rr = 0; rr < nrows; ++rr) {
      const int row = row0 + rr;
      const int sw = ((row & 3) << 2) | ((row >> 2) & 3);
      const u16* src = base + row * DC + ((l ^ sw) * 8);
      unsigned keep; const unsigned dst = (unsigned)(L_CKV + row * 1024) + (unsigned)(size_t)L;
      asm volatile("s_mov_b32 %0, m0\n\ts_mov_b32 m0, %2\n\ts_nop 0\n\tglobal_load_lds_dwordx4 %1, off nt\n\ts_mov_b32 m0, %0" : "=&s"(keep) : "v"(src), "s"(dst) : "memory");
    }
  };
  const v4u SENT4 = {SENT, SENT, SENT, SENT};
  auto fill_x4 = [&]() {
#pragma unroll
    for (int k = 0; k < 2; ++k) st16(SENT4, X4 + ((ab * NH + 16 * hg + 2 * w + k) * 64 + aj) * 1024 + l * 16);
    if (w == 0 && l < 8) st16(SENT4, X4S + ((ab * 64 + aj) * NH + 16 * hg) * 8 + l * 16);
  };
  auto check_flags = [&](int off, int nper) {
    const unsigned tag = TAG();
    for (unsigned sp = 0; sp < SPIN_MAX; ++sp) {
      asm volatile("" ::: "memory");
      bool ok = true;
      for (int k = 0; k < nper; ++k) ok &= ld4(off + (k * 64 + l) * 128) == tag;
      if (__all(ok)) break;
      __builtin_amdgcn_s_sleep(1);
    }
  };

  auto attention = [&](auto&& hook_post) {
    DRAIN();
    BAR();
    TS(11);
    if (w == 7 && l == 0) st4(TAG(), OFF_FLAGS + bid * 128);
    v4u qk[16], pv[16];
    {
      const int key = 16 * w + r16;
      const int sw = ((key & 3) << 2) | ((key >> 2) & 3);
#pragma unroll
      for (int s = 0; s < 16; ++s) qk[s] = *(LAS v4u*)(L + L_CKV + key * 1024 + (s >> 2) * 256 + (((4 * (s & 3) + g) ^ sw) * 16));
      const int q4 = r16 >> 2, p4 = l & 3;
#pragma unroll
      for (int s = 0; s < 4; ++s)
#pragma unroll
        for (int t = 0; t < 4; ++t) {
          v2u h2[2];
#pragma unroll
          for (int hf = 0; hf < 2; ++hf) {
            const int ky = 32 * s + 16 * hf + 4 * g + q4;
            const int sw2 = (q4 << 2) | g;
            const int ch = 8 * w + 4 * (t >> 1) + p4;
            const s16x4 v = __builtin_amdgcn_ds_read_tr16_b64_v4i16((LAS s16x4*)(L + L_CKV + ky * 1024 + ((ch ^ sw2) * 16) + 8 * (t & 1)));
            h2[hf] = __builtin_bit_cast(v2u, v);
          }
          pv[s * 4 + t] = v4u{h2[0].x, h2[0].y, h2[1].x, h2[1].y};
        }
    }
    int a_qw0 = L_QIMG + w * 1024 + ((l ^ w) * 16), a_qw1 = L_QIMG + (8 + w) * 1024 + ((l ^ (8 + w)) * 16);
    int a_qr[4];
#pragma unroll
    for (int t = 0; t < 4; ++t) a_qr[t] = L_QIMG + r16 * 1024 + (((4 * t + g) ^ r16) * 16);
    int a_mxw = L_MISC + r16 * 32 + w * 4, a_mxr = L_MISC + r16 * 32;
    int a_pw = L_PIMG + (w >> 1) * 1024 + l * 16 + (w & 1) * 8, a_pr = L_PIMG + l * 16;
    const int hd = 16 * hg + r16;
    int o_st = X4 + (((ab * NH + hd) * 64 + aj) * 512 + 64 * w + 8 * g) * 2, o_ss = X4S + ((ab * 64 + aj) * NH + hd) * 8;
    int o_q = X3 + (ab * NH + 16 * hg) * 1024 + tid * 16;
    KEEP(a_qw0); KEEP(a_qw1); KEEP4(a_qr); KEEP(a_mxw); KEEP(a_mxr); KEEP(a_pw); KEEP(a_pr); KEEP(o_st); KEEP(o_ss); KEEP(o_q);
#pragma unroll
    for (int s = 0; s < 16; ++s) { KEEP(qk[s]); KEEP(pv[s]); }
    TS(12);
    PIN();
    {
      v4u a0, a1;
      for (unsigned sp = 0; sp < SPIN_MAX; ++sp) {
        asm volatile("" ::: "memory");
        a0 = ld16(o_q); a1 = ld16(o_q + 8192);
        if (__all(umax4a(a1, umax4(a0)) != SENT)) break;
      }
      *(LAS v4u*)(L + a_qw0) = a0; *(LAS v4u*)(L + a_qw1) = a1;
    }
    TS(13);
    unsigned lf[4] = {0u, 0u, 0u, 0u};
    if (w == 6) {
#pragma unroll
      for (int k = 0; k < 4; ++k) lf[k] = ld4(OFF_FLAGS + (k * 64 + l) * 128);
    }
    PIN();
    BAR();
    TS(14);
    f32x4 sacc = {0.f, 0.f, 0.f, 0.f}, sacc2 = {0.f, 0.f, 0.f, 0.f};
    {
      v4u qf[16];
#pragma unroll
      for (int s = 0; s < 16; ++s) qf[s] = *(LAS v4u*)(L + a_qr[s & 3] + (s >> 2) * 256);
      PIN();
#pragma unroll
      for (int s = 0; s < 16; s += 2) { sacc = mfma16(qk[s], qf[s], sacc); sacc2 = mfma16(qk[s + 1], qf[s + 1], sacc2); }
    }
    sacc += sacc2;
    const float mloc = x16_max(fmaxf(fmaxf(sacc[0], sacc[1]), fmaxf(sacc[2], sacc[3])));
    *(LAS float*)(L + a_mxw) = mloc;
    TS(15);
    BAR();
    TS(16);
    const f32x4 m0 = *(LAS f32x4*)(L + a_mxr), m1 = *(LAS f32x4*)(L + a_mxr + 16);
    const float m = fmaxf(fmaxf(fmaxf(m0[0], m0[1]), fmaxf(m0[2], m0[3])), fmaxf(fmaxf(m1[0], m1[1]), fmaxf(m1[2], m1[3])));
    const unsigned d0 = pk2(__builtin_amdgcn_exp2f(sacc[0] - m), __builtin_amdgcn_exp2f(sacc[1] - m)), d1 = pk2(__builtin_amdgcn_exp2f(sacc[2] - m), __builtin_amdgcn_exp2f(sacc[3] - m));
    *(LAS v2u*)(L + a_pw) = v2u{d0, d1};
    BAR();
    TS(17);
    f32x4 o[4], lacc = {0.f, 0.f, 0.f, 0.f};
#pragma unroll
    for (int t = 0; t < 4; ++t) o[t] = f32x4{0.f, 0.f, 0.f, 0.f};
    v4u pf[4];
#pragma unroll
    for (int s = 0; s < 4; ++s) pf[s] = *(LAS v4u*)(L + a_pr + s * 1024);
    PIN();
#pragma unroll
    for (int s = 0; s < 4; ++s)
#pragma unroll
      for (int t = 0; t < 4; ++t) o[t] = mfma16(pv[s * 4 + t], pf[s], o[t]);
    st16(v4u{pk2(o[0][0], o[0][1]), pk2(o[0][2], o[0][3]), pk2(o[1][0], o[1][1]), pk2(o[1][2], o[1][3])}, o_st);
    st16(v4u{pk2(o[2][0], o[2][1]), pk2(o[2][2], o[2][3]), pk2(o[3][0], o[3][1]), pk2(o[3][2], o[3][3])}, o_st + 64);
    if (w == 0) {
      const v4u ones = {0x3F803F80u, 0x3F803F80u, 0x3F803F80u, 0x3F803F80u};
#pragma unroll
      for (int s = 0; s < 4; ++s) lacc = mfma16(ones, pf[s], lacc);
      if (g == 0) st8(v2u{__float_as_uint(m), __float_as_uint(lacc[0])}, o_ss);
    }
    TS(18);
    PIN();
    hook_post();
    PIN();
    if (w == 6) {
      const unsigned tag = TAG();
      if (!__all((lf[0] == tag) & (lf[1] == tag) & (lf[2] == tag) & (lf[3] == tag))) check_flags(OFF_FLAGS, 4);
    }
    BAR();
    TS(19);
  };

  if (Ltype) {
    const int hh = idx >> 2, sub = idx & 3;
    const bool t1role = idx < 40;
    const int mg = idx % 10, pp1 = idx / 10;
    v4u uk[4];
    auto load_t3 = [&]() {
      const u16* src = p.wuk + ((size_t)(128 * sub + 16 * w + r16) * NH + hh) * DH + 8 * g;
#pragma unroll
      for (int s = 0; s < 4; ++s) uk[s] = ldnt(src + 32 * s);
    };
    auto stage_t3 = [&]() {
      if (CKV_B > 0) dma_ckv(16 * w, CKV_B);
      const int kq = (r16 & 7) >> 1, b = r16 & 1;
      int off = X2 + ((hh * 4 + kq) * 2 + b) * 256 + 16 * g;
      int a_w = L_RED + ((g & 1) * 128 + 16 * w + r16) * 2;
      int o_x3 = X3 + ((l >> 4) * NH + hh) * 1024 + sub * 256 + (l & 15) * 16;
      KEEP(off); KEEP(a_w); KEEP(o_x3);
      PIN();
      v4u a[4];
      for (unsigned sp = 0; sp < SPIN_MAX; ++sp) {
        asm volatile("" ::: "memory");
#pragma unroll
        for (int s = 0; s < 4; ++s) a[s] = ld16(off + 64 * s);
        if (__all(umax4a(a[3], umax4a(a[2], umax4a(a[1], umax4(a[0])))) != SENT)) break;
      }
      TS(9);
      f32x4 acc = mfma16(a[0], uk[0], f32x4{0.f, 0.f, 0.f, 0.f}), acc2 = mfma16(a[1], uk[1], f32x4{0.f, 0.f, 0.f, 0.f});
      acc = mfma16(a[2], uk[2], acc); acc2 = mfma16(a[3], uk[3], acc2);
      acc += acc2;
      const float sc = 0.08838834764831845f * 1.4426950408889634f;
      const float tot = rs16(acc[0] + acc[2], acc[1] + acc[3]) * sc;
      if (g < 2) *(LAS u16*)(L + a_w) = (u16)pk2(tot, tot);
      BAR();
      if (w == 0 && l < 32) {
        st16(*(LAS v4u*)(L + L_RED + l * 16), o_x3);
        TS(10);
      }
    };
    if (w < 5) {
      v4u wv2[8];
      const u16* src2 = p.wq + (size_t)(160 * sub + 32 * w + 8 * g) * (NH * DH) + hh * DH + 8 * r16;
      if (t1role) {
        v4u wv[8];
        const int col = (r16 < 8) ? (64 * mg + 8 * r16) : (DM + 64 * mg + 8 * (r16 - 8));
        const int k0 = 160 * pp1 + 32 * w + 8 * g;
        const v4u cv = *(const GAS v4u*)(p.c + (r16 & 1) * DM + k0);
        const unsigned xd = *(const GAS unsigned*)(p.x + (l >> 5) * DM + 64 * mg + 2 * (l & 31));
        const u16* src = p.wmod + (size_t)k0 * (6 * DM) + col;
#pragma unroll
        for (int j = 0; j < 8; ++j) wv[j] = ldnt(src + (size_t)j * (6 * DM));
        int a_w = L_RED + ((w * 2) * 128 + 8 * r16) * 4;
        int a_r = L_RED + ((l >> 5) * 128 + 2 * (l & 31)) * 4;
        int o_x1 = OFF_X1 + (((pp1 * 2 + (l >> 5)) * 320) + 32 * mg + (l & 31)) * 8;
        KEEP(a_w); KEEP(a_r); KEEP(o_x1);
        PIN();
        v4u a = silu8(cv);
        float xlo = bflo(xd), xhi = bfhi(xd);
        unsigned tag = TAG();
        KEEP(a); KEEP(xlo); KEEP(xhi); KEEP(tag);
        PIN();
        v4u f[8]; tr8(wv, f);
        KEEP8(f);
        TS(1);
        PIN();
#pragma unroll
        for (int j = 0; j < 8; ++j) wv2[j] = ldnt(src2 + (size_t)j * (NH * DH));
        PIN();
        f32x4 acc[8];
#pragma unroll
        for (int i = 0; i < 8; ++i) acc[i] = mfma16(a, f[i], f32x4{0.f, 0.f, 0.f, 0.f});
        if (g == 0) {
#pragma unroll
          for (int b = 0; b < 2; ++b) {
            *(LAS f32x4*)(L + a_w + b * 512) = f32x4{acc[0][b], acc[1][b], acc[2][b], acc[3][b]};
            *(LAS f32x4*)(L + a_w + b * 512 + 16) = f32x4{acc[4][b], acc[5][b], acc[6][b], acc[7][b]};
          }
        }
        TS(2);
        BAR();
        TS(3);
        if (w == 0) {
          f32x2 s[5], c2[5];
#pragma unroll
          for (int ww = 0; ww < 5; ++ww) { s[ww] = *(LAS f32x2*)(L + a_r + ww * 1024); c2[ww] = *(LAS f32x2*)(L + a_r + ww * 1024 + 256); }
          const f32x2 mr = *(LAS f32x2*)(L + L_MISC + (l >> 5) * 8);
          const float xn0 = (xlo - mr[0]) * mr[1], xn1 = (xhi - mr[0]) * mr[1];
          const float one = pp1 == 0 ? 1.f : 0.f;
          const float o0 = xn0 * (sum5(c2[0][0], c2[1][0], c2[2][0], c2[3][0], c2[4][0]) + one) + sum5(s[0][0], s[1][0], s[2][0], s[3][0], s[4][0]);
          const float o1 = xn1 * (sum5(c2[0][1], c2[1][1], c2[2][1], c2[3][1], c2[4][1]) + one) + sum5(s[0][1], s[1][1], s[2][1], s[3][1], s[4][1]);
          st8(v2u{pk2(o0, o1), tag}, o_x1);
          TS(4);
        }
      } else {
#pragma unroll
        for (int j = 0; j < 8; ++j) wv2[j] = ldnt(src2 + (size_t)j * (NH * DH));
      }
      load_t3();
      PIN();
      {
        unsigned tag = TAG();
        v4u f[8]; tr8(wv2, f);
        const int pp = (r16 & 7) >> 1, b = r16 & 1;
        int off = OFF_X1 + ((pp * 2 + b) * 320 + (160 * sub + 32 * w + 8 * g) / 2) * 8;
        int a_w = L_QIMG + (((w * 2 + (g & 1)) * 2) * 128 + 8 * r16) * 4;
        int a_r = L_QIMG + ((w & 1) * 128 + 2 * l) * 4, o_x2 = X2 + ((hh * 4 + sub) * 2 + (w & 1)) * 256 + l * 4;
        KEEP8(f); KEEP(off); KEEP(a_w); KEEP(tag); KEEP(a_r); KEEP(o_x2);
        TS(5);
        PIN();
        v4u a;
        for (unsigned sp = 0; sp < SPIN_MAX; ++sp) {
          asm volatile("" ::: "memory");
          const v4u u0 = ld16(off), u1 = ld16(off + 16);
          a = v4u{u0.x, u0.z, u1.x, u1.z};
          if (__all(((u0.y ^ tag) | (u0.w ^ tag) | (u1.y ^ tag) | (u1.w ^ tag)) == 0u)) break;
        }
        TS(6);
        f32x4 acc[8];
#pragma unroll
        for (int i = 0; i < 8; ++i) acc[i] = mfma16(a, f[i], f32x4{0.f, 0.f, 0.f, 0.f});
        if (g < 2) {
#pragma unroll
          for (int b2 = 0; b2 < 2; ++b2) {
            *(LAS f32x4*)(L + a_w + b2 * 512) = f32x4{acc[0][b2] + acc[0][b2 + 2], acc[1][b2] + acc[1][b2 + 2], acc[2][b2] + acc[2][b2 + 2], acc[3][b2] + acc[3][b2 + 2]};
            *(LAS f32x4*)(L + a_w + b2 * 512 + 16) = f32x4{acc[4][b2] + acc[4][b2 + 2], acc[5][b2] + acc[5][b2 + 2], acc[6][b2] + acc[6][b2 + 2], acc[7][b2] + acc[7][b2 + 2]};
          }
        }
        BAR();
        TS(7);
        if (w < 2) {
          f32x2 v[10];
#pragma unroll
          for (int k = 0; k < 10; ++k) v[k] = *(LAS f32x2*)(L + a_r + k * 1024);
          const float s0 = sum5(v[0][0], v[1][0], v[2][0], v[3][0], v[4][0]) + sum5(v[5][0], v[6][0], v[7][0], v[8][0], v[9][0]);
          const float s1 = sum5(v[0][1], v[1][1], v[2][1], v[3][1], v[4][1]) + sum5(v[5][1], v[6][1], v[7][1], v[8][1], v[9][1]);
          st4(pk2(s0, s1), o_x2);
          TS(8);
        }
      }
      stage_t3();
    } else {
      if (w == 7) {
        if (l < 32) st16(SENT4, X2 + (hh * 4 + sub) * 512 + l * 16);
        else st16(SENT4, X3 + (((l - 32) >> 4) * NH + hh) * 1024 + sub * 256 + (l & 15) * 16);
        DRAIN();
        if (l == 0) st4(TAG(), OFF_EFLAGS + idx * 128);
      } else if (t1role) {
        const int b = w - 5;
        float v[10]; float s = 0.f;
#pragma unroll
        for (int k = 0; k < 5; ++k) { const unsigned d = *(const GAS unsigned*)(p.x + b * DM + 2 * (l + 64 * k)); v[2 * k] = bflo(d); v[2 * k + 1] = bfhi(d); s += v[2 * k] + v[2 * k + 1]; }
        const float mean = wave_sum(s) * (1.f / DM);
        float q = 0.f;
#pragma unroll
        for (int k = 0; k < 10; ++k) { const float d = v[k] - mean; q += d * d; }
        const float var = wave_sum(q) * (1.f / DM);
        *(LAS f32x2*)(L + L_MISC + b * 8) = f32x2{mean, __builtin_amdgcn_rsqf(var + 1e-5f)};
      }
      if (t1role) BAR();
      {
        const int h0 = (w - 5) * 6, nh = w == 7 ? 4 : 6;
        for (int k = 0; k < nh; ++k) st16(SENT4, X4 + ((ab * NH + 16 * hg + h0 + k) * 64 + aj) * 1024 + l * 16);
        if (w == 7) {
          if (l < 8) st16(SENT4, X4S + ((ab * 64 + aj) * NH + 16 * hg) * 8 + l * 16);
          else if (l >= 16 && l < 32) {
            const int u = l - 16;
            if (idx < 80) st16(SENT4, X6 + (((idx / 10) * 2 + (u >> 3)) * DM + 64 * (idx % 10)) * 2 + (u & 7) * 16);
            else if (idx < 120) st16(SENT4, XM + ((u >> 3) * 2560 + 64 * (idx - 80)) * 2 + (u & 7) * 16);
          }
        }
      }
      load_t3();
      if (w == 6) check_flags(OFF_EFLAGS, 2);
      BAR();
      stage_t3();
    }
    dma_ckv(16 * w + CKV_B, 16 - CKV_B);
    v4u wpre[3][8];
    attention([&]() {
      if (w < 4) {
        const int kk = (l >> 3) & 1, ch = l & 7;
        if (idx < 80) {
          const int pp = idx / 10, n = idx % 10;
          const u16* src = p.wo + (size_t)(512 * pp + 64 * w + 32 * kk + 8 * g) * DM + 64 * n + 8 * ch;
#pragma unroll
          for (int k = 0; k < 2; ++k)
#pragma unroll
            for (int j = 0; j < 8; ++j) wpre[k][j] = ldnt(src + (size_t)(256 * k + j) * DM);
        } else if (idx < 120) {
          const int tb = idx - 80;
          const u16* src = p.wmod + (size_t)(64 * w + 32 * kk + 8 * g) * (6 * DM) + 2 * DM + 64 * tb + 8 * ch;
#pragma unroll
          for (int k = 0; k < 2; ++k)
#pragma unroll
            for (int j = 0; j < 8; ++j) wpre[k][j] = ldnt(src + (size_t)(256 * k + j) * (6 * DM));
          if (w < 2) {
#pragma unroll
            for (int j = 0; j < 8; ++j) wpre[2][j] = ldnt(src + (size_t)(512 + j) * (6 * DM));
          }
        }
      }
    });
    LAS float* red = (LAS float*)(L + L_RED);
    if (idx < 80) {
      const int pp = idx / 10, n = idx % 10;
      if (w < 4) {
        v4u f0[8], f1[8]; tr8(wpre[0], f0); tr8(wpre[1], f1);
        const int rho = r16 & 7, cq = rho >> 1, b = rho & 1, kk2 = r16 >> 3;
        int off = X5 + (((4 * pp + (w >> 1)) * 4 + cq) * 2 + b) * 256 + (64 * (w & 1) + 32 * kk2 + 8 * g) * 2;
        int a_w = L_RED + (((w * 2 + (g >> 1)) * 2 + (g & 1)) * 64 + 8 * (r16 & 7)) * 4;
        KEEP8(f0); KEEP8(f1); KEEP(off); KEEP(a_w);
        PIN();
        v4u a0, a1;
        for (unsigned sp = 0; sp < SPIN_MAX; ++sp) {
          asm volatile("" ::: "memory");
          a0 = ld16(off); a1 = ld16(off + 2 * 4 * 2 * 256);
          if (__all(umax4a(a1, umax4(a0)) != SENT)) break;
        }
        TS(24);
        f32x4 d[8];
#pragma unroll
        for (int i = 0; i < 8; ++i) d[i] = mfma16(a0, f0[i], f32x4{0.f, 0.f, 0.f, 0.f});
#pragma unroll
        for (int i = 0; i < 8; ++i) d[i] = mfma16(a1, f1[i], d[i]);
        float s[8];
#pragma unroll
        for (int i = 0; i < 8; ++i) s[i] = rs16(d[i][0] + d[i][2], d[i][1] + d[i][3]);
        if ((g >> 1) == (r16 >> 3)) {
          *(LAS f32x4*)(L + a_w) = f32x4{s[0], s[1], s[2], s[3]}; *(LAS f32x4*)(L + a_w + 16) = f32x4{s[4], s[5], s[6], s[7]};
        }
      }
      int a_r = L_RED + ((l >> 5) * 64 + 2 * (l & 31)) * 4, o_x6 = X6 + ((pp * 2 + (l >> 5)) * DM + 64 * n + 2 * (l & 31)) * 2;
      KEEP(a_r); KEEP(o_x6);
      BAR();
      TS(25);
      if (w == 0) {
        f32x2 v[8];
#pragma unroll
        for (int k = 0; k < 8; ++k) v[k] = *(LAS f32x2*)(L + a_r + k * 512);
        const float s0 = ((v[0][0] + v[1][0]) + (v[2][0] + v[3][0])) + ((v[4][0] + v[5][0]) + (v[6][0] + v[7][0]));
        const float s1 = ((v[0][1] + v[1][1]) + (v[2][1] + v[3][1])) + ((v[4][1] + v[5][1]) + (v[6][1] + v[7][1]));
        st4(pk2(s0, s1), o_x6);
        TS(29);
      }
    } else if (idx < 120) {
      const int tb = idx - 80;
      if (w < 4) {
        f32x4 acc[8];
#pragma unroll
        for (int i = 0; i < 8; ++i) acc[i] = f32x4{0.f, 0.f, 0.f, 0.f};
#pragma unroll
        for (int k = 0; k < 3; ++k) {
          if (k < 2 || w < 2) {
            const v4u cv = *(const GAS v4u*)(p.c + (r16 & 1) * DM + 64 * (w + 4 * k) + 32 * (r16 >> 3) + 8 * g);
            const v4u a = silu8(cv);
            v4u f[8];
            tr8(wpre[k], f);
#pragma unroll
            for (int i = 0; i < 8; ++i) acc[i] = mfma16(a, f[i], acc[i]);
          }
        }
        if ((g == 0 && r16 < 8) || (g == 2 && r16 >= 8)) {
#pragma unroll
          for (int b2 = 0; b2 < 2; ++b2) {
            LAS float* d = red + ((w * 2 + (g >> 1)) * 2 + b2) * 64 + 8 * (r16 & 7);
            *(LAS f32x4*)d = f32x4{acc[0][b2], acc[1][b2], acc[2][b2], acc[3][b2]}; *(LAS f32x4*)(d + 4) = f32x4{acc[4][b2], acc[5][b2], acc[6][b2], acc[7][b2]};
          }
        }
      }
      BAR();
      if (w == 0) {
        const int b = l >> 5, pr = l & 31;
        float s0 = 0.f, s1 = 0.f;
#pragma unroll
        for (int k = 0; k < 8; ++k) { const f32x2 v = *(LAS f32x2*)(red + (k * 2 + b) * 64 + 2 * pr); s0 += v[0]; s1 += v[1]; }
        st4(pk2(s0, s1), XM + (b * 2560 + 64 * tb + 2 * pr) * 2);
        TS(29);
      }
    }
  } else {
    fill_x4();
    if (w == 1 && l < 32) st16(SENT4, X5 + idx * 512 + l * 16);
    if (idx < 54 && w >= 2 && w < 5) { const int u = (w - 2) * 64 + l; if (u < 160) st16(SENT4, X7 + idx * 2560 + u * 16); }
    dma_ckv(16 * w, 16);
    if (w == 6) check_flags(OFF_EFLAGS, 2);
    const int h5 = idx >> 2, cq = idx & 3;
    v4u wpre[8];
    attention([&]() {
      if (w < 4) {
        const u16* src = p.wuv + ((size_t)(128 * cq + 32 * w + 8 * g) * NH + h5) * DH + 8 * r16;
#pragma unroll
        for (int j = 0; j < 8; ++j) wpre[j] = ldnt(src + (size_t)j * (NH * DH));
      }
    });
    {
      const int b = w >> 2;
      int off = X4 + (((b * NH + h5) * 64 + 16 * (w & 3) + g) * 512 + 128 * cq + 8 * r16) * 2;
      int offs = X4S + ((b * 64 + 16 * (w & 3) + g) * NH + h5) * 8;
      const int cofs = 8 * r16 + 4 * (l >> 5) + 2 * ((l >> 4) & 1);
      int a_w = L_RED + w * 256 + (((cofs >> 3) ^ w) * 16) + (cofs & 7) * 2;
      int a_s = L_MISC + 1024 + w * 8;
      KEEP(off); KEEP(offs); KEEP(a_w); KEEP(a_s);
      PIN();
      v4u a[4]; v2u sv[4];
      for (unsigned sp = 0; sp < SPIN_MAX; ++sp) {
        asm volatile("" ::: "memory");
#pragma unroll
        for (int k = 0; k < 4; ++k) { sv[k] = ld8(offs + k * 1024); a[k] = ld16(off + k * 4096); }
        unsigned m = umax3(umax3(sv[0].x, sv[0].y, sv[1].x), umax3(sv[1].y, sv[2].x, sv[2].y), umax3(sv[3].x, sv[3].y, 0u));
        m = umax4a(a[1], umax4a(a[0], m)); m = umax4a(a[3], umax4a(a[2], m));
        if (__all(m != SENT)) break;
      }
      TS(20);
      float mw = fmaxf(fmaxf(__uint_as_float(sv[0].x), __uint_as_float(sv[1].x)), fmaxf(__uint_as_float(sv[2].x), __uint_as_float(sv[3].x)));
      mw = x16_max(mw);
      float acc[8] = {0.f, 0.f, 0.f, 0.f, 0.f, 0.f, 0.f, 0.f}; float lw = 0.f;
#pragma unroll
      for (int k = 0; k < 4; ++k) {
        const float e = __builtin_amdgcn_exp2f(__uint_as_float(sv[k].x) - mw);
        const unsigned wlo = pk2(e, 0.f), whi = pk2(0.f, e);
        lw += __uint_as_float(whi) * __uint_as_float(sv[k].y);
#pragma unroll
        for (int d = 0; d < 4; ++d) { acc[2 * d] = dot2(a[k][d], wlo, acc[2 * d]); acc[2 * d + 1] = dot2(a[k][d], whi, acc[2 * d + 1]); }
      }
      const float u0 = rs16(rs32(acc[0], acc[4]), rs32(acc[2], acc[6])), u1 = rs16(rs32(acc[1], acc[5]), rs32(acc[3], acc[7]));
      *(LAS unsigned*)(L + a_w) = pk2(u0, u1);
      lw = x16_sum(lw);
      *(LAS f32x2*)(L + a_s) = f32x2{mw, lw};
    }
    {
      v4u f[8];
      if (w < 4) { tr8(wpre, f); KEEP8(f); }
      const int row = r16 & 7;
      int a_a = L_RED + row * 256 + (((4 * w + g) ^ row) * 16);
      int a_st = L_MISC + 1024 + (g & 1) * 32;
      int a_w2 = L_QIMG + ((w * 2 + (g & 1)) * 128 + 8 * r16) * 4;
      int a_r2 = L_QIMG + ((w & 1) * 128 + 2 * l) * 4, o_x5 = X5 + (idx * 2 + (w & 1)) * 256 + l * 4;
      KEEP(a_a); KEEP(a_st); KEEP(a_w2); KEEP(a_r2); KEEP(o_x5);
      BAR();
      TS(21);
      if (w < 4) {
        const v4u a = *(LAS v4u*)(L + a_a);
        const f32x4 s01 = *(LAS f32x4*)(L + a_st), s23 = *(LAS f32x4*)(L + a_st + 16);
        f32x4 d[8];
#pragma unroll
        for (int i = 0; i < 8; ++i) d[i] = mfma16(a, f[i], f32x4{0.f, 0.f, 0.f, 0.f});
        const float mb = fmaxf(fmaxf(s01[0], s01[2]), fmaxf(s23[0], s23[2]));
        float f0 = __builtin_amdgcn_exp2f(s01[0] - mb), f1 = __builtin_amdgcn_exp2f(s01[2] - mb), f2 = __builtin_amdgcn_exp2f(s23[0] - mb), f3 = __builtin_amdgcn_exp2f(s23[2] - mb);
        const float inv = __builtin_amdgcn_rcpf((f0 * s01[1] + f1 * s01[3]) + (f2 * s23[1] + f3 * s23[3]));
        f0 *= inv; f1 *= inv; f2 *= inv; f3 *= inv;
        float v[8];
#pragma unroll
        for (int i = 0; i < 8; ++i) v[i] = (d[i][0] * f0 + d[i][1] * f1) + (d[i][2] * f2 + d[i][3] * f3);
        if (g < 2) { *(LAS f32x4*)(L + a_w2) = f32x4{v[0], v[1], v[2], v[3]}; *(LAS f32x4*)(L + a_w2 + 16) = f32x4{v[4], v[5], v[6], v[7]}; }
      }
      BAR();
      TS(22);
      if (w < 2) {
        f32x2 v[4];
#pragma unroll
        for (int k = 0; k < 4; ++k) v[k] = *(LAS f32x2*)(L + a_r2 + k * 1024);
        st4(pk2((v[0][0] + v[1][0]) + (v[2][0] + v[3][0]), (v[0][1] + v[1][1]) + (v[2][1] + v[3][1])), o_x5);
        TS(23);
      }
    }
    if (idx < 54) {
      const int kk = (l >> 3) & 1, ch = l & 7;
      v4u wa[3][8], wdn[8];
      if (w < 4) {
        const u16* wsrc = (ch < 4 ? p.wg : p.wu) + 32 * idx + 8 * (ch & 3) + (size_t)(64 * w + 32 * kk + 8 * g) * DF;
#pragma unroll
        for (int k = 0; k < 2; ++k)
#pragma unroll
          for (int j = 0; j < 8; ++j) wa[k][j] = ldnt(wsrc + (size_t)(256 * k + j) * DF);
        if (w < 2) {
#pragma unroll
          for (int j = 0; j < 8; ++j) wa[2][j] = ldnt(wsrc + (size_t)(512 + j) * DF);
        }
      }
      if (w < 5) {
        const u16* src = p.wd + (size_t)(32 * idx + 8 * g) * DM + 128 * w + 8 * r16;
#pragma unroll
        for (int j = 0; j < 8; ++j) wdn[j] = ldnt(src + (size_t)j * DM);
      }
      PIN();
      const int b = w / 3, cc = 32 * (w % 3) + (l & 31), half = l >> 5;
      const bool act_lane = (w < 6) && (cc < 80);
      float x1[4] = {0.f, 0.f, 0.f, 0.f}, cb4[4], sb4[4];
      int a_h2w = L_RED + (b * DM + 8 * cc + 4 * half) * 2;
      if (w < 6) {
        const int ccc = cc < 80 ? cc : 79;
        const int mo = 8 * ccc + 4 * half;
        v2u gav, sbv, cbv;
        for (unsigned sp = 0; sp < SPIN_MAX; ++sp) {
          asm volatile("" ::: "memory");
          gav = ld8(XM + (b * 2560 + mo) * 2); sbv = ld8(XM + (b * 2560 + 640 + mo) * 2); cbv = ld8(XM + (b * 2560 + 1280 + mo) * 2);
          if (__all(umax3(umax3(gav.x, gav.y, sbv.x), umax3(sbv.y, cbv.x, cbv.y), 0u) != SENT)) break;
        }
        const v2u xv = *(const GAS v2u*)(p.x + b * DM + mo);
        float xf[4] = {bflo(xv.x), bfhi(xv.x), bflo(xv.y), bfhi(xv.y)}, ga4[4] = {bflo(gav.x), bfhi(gav.x), bflo(gav.y), bfhi(gav.y)};
        cb4[0] = 1.f + bflo(cbv.x); cb4[1] = 1.f + bfhi(cbv.x); cb4[2] = 1.f + bflo(cbv.y); cb4[3] = 1.f + bfhi(cbv.y);
        sb4[0] = bflo(sbv.x); sb4[1] = bfhi(sbv.x); sb4[2] = bflo(sbv.y); sb4[3] = bfhi(sbv.y);
        if (!act_lane) { ga4[0] = ga4[1] = ga4[2] = ga4[3] = 0.f; xf[0] = xf[1] = xf[2] = xf[3] = 0.f; }
        int off = X6 + ((4 * half * 2 + b) * DM + 8 * ccc) * 2;
        int a_st = L_MISC + w * 8;
        unsigned one_lo = 0x00003F80u, one_hi = 0x3F800000u;
        KEEP(one_lo); KEEP(one_hi);
        KEEP4(xf); KEEP4(ga4); KEEP4(cb4); KEEP4(sb4); KEEP(off); KEEP(a_st); KEEP(a_h2w);
        PIN();
        v4u a[4];
        for (unsigned sp = 0; sp < SPIN_MAX; ++sp) {
          asm volatile("" ::: "memory");
#pragma unroll
          for (int k = 0; k < 4; ++k) a[k] = ld16(off + k * 2560);
          if (__all(umax4a(a[3], umax4a(a[2], umax4a(a[1], umax4(a[0])))) != SENT)) break;
        }
        TS(24);
        float acc[8];
#pragma unroll
        for (int d = 0; d < 4; ++d) {
          acc[2 * d] = dot2(a[3][d], one_lo, dot2(a[2][d], one_lo, dot2(a[1][d], one_lo, bflo(a[0][d]))));
          acc[2 * d + 1] = dot2(a[3][d], one_hi, dot2(a[2][d], one_hi, dot2(a[1][d], one_hi, bfhi(a[0][d]))));
        }
#pragma unroll
        for (int e = 0; e < 4; ++e) x1[e] = xf[e] + ga4[e] * rs32(acc[e], acc[e + 4]);
        const float s1 = wave_sum((x1[0] + x1[1]) + (x1[2] + x1[3]));
        const float s2 = wave_sum((x1[0] * x1[0] + x1[1] * x1[1]) + (x1[2] * x1[2] + x1[3] * x1[3]));
        *(LAS f32x2*)(L + a_st) = f32x2{s1, s2};
      }
      v4u fa[3][8], fd[8];
      if (w < 4) { tr8(wa[0], fa[0]); tr8(wa[1], fa[1]); if (w < 2) tr8(wa[2], fa[2]); }
      int a_st2 = L_MISC + b * 24;
      int a_ar = L_RED + ((r16 & 1) * DM + 64 * w + 32 * (r16 >> 3) + 8 * g) * 2;
      int a_rw = L_QIMG + (((w * 2 + (g >> 1)) * 2) * 64 + 8 * (r16 & 7)) * 4;
      KEEP(a_st2); KEEP(a_ar); KEEP(a_rw);
      BAR();
      TS(25);
      if (act_lane) {
        const f32x2 t0 = *(LAS f32x2*)(L + a_st2), t1 = *(LAS f32x2*)(L + a_st2 + 8), t2 = *(LAS f32x2*)(L + a_st2 + 16);
        const float mean = (t0[0] + t1[0] + t2[0]) * (1.f / DM);
        const float var = fmaxf((t0[1] + t1[1] + t2[1]) * (1.f / DM) - mean * mean, 0.f);
        const float rstd = __builtin_amdgcn_rsqf(var + 1e-5f);
        float hv[4];
#pragma unroll
        for (int e = 0; e < 4; ++e) hv[e] = (x1[e] - mean) * rstd * cb4[e] + sb4[e];
        *(LAS v2u*)(L + a_h2w) = v2u{pk2(hv[0], hv[1]), pk2(hv[2], hv[3])};
      }
      BAR();
      TS(26);
      if (w < 4) {
        const v4u a0 = *(LAS v4u*)(L + a_ar), a1 = *(LAS v4u*)(L + a_ar + 512);
        v4u a2 = a0;
        if (w < 2) a2 = *(LAS v4u*)(L + a_ar + 1024);
        f32x4 acc[8];
#pragma unroll
        for (int i = 0; i < 8; ++i) acc[i] = mfma16(a0, fa[0][i], f32x4{0.f, 0.f, 0.f, 0.f});
#pragma unroll
        for (int i = 0; i < 8; ++i) acc[i] = mfma16(a1, fa[1][i], acc[i]);
        if (w < 2) {
#pragma unroll
          for (int i = 0; i < 8; ++i) acc[i] = mfma16(a2, fa[2][i], acc[i]);
        }
        if ((g == 0 && r16 < 8) || (g == 2 && r16 >= 8)) {
#pragma unroll
          for (int b2 = 0; b2 < 2; ++b2) {
            *(LAS f32x4*)(L + a_rw + b2 * 256) = f32x4{acc[0][b2], acc[1][b2], acc[2][b2], acc[3][b2]};
            *(LAS f32x4*)(L + a_rw + b2 * 256 + 16) = f32x4{acc[4][b2], acc[5][b2], acc[6][b2], acc[7][b2]};
          }
        }
      }
      if (w < 5) { tr8(wdn, fd); KEEP8(fd); }
      int a_gr = L_QIMG + ((l >> 5) * 64 + (l & 31)) * 4, a_aw = L_MISC + 512 + l * 2, a_acr = L_MISC + 512 + ((r16 & 1) * 32 + 8 * g) * 2;
      int o_x7 = X7 + (idx * 2 * DM + 128 * w + 8 * r16) * 2;
      KEEP(a_gr); KEEP(a_aw); KEEP(a_acr); KEEP(o_x7);
      BAR();
      TS(27);
      if (w == 0) {
        float gv[8], uv[8];
#pragma unroll
        for (int k = 0; k < 8; ++k) { gv[k] = *(LAS float*)(L + a_gr + k * 512); uv[k] = *(LAS float*)(L + a_gr + k * 512 + 128); }
        const float gs = ((gv[0] + gv[1]) + (gv[2] + gv[3])) + ((gv[4] + gv[5]) + (gv[6] + gv[7]));
        const float us = ((uv[0] + uv[1]) + (uv[2] + uv[3])) + ((uv[4] + uv[5]) + (uv[6] + uv[7]));
        *(LAS u16*)(L + a_aw) = (u16)pk2(silu(gs) * us, 0.f);
      }
      BAR();
      TS(28);
      if (w < 5) {
        const v4u a = *(LAS v4u*)(L + a_acr);
        f32x4 d[8];
#pragma unroll
        for (int i = 0; i < 8; ++i) d[i] = mfma16(a, fd[i], f32x4{0.f, 0.f, 0.f, 0.f});
        if (g == 0) {
#pragma unroll
          for (int b2 = 0; b2 < 2; ++b2)
            st16(v4u{pk2(d[0][b2], d[1][b2]), pk2(d[2][b2], d[3][b2]), pk2(d[4][b2], d[5][b2]), pk2(d[6][b2], d[7][b2])}, o_x7 + b2 * DM * 2);
        }
        TS(29);
      }
    } else if (idx >= 64 && idx < 84) {
      const int n = idx - 64;
      float base0 = 0.f, base1 = 0.f, gb0 = 0.f, gb1 = 0.f;
      if (w == 7 && l < 32) {
        const int b = l >> 4, pr = l & 15, m = 32 * n + 2 * pr;
        unsigned ga = 0, gb = 0, ap[8];
        for (unsigned sp = 0; sp < SPIN_MAX; ++sp) {
          asm volatile("" ::: "memory");
          ga = ld4(XM + (b * 2560 + m) * 2); gb = ld4(XM + (b * 2560 + 1920 + m) * 2);
#pragma unroll
          for (int k = 0; k < 8; ++k) ap[k] = ld4(X6 + ((k * 2 + b) * DM + m) * 2);
          if (__all(umax3(umax3(umax3(ap[0], ap[1], ap[2]), umax3(ap[3], ap[4], ap[5]), umax3(ap[6], ap[7], ga)), gb, 0u) != SENT)) break;
        }
        const unsigned xd = *(const GAS unsigned*)(p.x + b * DM + m);
        float s0 = 0.f, s1 = 0.f;
#pragma unroll
        for (int k = 0; k < 8; ++k) { s0 += bflo(ap[k]); s1 += bfhi(ap[k]); }
        base0 = bflo(xd) + bflo(ga) * s0; base1 = bfhi(xd) + bfhi(ga) * s1; gb0 = bflo(gb); gb1 = bfhi(gb);
        KEEP(base0); KEEP(base1); KEEP(gb0); KEEP(gb1);
      }
      int a_fr = L_RED + ((l >> 4) * 32 + 2 * (l & 15)) * 4;
      KEEP(a_fr);
      if (w < 7) {
        const int task = 8 * w + (l >> 3), b = (l >> 2) & 1, ch = l & 3;
        const bool valid = task < 54;
        int off = X7 + (((valid ? task : 0) * 2 + b) * DM + 32 * n + 8 * ch) * 2;
        int a_w = L_RED + (w * 64 + b * 32 + 8 * ch + 4 * (l >> 5) + 2 * ((l >> 4) & 1)) * 4;
        unsigned msk = valid ? 0xFFFFFFFFu : 0u;
        KEEP(off); KEEP(a_w); KEEP(msk);
        PIN();
        v4u a;
        for (unsigned sp = 0; sp < SPIN_MAX; ++sp) {
          asm volatile("" ::: "memory");
          a = ld16(off);
          if (__all(umax4(a) != SENT)) break;
        }
        TS(29);
        float v[8];
#pragma unroll
        for (int d = 0; d < 4; ++d) { const unsigned x = a[d] & msk; v[2 * d] = bflo(x); v[2 * d + 1] = bfhi(x); }
        float t0 = rs16(rs32(v[0], v[4]), rs32(v[2], v[6])), t1 = rs16(rs32(v[1], v[5]), rs32(v[3], v[7]));
        t0 += dppf<0x128>(t0); t1 += dppf<0x128>(t1);
        *(LAS f32x2*)(L + a_w) = f32x2{t0, t1};
      }
      BAR();
      if (w == 7 && l < 32) {
        f32x2 v[7];
#pragma unroll
        for (int k = 0; k < 7; ++k) v[k] = *(LAS f32x2*)(L + a_fr + k * 256);
        const float s0 = ((v[0][0] + v[1][0]) + (v[2][0] + v[3][0])) + ((v[4][0] + v[5][0]) + v[6][0]);
        const float s1 = ((v[0][1] + v[1][1]) + (v[2][1] + v[3][1])) + ((v[4][1] + v[5][1]) + v[6][1]);
        *(GAS unsigned*)(p.out + (l >> 4) * DM + 32 * n + 2 * (l & 15)) = pk2(base0 + gb0 * s0, base1 + gb1 * s1);
        if (n == 0 && l == 0) st4(TAG(), OFF_EPOCH);
      }
    }
  }
done: ;
}

extern "C" void kernel_launch(void* const* d_in, const int* in_sizes, int n_in, void* d_out, int out_size, void* d_ws, size_t ws_size, hipStream_t stream) {
  Params p;
  p.x = (const u16*)d_in[0]; p.ckv = (const u16*)d_in[1]; p.wq = (const u16*)d_in[2]; p.wuk = (const u16*)d_in[3]; p.wuv = (const u16*)d_in[4]; p.wo = (const u16*)d_in[5];
  p.wg = (const u16*)d_in[7]; p.wu = (const u16*)d_in[8]; p.wd = (const u16*)d_in[9]; p.c = (const u16*)d_in[10]; p.wmod = (const u16*)d_in[11];
  p.out = (u16*)d_out; p.ws = (unsigned char*)d_ws;
  mega<<<256, 512, 0, stream>>>(p);
}
```

```cpp
#include <hip/hip_runtime.h>
#include <stdint.h>

#define GAS __attribute__((address_space(1)))
#define LAS __attribute__((address_space(3)))
typedef unsigned v4u __attribute__((ext_vector_type(4)));
typedef unsigned v2u __attribute__((ext_vector_type(2)));
typedef float f32x4 __attribute__((ext_vector_type(4)));
typedef float f32x2 __attribute__((ext_vector_type(2)));
typedef __bf16 bf16x8 __attribute__((ext_vector_type(8)));
typedef __bf16 bf16x2 __attribute__((ext_vector_type(2)));
typedef short s16x4 __attribute__((ext_vector_type(4)));
typedef unsigned short u16;
typedef unsigned long long u64;

constexpr int NB = 2, NH = 32, SK = 8192, DH = 128, DM = 640, DC = 512, DF = 1728;
constexpr int OFF_EPOCH = 0;
constexpr int OFF_FLAGS = 4096;
constexpr int OFF_EFLAGS = OFF_FLAGS + 256 * 128;
constexpr int OFF_X1 = OFF_EFLAGS + 128 * 128;
constexpr int OFF_R = 81920;
constexpr int X2 = OFF_R;
constexpr int X3 = X2 + 65536;
constexpr int X4 = X3 + 65536;
constexpr int X4S = X4 + 4194304;
constexpr int X5 = X4S + 32768;
constexpr int X6 = X5 + 65536;
constexpr int X7 = X6 + 20480;
constexpr int XM = X7 + 138240;
constexpr int R_END = XM + 10240;
static_assert(OFF_X1 + 20480 <= OFF_R, "map");
constexpr int WS_USED = R_END;
#define CKV_A 0
#define CKV_B 6
constexpr unsigned SENT = 0xFFFFFFFFu;
constexpr int AUX_SC1 = 16;
constexpr unsigned SPIN_MAX = 1u << 18;
constexpr int L_CKV = 0;
constexpr int L_QIMG = 131072;
constexpr int L_PIMG = L_QIMG + 16384;
constexpr int L_RED = L_PIMG + 4096;
constexpr int L_MISC = L_RED + 8192;
constexpr int LDS_BYTES = L_MISC + 2048;
static_assert(LDS_BYTES <= 163840, "LDS");

#define TS(k) do { } while (0)

struct Params {
  const u16 *x, *ckv, *wq, *wuk, *wuv, *wo, *wg, *wu, *wd, *c, *wmod;
  u16* out;
  unsigned char* ws;
};

#define BAR() do { asm volatile("s_waitcnt lgkmcnt(0)" ::: "memory"); __builtin_amdgcn_s_barrier(); asm volatile("" ::: "memory"); } while (0)
#define DRAIN() asm volatile("s_waitcnt vmcnt(0)" ::: "memory")
#define PIN() __builtin_amdgcn_sched_barrier(0)
#define KEEP(x) asm volatile("" : "+v"(x))
#define KEEP4(a) do { KEEP(a[0]); KEEP(a[1]); KEEP(a[2]); KEEP(a[3]); } while (0)
#define KEEP8(a) do { KEEP(a[0]); KEEP(a[1]); KEEP(a[2]); KEEP(a[3]); KEEP(a[4]); KEEP(a[5]); KEEP(a[6]); KEEP(a[7]); } while (0)

__device__ __forceinline__ float bflo(unsigned d) { return __uint_as_float(d << 16); }
__device__ __forceinline__ float bfhi(unsigned d) { return __uint_as_float(d & 0xffff0000u); }
__device__ __forceinline__ unsigned pk2(float lo, float hi) { f32x2 f = {lo, hi}; return __builtin_bit_cast(unsigned, __builtin_convertvector(f, bf16x2)); }
__device__ __forceinline__ f32x4 mfma16(v4u a, v4u b, f32x4 c) { return __builtin_amdgcn_mfma_f32_16x16x32_bf16(__builtin_bit_cast(bf16x8, a), __builtin_bit_cast(bf16x8, b), c, 0, 0, 0); }
__device__ __forceinline__ float dot2(unsigned a, unsigned b, float c) { return __builtin_amdgcn_fdot2_f32_bf16(__builtin_bit_cast(bf16x2, a), __builtin_bit_cast(bf16x2, b), c, false); }
__device__ __forceinline__ v4u ldnt(const u16* p) { return __builtin_nontemporal_load((const GAS v4u*)p); }
__device__ __forceinline__ unsigned umax3(unsigned a, unsigned b, unsigned c) { unsigned t = a > b ? a : b; return t > c ? t : c; }
__device__ __forceinline__ unsigned umax4(v4u v) { return umax3(umax3(v.x, v.y, v.z), v.w, 0u); }
__device__ __forceinline__ unsigned umax4a(v4u v, unsigned m) { return umax3(umax3(v.x, v.y, v.z), v.w, m); }
__device__ __forceinline__ float x16_max(float x) {
  auto s = __builtin_amdgcn_permlane16_swap(__float_as_uint(x), __float_as_uint(x), false, false);
  x = fmaxf(__uint_as_float(s[0]), __uint_as_float(s[1]));
  auto t = __builtin_amdgcn_permlane32_swap(__float_as_uint(x), __float_as_uint(x), false, false);
  return fmaxf(__uint_as_float(t[0]), __uint_as_float(t[1]));
}
__device__ __forceinline__ float x16_sum(float x) {
  auto s = __builtin_amdgcn_permlane16_swap(__float_as_uint(x), __float_as_uint(x), false, false);
  x = __uint_as_float(s[0]) + __uint_as_float(s[1]);
  auto t = __builtin_amdgcn_permlane32_swap(__float_as_uint(x), __float_as_uint(x), false, false);
  return __uint_as_float(t[0]) + __uint_as_float(t[1]);
}
__device__ __forceinline__ float rs32(float a, float b) { auto t = __builtin_amdgcn_permlane32_swap(__float_as_uint(a), __float_as_uint(b), false, false); return __uint_as_float(t[0]) + __uint_as_float(t[1]); }
__device__ __forceinline__ float rs16(float a, float b) { auto t = __builtin_amdgcn_permlane16_swap(__float_as_uint(a), __float_as_uint(b), false, false); return __uint_as_float(t[0]) + __uint_as_float(t[1]); }
template <int CTRL> __device__ __forceinline__ float dppf(float x) { return __builtin_bit_cast(float, __builtin_amdgcn_mov_dpp(__builtin_bit_cast(int, x), CTRL, 0xf, 0xf, true)); }
__device__ __forceinline__ float row16_sum(float x) { x += dppf<0xB1>(x); x += dppf<0x4E>(x); x += dppf<0x141>(x); x += dppf<0x128>(x); return x; }
__device__ __forceinline__ float wave_sum(float x) { return x16_sum(row16_sum(x)); }
__device__ __forceinline__ float silu(float v) { return v * __builtin_amdgcn_rcpf(1.f + __builtin_amdgcn_exp2f(v * -1.4426950408889634f)); }

__device__ __forceinline__ void tr8(const v4u (&w)[8], v4u (&f)[8]) {
#pragma unroll
  for (int d = 0; d < 4; ++d) {
#pragma unroll
    for (int i2 = 0; i2 < 4; ++i2) {
      f[2 * i2][d] = __builtin_amdgcn_perm(w[2 * d + 1][i2], w[2 * d][i2], 0x05040100u);
      f[2 * i2 + 1][d] = __builtin_amdgcn_perm(w[2 * d + 1][i2], w[2 * d][i2], 0x07060302u);
    }
  }
}
__device__ __forceinline__ v4u silu8(v4u c) {
  v4u r;
#pragma unroll
  for (int d = 0; d < 4; ++d) r[d] = pk2(silu(bflo(c[d])), silu(bfhi(c[d])));
  return r;
}
__device__ __forceinline__ float sum5(float a, float b, float c, float d, float e) { return ((a + b) + (c + d)) + e; }

__global__ void __launch_bounds__(512, 2) mega(Params p) {
  __shared__ __attribute__((aligned(16))) unsigned char smem[LDS_BYTES];
  LAS unsigned char* L = (LAS unsigned char*)smem;
  const int tid = threadIdx.x, l = tid & 63, w = __builtin_amdgcn_readfirstlane(tid >> 6);
  const int bid = blockIdx.x;
  const int g = l >> 4, r16 = l & 15;
  TS(0);
  const __amdgpu_buffer_rsrc_t rs = __builtin_amdgcn_make_buffer_rsrc((void*)p.ws, 0, WS_USED, 0x00020000);
  const bool Ltype = bid >= 128;
  const int idx = bid & 127;
  const int ab = idx >> 6, aj = idx & 63, hg = bid >> 7;
  const unsigned epoch_raw = __builtin_amdgcn_raw_buffer_load_b32(rs, OFF_EPOCH, 0, AUX_SC1);
  auto TAG = [&]() { unsigned t = epoch_raw; asm volatile("" : "+v"(t)); return t + 1u; };

  auto ld16 = [&](int off) { return __builtin_amdgcn_raw_buffer_load_b128(rs, off, 0, AUX_SC1); };
  auto ld8 = [&](int off) { return __builtin_amdgcn_raw_buffer_load_b64(rs, off, 0, AUX_SC1); };
  auto ld4 = [&](int off) { return __builtin_amdgcn_raw_buffer_load_b32(rs, off, 0, AUX_SC1); };
  auto st16 = [&](v4u v, int off) { __builtin_amdgcn_raw_buffer_store_b128(v, rs, off, 0, AUX_SC1); };
  auto st8 = [&](v2u v, int off) { __builtin_amdgcn_raw_buffer_store_b64(v, rs, off, 0, AUX_SC1); };
  auto st4 = [&](unsigned v, int off) { __builtin_amdgcn_raw_buffer_store_b32(v, rs, off, 0, AUX_SC1); };
  auto dma_ckv = [&](int row0, int nrows) {
    const u16* base = p.ckv + ((size_t)ab * SK + aj * 128) * DC;
    for (int rr = 0; rr < nrows; ++rr) {
      const int row = row0 + rr;
      const int sw = ((row & 3) << 2) | ((row >> 2) & 3);
      const u16* src = base + row * DC + ((l ^ sw) * 8);
      unsigned keep; const unsigned dst = (unsigned)(L_CKV + row * 1024) + (unsigned)(size_t)L;
      asm volatile("s_mov_b32 %0, m0\n\ts_mov_b32 m0, %2\n\ts_nop 0\n\tglobal_load_lds_dwordx4 %1, off nt\n\ts_mov_b32 m0, %0" : "=&s"(keep) : "v"(src), "s"(dst) : "memory");
    }
  };
  const v4u SENT4 = {SENT, SENT, SENT, SENT};
  auto fill_x4 = [&]() {
#pragma unroll
    for (int k = 0; k < 2; ++k) st16(SENT4, X4 + ((ab * NH + 16 * hg + 2 * w + k) * 64 + aj) * 1024 + l * 16);
    if (w == 0 && l < 8) st16(SENT4, X4S + ((ab * 64 + aj) * NH + 16 * hg) * 8 + l * 16);
  };
  auto check_flags = [&](int off, int nper) {
    const unsigned tag = TAG();
    for (unsigned sp = 0; sp < SPIN_MAX; ++sp) {
      asm volatile("" ::: "memory");
      bool ok = true;
      for (int k = 0; k < nper; ++k) ok &= ld4(off + (k * 64 + l) * 128) == tag;
      if (__all(ok)) break;
      __builtin_amdgcn_s_sleep(1);
    }
  };

  auto attention = [&](auto&& hook_post) {
    DRAIN();
    BAR();
    TS(11);
    if (w == 7 && l == 0) st4(TAG(), OFF_FLAGS + bid * 128);
    v4u qk[16], pv[16];
    {
      const int key = 16 * w + r16;
      const int sw = ((key & 3) << 2) | ((key >> 2) & 3);
#pragma unroll
      for (int s = 0; s < 16; ++s) qk[s] = *(LAS v4u*)(L + L_CKV + key * 1024 + (s >> 2) * 256 + (((4 * (s & 3) + g) ^ sw) * 16));
      const int q4 = r16 >> 2, p4 = l & 3;
#pragma unroll
      for (int s = 0; s < 4; ++s)
#pragma unroll
        for (int t = 0; t < 4; ++t) {
          v2u h2[2];
#pragma unroll
          for (int hf = 0; hf < 2; ++hf) {
            const int ky = 32 * s + 16 * hf + 4 * g + q4;
            const int sw2 = (q4 << 2) | g;
            const int ch = 8 * w + 4 * (t >> 1) + p4;
            const s16x4 v = __builtin_amdgcn_ds_read_tr16_b64_v4i16((LAS s16x4*)(L + L_CKV + ky * 1024 + ((ch ^ sw2) * 16) + 8 * (t & 1)));
            h2[hf] = __builtin_bit_cast(v2u, v);
          }
          pv[s * 4 + t] = v4u{h2[0].x, h2[0].y, h2[1].x, h2[1].y};
        }
    }
    int a_qw0 = L_QIMG + w * 1024 + ((l ^ w) * 16), a_qw1 = L_QIMG + (8 + w) * 1024 + ((l ^ (8 + w)) * 16);
    int a_qr[4];
#pragma unroll
    for (int t = 0; t < 4; ++t) a_qr[t] = L_QIMG + r16 * 1024 + (((4 * t + g) ^ r16) * 16);
    int a_mxw = L_MISC + r16 * 32 + w * 4, a_mxr = L_MISC + r16 * 32;
    int a_pw = L_PIMG + (w >> 1) * 1024 + l * 16 + (w & 1) * 8, a_pr = L_PIMG + l * 16;
    const int hd = 16 * hg + r16;
    int o_st = X4 + (((ab * NH + hd) * 64 + aj) * 512 + 64 * w + 8 * g) * 2, o_ss = X4S + ((ab * 64 + aj) * NH + hd) * 8;
    int o_q = X3 + (ab * NH + 16 * hg) * 1024 + tid * 16;
    KEEP(a_qw0); KEEP(a_qw1); KEEP4(a_qr); KEEP(a_mxw); KEEP(a_mxr); KEEP(a_pw); KEEP(a_pr); KEEP(o_st); KEEP(o_ss); KEEP(o_q);
#pragma unroll
    for (int s = 0; s < 16; ++s) { KEEP(qk[s]); KEEP(pv[s]); }
    TS(12);
    PIN();
    {
      v4u a0, a1;
      for (unsigned sp = 0; sp < SPIN_MAX; ++sp) {
        asm volatile("" ::: "memory");
        a0 = ld16(o_q); a1 = ld16(o_q + 8192);
        if (__all(umax4a(a1, umax4(a0)) != SENT)) break;
      }
      *(LAS v4u*)(L + a_qw0) = a0; *(LAS v4u*)(L + a_qw1) = a1;
    }
    TS(13);
    unsigned lf[4] = {0u, 0u, 0u, 0u};
    if (w == 6) {
#pragma unroll
      for (int k = 0; k < 4; ++k) lf[k] = ld4(OFF_FLAGS + (k * 64 + l) * 128);
    }
    PIN();
    BAR();
    TS(14);
    f32x4 sacc = {0.f, 0.f, 0.f, 0.f}, sacc2 = {0.f, 0.f, 0.f, 0.f};
    {
      v4u qf[16];
#pragma unroll
      for (int s = 0; s < 16; ++s) qf[s] = *(LAS v4u*)(L + a_qr[s & 3] + (s >> 2) * 256);
      PIN();
#pragma unroll
      for (int s = 0; s < 16; s += 2) { sacc = mfma16(qk[s], qf[s], sacc); sacc2 = mfma16(qk[s + 1], qf[s + 1], sacc2); }
    }
    sacc += sacc2;
    const float mloc = x16_max(fmaxf(fmaxf(sacc[0], sacc[1]), fmaxf(sacc[2], sacc[3])));
    *(LAS float*)(L + a_mxw) = mloc;
    TS(15);
    BAR();
    TS(16);
    const f32x4 m0 = *(LAS f32x4*)(L + a_mxr), m1 = *(LAS f32x4*)(L + a_mxr + 16);
    const float m = fmaxf(fmaxf(fmaxf(m0[0], m0[1]), fmaxf(m0[2], m0[3])), fmaxf(fmaxf(m1[0], m1[1]), fmaxf(m1[2], m1[3])));
    const unsigned d0 = pk2(__builtin_amdgcn_exp2f(sacc[0] - m), __builtin_amdgcn_exp2f(sacc[1] - m)), d1 = pk2(__builtin_amdgcn_exp2f(sacc[2] - m), __builtin_amdgcn_exp2f(sacc[3] - m));
    *(LAS v2u*)(L + a_pw) = v2u{d0, d1};
    BAR();
    TS(17);
    f32x4 o[4], lacc = {0.f, 0.f, 0.f, 0.f};
#pragma unroll
    for (int t = 0; t < 4; ++t) o[t] = f32x4{0.f, 0.f, 0.f, 0.f};
    v4u pf[4];
#pragma unroll
    for (int s = 0; s < 4; ++s) pf[s] = *(LAS v4u*)(L + a_pr + s * 1024);
    PIN();
#pragma unroll
    for (int s = 0; s < 4; ++s)
#pragma unroll
      for (int t = 0; t < 4; ++t) o[t] = mfma16(pv[s * 4 + t], pf[s], o[t]);
    st16(v4u{pk2(o[0][0], o[0][1]), pk2(o[0][2], o[0][3]), pk2(o[1][0], o[1][1]), pk2(o[1][2], o[1][3])}, o_st);
    st16(v4u{pk2(o[2][0], o[2][1]), pk2(o[2][2], o[2][3]), pk2(o[3][0], o[3][1]), pk2(o[3][2], o[3][3])}, o_st + 64);
    if (w == 0) {
      const v4u ones = {0x3F803F80u, 0x3F803F80u, 0x3F803F80u, 0x3F803F80u};
#pragma unroll
      for (int s = 0; s < 4; ++s) lacc = mfma16(ones, pf[s], lacc);
      if (g == 0) st8(v2u{__float_as_uint(m), __float_as_uint(lacc[0])}, o_ss);
    }
    TS(18);
    PIN();
    hook_post();
    PIN();
    if (w == 6) {
      const unsigned tag = TAG();
      if (!__all((lf[0] == tag) & (lf[1] == tag) & (lf[2] == tag) & (lf[3] == tag))) check_flags(OFF_FLAGS, 4);
    }
    BAR();
    TS(19);
  };

  if (Ltype) {
    const int hh = idx >> 2, sub = idx & 3;
    const bool t1role = idx < 40;
    const int mg = idx % 10, pp1 = idx / 10;
    v4u uk[4];
    auto load_t3 = [&]() {
      const u16* src = p.wuk + ((size_t)(128 * sub + 16 * w + r16) * NH + hh) * DH + 8 * g;
#pragma unroll
      for (int s = 0; s < 4; ++s) uk[s] = ldnt(src + 32 * s);
    };
    auto stage_t3 = [&]() {
      if (CKV_B > 0) dma_ckv(16 * w, CKV_B);
      const int kq = (r16 & 7) >> 1, b = r16 & 1;
      int off = X2 + ((hh * 4 + kq) * 2 + b) * 256 + 16 * g;
      int a_w = L_RED + ((g & 1) * 128 + 16 * w + r16) * 2;
      int o_x3 = X3 + ((l >> 4) * NH + hh) * 1024 + sub * 256 + (l & 15) * 16;
      KEEP(off); KEEP(a_w); KEEP(o_x3);
      PIN();
      v4u a[4];
      for (unsigned sp = 0; sp < SPIN_MAX; ++sp) {
        asm volatile("" ::: "memory");
#pragma unroll
        for (int s = 0; s < 4; ++s) a[s] = ld16(off + 64 * s);
        if (__all(umax4a(a[3], umax4a(a[2], umax4a(a[1], umax4(a[0])))) != SENT)) break;
      }
      TS(9);
      f32x4 acc = mfma16(a[0], uk[0], f32x4{0.f, 0.f, 0.f, 0.f}), acc2 = mfma16(a[1], uk[1], f32x4{0.f, 0.f, 0.f, 0.f});
      acc = mfma16(a[2], uk[2], acc); acc2 = mfma16(a[3], uk[3], acc2);
      acc += acc2;
      const float sc = 0.08838834764831845f * 1.4426950408889634f;
      const float tot = rs16(acc[0] + acc[2], acc[1] + acc[3]) * sc;
      if (g < 2) *(LAS u16*)(L + a_w) = (u16)pk2(tot, tot);
      BAR();
      if (w == 0 && l < 32) {
        st16(*(LAS v4u*)(L + L_RED + l * 16), o_x3);
        TS(10);
      }
    };
    if (w < 5) {
      v4u wv2[8];
      const u16* src2 = p.wq + (size_t)(160 * sub + 32 * w + 8 * g) * (NH * DH) + hh * DH + 8 * r16;
      if (t1role) {
        v4u wv[8];
        const int col = (r16 < 8) ? (64 * mg + 8 * r16) : (DM + 64 * mg + 8 * (r16 - 8));
        const int k0 = 160 * pp1 + 32 * w + 8 * g;
        const v4u cv = *(const GAS v4u*)(p.c + (r16 & 1) * DM + k0);
        const unsigned xd = *(const GAS unsigned*)(p.x + (l >> 5) * DM + 64 * mg + 2 * (l & 31));
        const u16* src = p.wmod + (size_t)k0 * (6 * DM) + col;
#pragma unroll
        for (int j = 0; j < 8; ++j) wv[j] = ldnt(src + (size_t)j * (6 * DM));
        int a_w = L_RED + ((w * 2) * 128 + 8 * r16) * 4;
        int a_r = L_RED + ((l >> 5) * 128 + 2 * (l & 31)) * 4;
        int o_x1 = OFF_X1 + (((pp1 * 2 + (l >> 5)) * 320) + 32 * mg + (l & 31)) * 8;
        KEEP(a_w); KEEP(a_r); KEEP(o_x1);
        PIN();
        v4u a = silu8(cv);
        float xlo = bflo(xd), xhi = bfhi(xd);
        unsigned tag = TAG();
        KEEP(a); KEEP(xlo); KEEP(xhi); KEEP(tag);
        PIN();
        v4u f[8]; tr8(wv, f);
        KEEP8(f);
        TS(1);
        PIN();
#pragma unroll
        for (int j = 0; j < 8; ++j) wv2[j] = ldnt(src2 + (size_t)j * (NH * DH));
        PIN();
        f32x4 acc[8];
#pragma unroll
        for (int i = 0; i < 8; ++i) acc[i] = mfma16(a, f[i], f32x4{0.f, 0.f, 0.f, 0.f});
        if (g == 0) {
#pragma unroll
          for (int b = 0; b < 2; ++b) {
            *(LAS f32x4*)(L + a_w + b * 512) = f32x4{acc[0][b], acc[1][b], acc[2][b], acc[3][b]};
            *(LAS f32x4*)(L + a_w + b * 512 + 16) = f32x4{acc[4][b], acc[5][b], acc[6][b], acc[7][b]};
          }
        }
        TS(2);
        BAR();
        TS(3);
        if (w == 0) {
          f32x2 s[5], c2[5];
#pragma unroll
          for (int ww = 0; ww < 5; ++ww) { s[ww] = *(LAS f32x2*)(L + a_r + ww * 1024); c2[ww] = *(LAS f32x2*)(L + a_r + ww * 1024 + 256); }
          const f32x2 mr = *(LAS f32x2*)(L + L_MISC + (l >> 5) * 8);
          const float xn0 = (xlo - mr[0]) * mr[1], xn1 = (xhi - mr[0]) * mr[1];
          const float one = pp1 == 0 ? 1.f : 0.f;
          const float o0 = xn0 * (sum5(c2[0][0], c2[1][0], c2[2][0], c2[3][0], c2[4][0]) + one) + sum5(s[0][0], s[1][0], s[2][0], s[3][0], s[4][0]);
          const float o1 = xn1 * (sum5(c2[0][1], c2[1][1], c2[2][1], c2[3][1], c2[4][1]) + one) + sum5(s[0][1], s[1][1], s[2][1], s[3][1], s[4][1]);
          st8(v2u{pk2(o0, o1), tag}, o_x1);
          TS(4);
        }
      } else {
#pragma unroll
        for (int j = 0; j < 8; ++j) wv2[j] = ldnt(src2 + (size_t)j * (NH * DH));
      }
      load_t3();
      PIN();
      {
        unsigned tag = TAG();
        v4u f[8]; tr8(wv2, f);
        const int pp = (r16 & 7) >> 1, b = r16 & 1;
        int off = OFF_X1 + ((pp * 2 + b) * 320 + (160 * sub + 32 * w + 8 * g) / 2) * 8;
        int a_w = L_QIMG + (((w * 2 + (g & 1)) * 2) * 128 + 8 * r16) * 4;
        int a_r = L_QIMG + ((w & 1) * 128 + 2 * l) * 4, o_x2 = X2 + ((hh * 4 + sub) * 2 + (w & 1)) * 256 + l * 4;
        KEEP8(f); KEEP(off); KEEP(a_w); KEEP(tag); KEEP(a_r); KEEP(o_x2);
        TS(5);
        PIN();
        v4u a;
        for (unsigned sp = 0; sp < SPIN_MAX; ++sp) {
          asm volatile("" ::: "memory");
          const v4u u0 = ld16(off), u1 = ld16(off + 16);
          a = v4u{u0.x, u0.z, u1.x, u1.z};
          if (__all(((u0.y ^ tag) | (u0.w ^ tag) | (u1.y ^ tag) | (u1.w ^ tag)) == 0u)) break;
        }
        TS(6);
        f32x4 acc[8];
#pragma unroll
        for (int i = 0; i < 8; ++i) acc[i] = mfma16(a, f[i], f32x4{0.f, 0.f, 0.f, 0.f});
        if (g < 2) {
#pragma unroll
          for (int b2 = 0; b2 < 2; ++b2) {
            *(LAS f32x4*)(L + a_w + b2 * 512) = f32x4{acc[0][b2] + acc[0][b2 + 2], acc[1][b2] + acc[1][b2 + 2], acc[2][b2] + acc[2][b2 + 2], acc[3][b2] + acc[3][b2 + 2]};
            *(LAS f32x4*)(L + a_w + b2 * 512 + 16) = f32x4{acc[4][b2] + acc[4][b2 + 2], acc[5][b2] + acc[5][b2 + 2], acc[6][b2] + acc[6][b2 + 2], acc[7][b2] + acc[7][b2 + 2]};
          }
        }
        BAR();
        TS(7);
        if (w < 2) {
          f32x2 v[10];
#pragma unroll
          for (int k = 0; k < 10; ++k) v[k] = *(LAS f32x2*)(L + a_r + k * 1024);
          const float s0 = sum5(v[0][0], v[1][0], v[2][0], v[3][0], v[4][0]) + sum5(v[5][0], v[6][0], v[7][0], v[8][0], v[9][0]);
          const float s1 = sum5(v[0][1], v[1][1], v[2][1], v[3][1], v[4][1]) + sum5(v[5][1], v[6][1], v[7][1], v[8][1], v[9][1]);
          st4(pk2(s0, s1), o_x2);
          TS(8);
        }
      }
      stage_t3();
    } else {
      if (w == 7) {
        if (l < 32) st16(SENT4, X2 + (hh * 4 + sub) * 512 + l * 16);
        else st16(SENT4, X3 + (((l - 32) >> 4) * NH + hh) * 1024 + sub * 256 + (l & 15) * 16);
        DRAIN();
        if (l == 0) st4(TAG(), OFF_EFLAGS + idx * 128);
      } else if (t1role) {
        const int b = w - 5;
        float v[10]; float s = 0.f;
#pragma unroll
        for (int k = 0; k < 5; ++k) { const unsigned d = *(const GAS unsigned*)(p.x + b * DM + 2 * (l + 64 * k)); v[2 * k] = bflo(d); v[2 * k + 1] = bfhi(d); s += v[2 * k] + v[2 * k + 1]; }
        const float mean = wave_sum(s) * (1.f / DM);
        float q = 0.f;
#pragma unroll
        for (int k = 0; k < 10; ++k) { const float d = v[k] - mean; q += d * d; }
        const float var = wave_sum(q) * (1.f / DM);
        *(LAS f32x2*)(L + L_MISC + b * 8) = f32x2{mean, __builtin_amdgcn_rsqf(var + 1e-5f)};
      }
      if (t1role) BAR();
      {
        const int h0 = (w - 5) * 6, nh = w == 7 ? 4 : 6;
        for (int k = 0; k < nh; ++k) st16(SENT4, X4 + ((ab * NH + 16 * hg + h0 + k) * 64 + aj) * 1024 + l * 16);
        if (w == 7) {
          if (l < 8) st16(SENT4, X4S + ((ab * 64 + aj) * NH + 16 * hg) * 8 + l * 16);
          else if (l >= 16 && l < 32) {
            const int u = l - 16;
            if (idx < 80) st16(SENT4, X6 + (((idx / 10) * 2 + (u >> 3)) * DM + 64 * (idx % 10)) * 2 + (u & 7) * 16);
            else if (idx < 120) st16(SENT4, XM + ((u >> 3) * 2560 + 64 * (idx - 80)) * 2 + (u & 7) * 16);
          }
        }
      }
      load_t3();
      if (w == 6) check_flags(OFF_EFLAGS, 2);
      BAR();
      stage_t3();
    }
    dma_ckv(16 * w + CKV_B, 16 - CKV_B);
    v4u wpre[3][8];
    attention([&]() {
      if (w < 4) {
        const int kk = (l >> 3) & 1, ch = l & 7;
        if (idx < 80) {
          const int pp = idx / 10, n = idx % 10;
          const u16* src = p.wo + (size_t)(512 * pp + 64 * w + 32 * kk + 8 * g) * DM + 64 * n + 8 * ch;
#pragma unroll
          for (int k = 0; k < 2; ++k)
#pragma unroll
            for (int j = 0; j < 8; ++j) wpre[k][j] = ldnt(src + (size_t)(256 * k + j) * DM);
        } else if (idx < 120) {
          const int tb = idx - 80;
          const u16* src = p.wmod + (size_t)(64 * w + 32 * kk + 8 * g) * (6 * DM) + 2 * DM + 64 * tb + 8 * ch;
#pragma unroll
          for (int k = 0; k < 2; ++k)
#pragma unroll
            for (int j = 0; j < 8; ++j) wpre[k][j] = ldnt(src + (size_t)(256 * k + j) * (6 * DM));
          if (w < 2) {
#pragma unroll
            for (int j = 0; j < 8; ++j) wpre[2][j] = ldnt(src + (size_t)(512 + j) * (6 * DM));
          }
        }
      }
    });
    LAS float* red = (LAS float*)(L + L_RED);
    if (idx < 80) {
      const int pp = idx / 10, n = idx % 10;
      if (w < 4) {
        v4u f0[8], f1[8]; tr8(wpre[0], f0); tr8(wpre[1], f1);
        const int rho = r16 & 7, cq = rho >> 1, b = rho & 1, kk2 = r16 >> 3;
        int off = X5 + (((4 * pp + (w >> 1)) * 4 + cq) * 2 + b) * 256 + (64 * (w & 1) + 32 * kk2 + 8 * g) * 2;
        int a_w = L_RED + (((w * 2 + (g >> 1)) * 2 + (g & 1)) * 64 + 8 * (r16 & 7)) * 4;
        KEEP8(f0); KEEP8(f1); KEEP(off); KEEP(a_w);
        PIN();
        v4u a0, a1;
        for (unsigned sp = 0; sp < SPIN_MAX; ++sp) {
          asm volatile("" ::: "memory");
          a0 = ld16(off); a1 = ld16(off + 2 * 4 * 2 * 256);
          if (__all(umax4a(a1, umax4(a0)) != SENT)) break;
        }
        TS(24);
        f32x4 d[8];
#pragma unroll
        for (int i = 0; i < 8; ++i) d[i] = mfma16(a0, f0[i], f32x4{0.f, 0.f, 0.f, 0.f});
#pragma unroll
        for (int i = 0; i < 8; ++i) d[i] = mfma16(a1, f1[i], d[i]);
        float s[8];
#pragma unroll
        for (int i = 0; i < 8; ++i) s[i] = rs16(d[i][0] + d[i][2], d[i][1] + d[i][3]);
        if ((g >> 1) == (r16 >> 3)) {
          *(LAS f32x4*)(L + a_w) = f32x4{s[0], s[1], s[2], s[3]}; *(LAS f32x4*)(L + a_w + 16) = f32x4{s[4], s[5], s[6], s[7]};
        }
      }
      int a_r = L_RED + ((l >> 5) * 64 + 2 * (l & 31)) * 4, o_x6 = X6 + ((pp * 2 + (l >> 5)) * DM + 64 * n + 2 * (l & 31)) * 2;
      KEEP(a_r); KEEP(o_x6);
      BAR();
      TS(25);
      if (w == 0) {
        f32x2 v[8];
#pragma unroll
        for (int k = 0; k < 8; ++k) v[k] = *(LAS f32x2*)(L + a_r + k * 512);
        const float s0 = ((v[0][0] + v[1][0]) + (v[2][0] + v[3][0])) + ((v[4][0] + v[5][0]) + (v[6][0] + v[7][0]));
        const float s1 = ((v[0][1] + v[1][1]) + (v[2][1] + v[3][1])) + ((v[4][1] + v[5][1]) + (v[6][1] + v[7][1]));
        st4(pk2(s0, s1), o_x6);
        TS(29);
      }
    } else if (idx < 120) {
      const int tb = idx - 80;
      if (w < 4) {
        f32x4 acc[8];
#pragma unroll
        for (int i = 0; i < 8; ++i) acc[i] = f32x4{0.f, 0.f, 0.f, 0.f};
#pragma unroll
        for (int k = 0; k < 3; ++k) {
          if (k < 2 || w < 2) {
            const v4u cv = *(const GAS v4u*)(p.c + (r16 & 1) * DM + 64 * (w + 4 * k) + 32 * (r16 >> 3) + 8 * g);
            const v4u a = silu8(cv);
            v4u f[8];
            tr8(wpre[k], f);
#pragma unroll
            for (int i = 0; i < 8; ++i) acc[i] = mfma16(a, f[i], acc[i]);
          }
        }
        if ((g == 0 && r16 < 8) || (g == 2 && r16 >= 8)) {
#pragma unroll
          for (int b2 = 0; b2 < 2; ++b2) {
            LAS float* d = red + ((w * 2 + (g >> 1)) * 2 + b2) * 64 + 8 * (r16 & 7);
            *(LAS f32x4*)d = f32x4{acc[0][b2], acc[1][b2], acc[2][b2], acc[3][b2]}; *(LAS f32x4*)(d + 4) = f32x4{acc[4][b2], acc[5][b2], acc[6][b2], acc[7][b2]};
          }
        }
      }
      BAR();
      if (w == 0) {
        const int b = l >> 5, pr = l & 31;
        float s0 = 0.f, s1 = 0.f;
#pragma unroll
        for (int k = 0; k < 8; ++k) { const f32x2 v = *(LAS f32x2*)(red + (k * 2 + b) * 64 + 2 * pr); s0 += v[0]; s1 += v[1]; }
        st4(pk2(s0, s1), XM + (b * 2560 + 64 * tb + 2 * pr) * 2);
        TS(29);
      }
    }
  } else {
    fill_x4();
    if (w == 1 && l < 32) st16(SENT4, X5 + idx * 512 + l * 16);
    if (idx < 54 && w >= 2 && w < 5) { const int u = (w - 2) * 64 + l; if (u < 160) st16(SENT4, X7 + idx * 2560 + u * 16); }
    dma_ckv(16 * w, 16);
    if (w == 6) check_flags(OFF_EFLAGS, 2);
    const int h5 = idx >> 2, cq = idx & 3;
    v4u wpre[8];
    attention([&]() {
      if (w < 4) {
        const u16* src = p.wuv + ((size_t)(128 * cq + 32 * w + 8 * g) * NH + h5) * DH + 8 * r16;
#pragma unroll
        for (int j = 0; j < 8; ++j) wpre[j] = ldnt(src + (size_t)j * (NH * DH));
      }
    });
    {
      const int b = w >> 2;
      int off = X4 + (((b * NH + h5) * 64 + 16 * (w & 3) + g) * 512 + 128 * cq + 8 * r16) * 2;
      int offs = X4S + ((b * 64 + 16 * (w & 3) + g) * NH + h5) * 8;
      const int cofs = 8 * r16 + 4 * (l >> 5) + 2 * ((l >> 4) & 1);
      int a_w = L_RED + w * 256 + (((cofs >> 3) ^ w) * 16) + (cofs & 7) * 2;
      int a_s = L_MISC + 1024 + w * 8;
      KEEP(off); KEEP(offs); KEEP(a_w); KEEP(a_s);
      PIN();
      v4u a[4]; v2u sv[4];
      for (unsigned sp = 0; sp < SPIN_MAX; ++sp) {
        asm volatile("" ::: "memory");
#pragma unroll
        for (int k = 0; k < 4; ++k) { sv[k] = ld8(offs + k * 1024); a[k] = ld16(off + k * 4096); }
        unsigned m = umax3(umax3(sv[0].x, sv[0].y, sv[1].x), umax3(sv[1].y, sv[2].x, sv[2].y), umax3(sv[3].x, sv[3].y, 0u));
        m = umax4a(a[1], umax4a(a[0], m)); m = umax4a(a[3], umax4a(a[2], m));
        if (__all(m != SENT)) break;
      }
      TS(20);
      float mw = fmaxf(fmaxf(__uint_as_float(sv[0].x), __uint_as_float(sv[1].x)), fmaxf(__uint_as_float(sv[2].x), __uint_as_float(sv[3].x)));
      mw = x16_max(mw);
      float acc[8] = {0.f, 0.f, 0.f, 0.f, 0.f, 0.f, 0.f, 0.f}; float lw = 0.f;
#pragma unroll
      for (int k = 0; k < 4; ++k) {
        const float e = __builtin_amdgcn_exp2f(__uint_as_float(sv[k].x) - mw);
        const unsigned wlo = pk2(e, 0.f), whi = pk2(0.f, e);
        lw += __uint_as_float(whi) * __uint_as_float(sv[k].y);
#pragma unroll
        for (int d = 0; d < 4; ++d) { acc[2 * d] = dot2(a[k][d], wlo, acc[2 * d]); acc[2 * d + 1] = dot2(a[k][d], whi, acc[2 * d + 1]); }
      }
      const float u0 = rs16(rs32(acc[0], acc[4]), rs32(acc[2], acc[6])), u1 = rs16(rs32(acc[1], acc[5]), rs32(acc[3], acc[7]));
      *(LAS unsigned*)(L + a_w) = pk2(u0, u1);
      lw = x16_sum(lw);
      *(LAS f32x2*)(L + a_s) = f32x2{mw, lw};
    }
    {
      v4u f[8];
      if (w < 4) { tr8(wpre, f); KEEP8(f); }
      const int row = r16 & 7;
      int a_a = L_RED + row * 256 + (((4 * w + g) ^ row) * 16);
      int a_st = L_MISC + 1024 + (g & 1) * 32;
      int a_w2 = L_QIMG + ((w * 2 + (g & 1)) * 128 + 8 * r16) * 4;
      int a_r2 = L_QIMG + ((w & 1) * 128 + 2 * l) * 4, o_x5 = X5 + (idx * 2 + (w & 1)) * 256 + l * 4;
      KEEP(a_a); KEEP(a_st); KEEP(a_w2); KEEP(a_r2); KEEP(o_x5);
      BAR();
      TS(21);
      if (w < 4) {
        const v4u a = *(LAS v4u*)(L + a_a);
        const f32x4 s01 = *(LAS f32x4*)(L + a_st), s23 = *(LAS f32x4*)(L + a_st + 16);
        f32x4 d[8];
#pragma unroll
        for (int i = 0; i < 8; ++i) d[i] = mfma16(a, f[i], f32x4{0.f, 0.f, 0.f, 0.f});
        const float mb = fmaxf(fmaxf(s01[0], s01[2]), fmaxf(s23[0], s23[2]));
        float f0 = __builtin_amdgcn_exp2f(s01[0] - mb), f1 = __builtin_amdgcn_exp2f(s01[2] - mb), f2 = __builtin_amdgcn_exp2f(s23[0] - mb), f3 = __builtin_amdgcn_exp2f(s23[2] - mb);
        const float inv = __builtin_amdgcn_rcpf((f0 * s01[1] + f1 * s01[3]) + (f2 * s23[1] + f3 * s23[3]));
        f0 *= inv; f1 *= inv; f2 *= inv; f3 *= inv;
        float v[8];
#pragma unroll
        for (int i = 0; i < 8; ++i) v[i] = (d[i][0] * f0 + d[i][1] * f1) + (d[i][2] * f2 + d[i][3] * f3);
        if (g < 2) { *(LAS f32x4*)(L + a_w2) = f32x4{v[0], v[1], v[2], v[3]}; *(LAS f32x4*)(L + a_w2 + 16) = f32x4{v[4], v[5], v[6], v[7]}; }
      }
      BAR();
      TS(22);
      if (w < 2) {
        f32x2 v[4];
#pragma unroll
        for (int k = 0; k < 4; ++k) v[k] = *(LAS f32x2*)(L + a_r2 + k * 1024);
        st4(pk2((v[0][0] + v[1][0]) + (v[2][0] + v[3][0]), (v[0][1] + v[1][1]) + (v[2][1] + v[3][1])), o_x5);
        TS(23);
      }
    }
    if (idx < 54) {
      const int kk = (l >> 3) & 1, ch = l & 7;
      v4u wa[3][8], wdn[8];
      if (w < 4) {
        const u16* wsrc = (ch < 4 ? p.wg : p.wu) + 32 * idx + 8 * (ch & 3) + (size_t)(64 * w + 32 * kk + 8 * g) * DF;
#pragma unroll
        for (int k = 0; k < 2; ++k)
#pragma unroll
          for (int j = 0; j < 8; ++j) wa[k][j] = ldnt(wsrc + (size_t)(256 * k + j) * DF);
        if (w < 2) {
#pragma unroll
          for (int j = 0; j < 8; ++j) wa[2][j] = ldnt(wsrc + (size_t)(512 + j) * DF);
        }
      }
      if (w < 5) {
        const u16* src = p.wd + (size_t)(32 * idx + 8 * g) * DM + 128 * w + 8 * r16;
#pragma unroll
        for (int j = 0; j < 8; ++j) wdn[j] = ldnt(src + (size_t)j * DM);
      }
      PIN();
      const int b = w / 3, cc = 32 * (w % 3) + (l & 31), half = l >> 5;
      const bool act_lane = (w < 6) && (cc < 80);
      float x1[4] = {0.f, 0.f, 0.f, 0.f}, cb4[4], sb4[4];
      int a_h2w = L_RED + (b * DM + 8 * cc + 4 * half) * 2;
      if (w < 6) {
        const int ccc = cc < 80 ? cc : 79;
        const int mo = 8 * ccc + 4 * half;
        v2u gav, sbv, cbv;
        for (unsigned sp = 0; sp < SPIN_MAX; ++sp) {
          asm volatile("" ::: "memory");
          gav = ld8(XM + (b * 2560 + mo) * 2); sbv = ld8(XM + (b * 2560 + 640 + mo) * 2); cbv = ld8(XM + (b * 2560 + 1280 + mo) * 2);
          if (__all(umax3(umax3(gav.x, gav.y, sbv.x), umax3(sbv.y, cbv.x, cbv.y), 0u) != SENT)) break;
        }
        const v2u xv = *(const GAS v2u*)(p.x + b * DM + mo);
        float xf[4] = {bflo(xv.x), bfhi(xv.x), bflo(xv.y), bfhi(xv.y)}, ga4[4] = {bflo(gav.x), bfhi(gav.x), bflo(gav.y), bfhi(gav.y)};
        cb4[0] = 1.f + bflo(cbv.x); cb4[1] = 1.f + bfhi(cbv.x); cb4[2] = 1.f + bflo(cbv.y); cb4[3] = 1.f + bfhi(cbv.y);
        sb4[0] = bflo(sbv.x); sb4[1] = bfhi(sbv.x); sb4[2] = bflo(sbv.y); sb4[3] = bfhi(sbv.y);
        if (!act_lane) { ga4[0] = ga4[1] = ga4[2] = ga4[3] = 0.f; xf[0] = xf[1] = xf[2] = xf[3] = 0.f; }
        int off = X6 + ((4 * half * 2 + b) * DM + 8 * ccc) * 2;
        int a_st = L_MISC + w * 8;
        unsigned one_lo = 0x00003F80u, one_hi = 0x3F800000u;
        KEEP(one_lo); KEEP(one_hi);
        KEEP4(xf); KEEP4(ga4); KEEP4(cb4); KEEP4(sb4); KEEP(off); KEEP(a_st); KEEP(a_h2w);
        PIN();
        v4u a[4];
        for (unsigned sp = 0; sp < SPIN_MAX; ++sp) {
          asm volatile("" ::: "memory");
#pragma unroll
          for (int k = 0; k < 4; ++k) a[k] = ld16(off + k * 2560);
          if (__all(umax4a(a[3], umax4a(a[2], umax4a(a[1], umax4(a[0])))) != SENT)) break;
        }
        TS(24);
        float acc[8];
#pragma unroll
        for (int d = 0; d < 4; ++d) {
          acc[2 * d] = dot2(a[3][d], one_lo, dot2(a[2][d], one_lo, dot2(a[1][d], one_lo, bflo(a[0][d]))));
          acc[2 * d + 1] = dot2(a[3][d], one_hi, dot2(a[2][d], one_hi, dot2(a[1][d], one_hi, bfhi(a[0][d]))));
        }
#pragma unroll
        for (int e = 0; e < 4; ++e) x1[e] = xf[e] + ga4[e] * rs32(acc[e], acc[e + 4]);
        const float s1 = wave_sum((x1[0] + x1[1]) + (x1[2] + x1[3]));
        const float s2 = wave_sum((x1[0] * x1[0] + x1[1] * x1[1]) + (x1[2] * x1[2] + x1[3] * x1[3]));
        *(LAS f32x2*)(L + a_st) = f32x2{s1, s2};
      }
      v4u fa[3][8], fd[8];
      if (w < 4) { tr8(wa[0], fa[0]); tr8(wa[1], fa[1]); if (w < 2) tr8(wa[2], fa[2]); }
      int a_st2 = L_MISC + b * 24;
      int a_ar = L_RED + ((r16 & 1) * DM + 64 * w + 32 * (r16 >> 3) + 8 * g) * 2;
      int a_rw = L_QIMG + (((w * 2 + (g >> 1)) * 2) * 64 + 8 * (r16 & 7)) * 4;
      KEEP(a_st2); KEEP(a_ar); KEEP(a_rw);
      BAR();
      TS(25);
      if (act_lane) {
        const f32x2 t0 = *(LAS f32x2*)(L + a_st2), t1 = *(LAS f32x2*)(L + a_st2 + 8), t2 = *(LAS f32x2*)(L + a_st2 + 16);
        const float mean = (t0[0] + t1[0] + t2[0]) * (1.f / DM);
        const float var = fmaxf((t0[1] + t1[1] + t2[1]) * (1.f / DM) - mean * mean, 0.f);
        const float rstd = __builtin_amdgcn_rsqf(var + 1e-5f);
        float hv[4];
#pragma unroll
        for (int e = 0; e < 4; ++e) hv[e] = (x1[e] - mean) * rstd * cb4[e] + sb4[e];
        *(LAS v2u*)(L + a_h2w) = v2u{pk2(hv[0], hv[1]), pk2(hv[2], hv[3])};
      }
      BAR();
      TS(26);
      if (w < 4) {
        const v4u a0 = *(LAS v4u*)(L + a_ar), a1 = *(LAS v4u*)(L + a_ar + 512);
        v4u a2 = a0;
        if (w < 2) a2 = *(LAS v4u*)(L + a_ar + 1024);
        f32x4 acc[8];
#pragma unroll
        for (int i = 0; i < 8; ++i) acc[i] = mfma16(a0, fa[0][i], f32x4{0.f, 0.f, 0.f, 0.f});
#pragma unroll
        for (int i = 0; i < 8; ++i) acc[i] = mfma16(a1, fa[1][i], acc[i]);
        if (w < 2) {
#pragma unroll
          for (int i = 0; i < 8; ++i) acc[i] = mfma16(a2, fa[2][i], acc[i]);
        }
        if ((g == 0 && r16 < 8) || (g == 2 && r16 >= 8)) {
#pragma unroll
          for (int b2 = 0; b2 < 2; ++b2) {
            *(LAS f32x4*)(L + a_rw + b2 * 256) = f32x4{acc[0][b2], acc[1][b2], acc[2][b2], acc[3][b2]};
            *(LAS f32x4*)(L + a_rw + b2 * 256 + 16) = f32x4{acc[4][b2], acc[5][b2], acc[6][b2], acc[7][b2]};
          }
        }
      }
      if (w < 5) { tr8(wdn, fd); KEEP8(fd); }
      int a_gr = L_QIMG + ((l >> 5) * 64 + (l & 31)) * 4, a_aw = L_MISC + 512 + l * 2, a_acr = L_MISC + 512 + ((r16 & 1) * 32 + 8 * g) * 2;
      int o_x7 = X7 + (idx * 2 * DM + 128 * w + 8 * r16) * 2;
      KEEP(a_gr); KEEP(a_aw); KEEP(a_acr); KEEP(o_x7);
      BAR();
      TS(27);
      if (w == 0) {
        float gv[8], uv[8];
#pragma unroll
        for (int k = 0; k < 8; ++k) { gv[k] = *(LAS float*)(L + a_gr + k * 512); uv[k] = *(LAS float*)(L + a_gr + k * 512 + 128); }
        const float gs = ((gv[0] + gv[1]) + (gv[2] + gv[3])) + ((gv[4] + gv[5]) + (gv[6] + gv[7]));
        const float us = ((uv[0] + uv[1]) + (uv[2] + uv[3])) + ((uv[4] + uv[5]) + (uv[6] + uv[7]));
        *(LAS u16*)(L + a_aw) = (u16)pk2(silu(gs) * us, 0.f);
      }
      BAR();
      TS(28);
      if (w < 5) {
        const v4u a = *(LAS v4u*)(L + a_acr);
        f32x4 d[8];
#pragma unroll
        for (int i = 0; i < 8; ++i) d[i] = mfma16(a, fd[i], f32x4{0.f, 0.f, 0.f, 0.f});
        if (g == 0) {
#pragma unroll
          for (int b2 = 0; b2 < 2; ++b2)
            st16(v4u{pk2(d[0][b2], d[1][b2]), pk2(d[2][b2], d[3][b2]), pk2(d[4][b2], d[5][b2]), pk2(d[6][b2], d[7][b2])}, o_x7 + b2 * DM * 2);
        }
        TS(29);
      }
    } else if (idx >= 64 && idx < 84) {
      const int n = idx - 64;
      float base0 = 0.f, base1 = 0.f, gb0 = 0.f, gb1 = 0.f;
      if (w == 7 && l < 32) {
        const int b = l >> 4, pr = l & 15, m = 32 * n + 2 * pr;
        unsigned ga = 0, gb = 0, ap[8];
        for (unsigned sp = 0; sp < SPIN_MAX; ++sp) {
          asm volatile("" ::: "memory");
          ga = ld4(XM + (b * 2560 + m) * 2); gb = ld4(XM + (b * 2560 + 1920 + m) * 2);
#pragma unroll
          for (int k = 0; k < 8; ++k) ap[k] = ld4(X6 + ((k * 2 + b) * DM + m) * 2);
          if (__all(umax3(umax3(umax3(ap[0], ap[1], ap[2]), umax3(ap[3], ap[4], ap[5]), umax3(ap[6], ap[7], ga)), gb, 0u) != SENT)) break;
        }
        const unsigned xd = *(const GAS unsigned*)(p.x + b * DM + m);
        float s0 = 0.f, s1 = 0.f;
#pragma unroll
        for (int k = 0; k < 8; ++k) { s0 += bflo(ap[k]); s1 += bfhi(ap[k]); }
        base0 = bflo(xd) + bflo(ga) * s0; base1 = bfhi(xd) + bfhi(ga) * s1; gb0 = bflo(gb); gb1 = bfhi(gb);
        KEEP(base0); KEEP(base1); KEEP(gb0); KEEP(gb1);
      }
      int a_fr = L_RED + ((l >> 4) * 32 + 2 * (l & 15)) * 4;
      KEEP(a_fr);
      if (w < 7) {
        const int task = 8 * w + (l >> 3), b = (l >> 2) & 1, ch = l & 3;
        const bool valid = task < 54;
        int off = X7 + (((valid ? task : 0) * 2 + b) * DM + 32 * n + 8 * ch) * 2;
        int a_w = L_RED + (w * 64 + b * 32 + 8 * ch + 4 * (l >> 5) + 2 * ((l >> 4) & 1)) * 4;
        unsigned msk = valid ? 0xFFFFFFFFu : 0u;
        KEEP(off); KEEP(a_w); KEEP(msk);
        PIN();
        v4u a;
        for (unsigned sp = 0; sp < SPIN_MAX; ++sp) {
          asm volatile("" ::: "memory");
          a = ld16(off);
          if (__all(umax4(a) != SENT)) break;
        }
        TS(29);
        float v[8];
#pragma unroll
        for (int d = 0; d < 4; ++d) { const unsigned x = a[d] & msk; v[2 * d] = bflo(x); v[2 * d + 1] = bfhi(x); }
        float t0 = rs16(rs32(v[0], v[4]), rs32(v[2], v[6])), t1 = rs16(rs32(v[1], v[5]), rs32(v[3], v[7]));
        t0 += dppf<0x128>(t0); t1 += dppf<0x128>(t1);
        *(LAS f32x2*)(L + a_w) = f32x2{t0, t1};
      }
      BAR();
      if (w == 7 && l < 32) {
        f32x2 v[7];
#pragma unroll
        for (int k = 0; k < 7; ++k) v[k] = *(LAS f32x2*)(L + a_fr + k * 256);
        const float s0 = ((v[0][0] + v[1][0]) + (v[2][0] + v[3][0])) + ((v[4][0] + v[5][0]) + v[6][0]);
        const float s1 = ((v[0][1] + v[1][1]) + (v[2][1] + v[3][1])) + ((v[4][1] + v[5][1]) + v[6][1]);
        *(GAS unsigned*)(p.out + (l >> 4) * DM + 32 * n + 2 * (l & 15)) = pk2(base0 + gb0 * s0, base1 + gb1 * s1);
        if (n == 0 && l == 0) st4(TAG(), OFF_EPOCH);
      }
    }
  }
done: ;
}

__global__ void __launch_bounds__(512, 2) empty_big(unsigned* p) {
  __shared__ unsigned sm[40000];
  if (blockIdx.x == 100000u) { sm[threadIdx.x * 70] = 1; __syncthreads(); p[70000000] = sm[(threadIdx.x ^ 1) * 70]; }
}
__global__ void empty_small(unsigned* p) { if (blockIdx.x == 100000u) p[70000000] = 1; }
extern "C" void kernel_launch(void* const* d_in, const int* in_sizes, int n_in, void* d_out, int out_size, void* d_ws, size_t ws_size, hipStream_t stream) {
  Params p;
  p.x = (const u16*)d_in[0]; p.ckv = (const u16*)d_in[1]; p.wq = (const u16*)d_in[2]; p.wuk = (const u16*)d_in[3]; p.wuv = (const u16*)d_in[4]; p.wo = (const u16*)d_in[5];
  p.wg = (const u16*)d_in[7]; p.wu = (const u16*)d_in[8]; p.wd = (const u16*)d_in[9]; p.c = (const u16*)d_in[10]; p.wmod = (const u16*)d_in[11];
  p.out = (u16*)d_out; p.ws = (unsigned char*)d_ws;
  mega<<<256, 512, 0, stream>>>(p);
  empty_small<<<1, 64, 0, stream>>>((unsigned*)d_ws);
}
```

```cpp
#include <hip/hip_runtime.h>
#include <stdint.h>

#define GAS __attribute__((address_space(1)))
#define LAS __attribute__((address_space(3)))
typedef unsigned v4u __attribute__((ext_vector_type(4)));
typedef unsigned v2u __attribute__((ext_vector_type(2)));
typedef float f32x4 __attribute__((ext_vector_type(4)));
typedef float f32x2 __attribute__((ext_vector_type(2)));
typedef __bf16 bf16x8 __attribute__((ext_vector_type(8)));
typedef __bf16 bf16x2 __attribute__((ext_vector_type(2)));
typedef short s16x4 __attribute__((ext_vector_type(4)));
typedef unsigned short u16;
typedef unsigned long long u64;

constexpr int NB = 2, NH = 32, SK = 8192, DH = 128, DM = 640, DC = 512, DF = 1728;
constexpr int OFF_EPOCH = 0;
constexpr int OFF_FLAGS = 4096;
constexpr int OFF_EFLAGS = OFF_FLAGS + 256 * 128;
constexpr int OFF_X1 = OFF_EFLAGS + 128 * 128;
constexpr int OFF_R = 81920;
constexpr int X2 = OFF_R;
constexpr int X3 = X2 + 65536;
constexpr int X4 = X3 + 65536;
constexpr int X4S = X4 + 4194304;
constexpr int X5 = X4S + 32768;
constexpr int X6 = X5 + 65536;
constexpr int X7 = X6 + 20480;
constexpr int XM = X7 + 138240;
constexpr int R_END = XM + 10240;
static_assert(OFF_X1 + 20480 <= OFF_R, "map");
constexpr int WS_USED = R_END;
#define CKV_A 0
#define CKV_B 6
constexpr unsigned SENT = 0xFFFFFFFFu;
constexpr int AUX_SC1 = 16;
constexpr unsigned SPIN_MAX = 1u << 18;
constexpr int L_CKV = 0;
constexpr int L_QIMG = 131072;
constexpr int L_PIMG = L_QIMG + 16384;
constexpr int L_RED = L_PIMG + 4096;
constexpr int L_MISC = L_RED + 8192;
constexpr int LDS_BYTES = L_MISC + 2048;
static_assert(LDS_BYTES <= 163840, "LDS");

#define TS(k) do { } while (0)

struct Params {
  const u16 *x, *ckv, *wq, *wuk, *wuv, *wo, *wg, *wu, *wd, *c, *wmod;
  u16* out;
  unsigned char* ws;
};

#define BAR() do { asm volatile("s_waitcnt lgkmcnt(0)" ::: "memory"); __builtin_amdgcn_s_barrier(); asm volatile("" ::: "memory"); } while (0)
#define DRAIN() asm volatile("s_waitcnt vmcnt(0)" ::: "memory")
#define PIN() __builtin_amdgcn_sched_barrier(0)
#define KEEP(x) asm volatile("" : "+v"(x))
#define KEEP4(a) do { KEEP(a[0]); KEEP(a[1]); KEEP(a[2]); KEEP(a[3]); } while (0)
#define KEEP8(a) do { KEEP(a[0]); KEEP(a[1]); KEEP(a[2]); KEEP(a[3]); KEEP(a[4]); KEEP(a[5]); KEEP(a[6]); KEEP(a[7]); } while (0)

__device__ __forceinline__ float bflo(unsigned d) { return __uint_as_float(d << 16); }
__device__ __forceinline__ float bfhi(unsigned d) { return __uint_as_float(d & 0xffff0000u); }
__device__ __forceinline__ unsigned pk2(float lo, float hi) { f32x2 f = {lo, hi}; return __builtin_bit_cast(unsigned, __builtin_convertvector(f, bf16x2)); }
__device__ __forceinline__ f32x4 mfma16(v4u a, v4u b, f32x4 c) { return __builtin_amdgcn_mfma_f32_16x16x32_bf16(__builtin_bit_cast(bf16x8, a), __builtin_bit_cast(bf16x8, b), c, 0, 0, 0); }
__device__ __forceinline__ float dot2(unsigned a, unsigned b, float c) { return __builtin_amdgcn_fdot2_f32_bf16(__builtin_bit_cast(bf16x2, a), __builtin_bit_cast(bf16x2, b), c, false); }
__device__ __forceinline__ v4u ldnt(const u16* p) { return __builtin_nontemporal_load((const GAS v4u*)p); }
__device__ __forceinline__ unsigned umax3(unsigned a, unsigned b, unsigned c) { unsigned t = a > b ? a : b; return t > c ? t : c; }
__device__ __forceinline__ unsigned umax4(v4u v) { return umax3(umax3(v.x, v.y, v.z), v.w, 0u); }
__device__ __forceinline__ unsigned umax4a(v4u v, unsigned m) { return umax3(umax3(v.x, v.y, v.z), v.w, m); }
__device__ __forceinline__ float x16_max(float x) {
  auto s = __builtin_amdgcn_permlane16_swap(__float_as_uint(x), __float_as_uint(x), false, false);
  x = fmaxf(__uint_as_float(s[0]), __uint_as_float(s[1]));
  auto t = __builtin_amdgcn_permlane32_swap(__float_as_uint(x), __float_as_uint(x), false, false);
  return fmaxf(__uint_as_float(t[0]), __uint_as_float(t[1]));
}
__device__ __forceinline__ float x16_sum(float x) {
  auto s = __builtin_amdgcn_permlane16_swap(__float_as_uint(x), __float_as_uint(x), false, false);
  x = __uint_as_float(s[0]) + __uint_as_float(s[1]);
  auto t = __builtin_amdgcn_permlane32_swap(__float_as_uint(x), __float_as_uint(x), false, false);
  return __uint_as_float(t[0]) + __uint_as_float(t[1]);
}
__device__ __forceinline__ float rs32(float a, float b) { auto t = __builtin_amdgcn_permlane32_swap(__float_as_uint(a), __float_as_uint(b), false, false); return __uint_as_float(t[0]) + __uint_as_float(t[1]); }
__device__ __forceinline__ float rs16(float a, float b) { auto t = __builtin_amdgcn_permlane16_swap(__float_as_uint(a), __float_as_uint(b), false, false); return __uint_as_float(t[0]) + __uint_as_float(t[1]); }
template <int CTRL> __device__ __forceinline__ float dppf(float x) { return __builtin_bit_cast(float, __builtin_amdgcn_mov_dpp(__builtin_bit_cast(int, x), CTRL, 0xf, 0xf, true)); }
__device__ __forceinline__ float row16_sum(float x) { x += dppf<0xB1>(x); x += dppf<0x4E>(x); x += dppf<0x141>(x); x += dppf<0x128>(x); return x; }
__device__ __forceinline__ float wave_sum(float x) { return x16_sum(row16_sum(x)); }
__device__ __forceinline__ float silu(float v) { return v * __builtin_amdgcn_rcpf(1.f + __builtin_amdgcn_exp2f(v * -1.4426950408889634f)); }

__device__ __forceinline__ void tr8(const v4u (&w)[8], v4u (&f)[8]) {
#pragma unroll
  for (int d = 0; d < 4; ++d) {
#pragma unroll
    for (int i2 = 0; i2 < 4; ++i2) {
      f[2 * i2][d] = __builtin_amdgcn_perm(w[2 * d + 1][i2], w[2 * d][i2], 0x05040100u);
      f[2 * i2 + 1][d] = __builtin_amdgcn_perm(w[2 * d + 1][i2], w[2 * d][i2], 0x07060302u);
    }
  }
}
__device__ __forceinline__ v4u silu8(v4u c) {
  v4u r;
#pragma unroll
  for (int d = 0; d < 4; ++d) r[d] = pk2(silu(bflo(c[d])), silu(bfhi(c[d])));
  return r;
}
__device__ __forceinline__ float sum5(float a, float b, float c, float d, float e) { return ((a + b) + (c + d)) + e; }

__global__ void __launch_bounds__(512, 2) mega(Params p) {
  __shared__ __attribute__((aligned(16))) unsigned char smem[LDS_BYTES];
  LAS unsigned char* L = (LAS unsigned char*)smem;
  const int tid = threadIdx.x, l = tid & 63, w = __builtin_amdgcn_readfirstlane(tid >> 6);
  const int bid = blockIdx.x;
  const int g = l >> 4, r16 = l & 15;
  TS(0);
  const __amdgpu_buffer_rsrc_t rs = __builtin_amdgcn_make_buffer_rsrc((void*)p.ws, 0, WS_USED, 0x00020000);
  const bool Ltype = bid >= 128;
  const int idx = bid & 127;
  const int ab = idx >> 6, aj = idx & 63, hg = bid >> 7;
  const unsigned epoch_raw = __builtin_amdgcn_raw_buffer_load_b32(rs, OFF_EPOCH, 0, AUX_SC1);
  auto TAG = [&]() { unsigned t = epoch_raw; asm volatile("" : "+v"(t)); return t + 1u; };

  auto ld16 = [&](int off) { return __builtin_amdgcn_raw_buffer_load_b128(rs, off, 0, AUX_SC1); };
  auto ld8 = [&](int off) { return __builtin_amdgcn_raw_buffer_load_b64(rs, off, 0, AUX_SC1); };
  auto ld4 = [&](int off) { return __builtin_amdgcn_raw_buffer_load_b32(rs, off, 0, AUX_SC1); };
  auto st16 = [&](v4u v, int off) { __builtin_amdgcn_raw_buffer_store_b128(v, rs, off, 0, AUX_SC1); };
  auto st8 = [&](v2u v, int off) { __builtin_amdgcn_raw_buffer_store_b64(v, rs, off, 0, AUX_SC1); };
  auto st4 = [&](unsigned v, int off) { __builtin_amdgcn_raw_buffer_store_b32(v, rs, off, 0, AUX_SC1); };
  auto dma_ckv = [&](int row0, int nrows) {
    const u16* base = p.ckv + ((size_t)ab * SK + aj * 128) * DC;
    for (int rr = 0; rr < nrows; ++rr) {
      const int row = row0 + rr;
      const int sw = ((row & 3) << 2) | ((row >> 2) & 3);
      const u16* src = base + row * DC + ((l ^ sw) * 8);
      unsigned keep; const unsigned dst = (unsigned)(L_CKV + row * 1024) + (unsigned)(size_t)L;
      asm volatile("s_mov_b32 %0, m0\n\ts_mov_b32 m0, %2\n\ts_nop 0\n\tglobal_load_lds_dwordx4 %1, off nt\n\ts_mov_b32 m0, %0" : "=&s"(keep) : "v"(src), "s"(dst) : "memory");
    }
  };
  const v4u SENT4 = {SENT, SENT, SENT, SENT};
  auto fill_x4 = [&]() {
#pragma unroll
    for (int k = 0; k < 2; ++k) st16(SENT4, X4 + ((ab * NH + 16 * hg + 2 * w + k) * 64 + aj) * 1024 + l * 16);
    if (w == 0 && l < 8) st16(SENT4, X4S + ((ab * 64 + aj) * NH + 16 * hg) * 8 + l * 16);
  };
  auto check_flags = [&](int off, int nper) {
    const unsigned tag = TAG();
    for (unsigned sp = 0; sp < SPIN_MAX; ++sp) {
      asm volatile("" ::: "memory");
      bool ok = true;
      for (int k = 0; k < nper; ++k) ok &= ld4(off + (k * 64 + l) * 128) == tag;
      if (__all(ok)) break;
      __builtin_amdgcn_s_sleep(1);
    }
  };

  auto attention = [&](auto&& hook_post) {
    DRAIN();
    BAR();
    TS(11);
    if (w == 7 && l == 0) st4(TAG(), OFF_FLAGS + bid * 128);
    v4u qk[16], pv[16];
    {
      const int key = 16 * w + r16;
      const int sw = ((key & 3) << 2) | ((key >> 2) & 3);
#pragma unroll
      for (int s = 0; s < 16; ++s) qk[s] = *(LAS v4u*)(L + L_CKV + key * 1024 + (s >> 2) * 256 + (((4 * (s & 3) + g) ^ sw) * 16));
      const int q4 = r16 >> 2, p4 = l & 3;
#pragma unroll
      for (int s = 0; s < 4; ++s)
#pragma unroll
        for (int t = 0; t < 4; ++t) {
          v2u h2[2];
#pragma unroll
          for (int hf = 0; hf < 2; ++hf) {
            const int ky = 32 * s + 16 * hf + 4 * g + q4;
            const int sw2 = (q4 << 2) | g;
            const int ch = 8 * w + 4 * (t >> 1) + p4;
            const s16x4 v = __builtin_amdgcn_ds_read_tr16_b64_v4i16((LAS s16x4*)(L + L_CKV + ky * 1024 + ((ch ^ sw2) * 16) + 8 * (t & 1)));
            h2[hf] = __builtin_bit_cast(v2u, v);
          }
          pv[s * 4 + t] = v4u{h2[0].x, h2[0].y, h2[1].x, h2[1].y};
        }
    }
    int a_qw0 = L_QIMG + w * 1024 + ((l ^ w) * 16), a_qw1 = L_QIMG + (8 + w) * 1024 + ((l ^ (8 + w)) * 16);
    int a_qr[4];
#pragma unroll
    for (int t = 0; t < 4; ++t) a_qr[t] = L_QIMG + r16 * 1024 + (((4 * t + g) ^ r16) * 16);
    int a_mxw = L_MISC + r16 * 32 + w * 4, a_mxr = L_MISC + r16 * 32;
    int a_pw = L_PIMG + (w >> 1) * 1024 + l * 16 + (w & 1) * 8, a_pr = L_PIMG + l * 16;
    const int hd = 16 * hg + r16;
    int o_st = X4 + (((ab * NH + hd) * 64 + aj) * 512 + 64 * w + 8 * g) * 2, o_ss = X4S + ((ab * 64 + aj) * NH + hd) * 8;
    int o_q = X3 + (ab * NH + 16 * hg) * 1024 + tid * 16;
    KEEP(a_qw0); KEEP(a_qw1); KEEP4(a_qr); KEEP(a_mxw); KEEP(a_mxr); KEEP(a_pw); KEEP(a_pr); KEEP(o_st); KEEP(o_ss); KEEP(o_q);
#pragma unroll
    for (int s = 0; s < 16; ++s) { KEEP(qk[s]); KEEP(pv[s]); }
    TS(12);
    PIN();
    {
      v4u a0, a1;
      for (unsigned sp = 0; sp < SPIN_MAX; ++sp) {
        asm volatile("" ::: "memory");
        a0 = ld16(o_q); a1 = ld16(o_q + 8192);
        if (__all(umax4a(a1, umax4(a0)) != SENT)) break;
      }
      *(LAS v4u*)(L + a_qw0) = a0; *(LAS v4u*)(L + a_qw1) = a1;
    }
    TS(13);
    unsigned lf[4] = {0u, 0u, 0u, 0u};
    if (w == 6) {
#pragma unroll
      for (int k = 0; k < 4; ++k) lf[k] = ld4(OFF_FLAGS + (k * 64 + l) * 128);
    }
    PIN();
    BAR();
    TS(14);
    f32x4 sacc = {0.f, 0.f, 0.f, 0.f}, sacc2 = {0.f, 0.f, 0.f, 0.f};
    {
      v4u qf[16];
#pragma unroll
      for (int s = 0; s < 16; ++s) qf[s] = *(LAS v4u*)(L + a_qr[s & 3] + (s >> 2) * 256);
      PIN();
#pragma unroll
      for (int s = 0; s < 16; s += 2) { sacc = mfma16(qk[s], qf[s], sacc); sacc2 = mfma16(qk[s + 1], qf[s + 1], sacc2); }
    }
    sacc += sacc2;
    const float mloc = x16_max(fmaxf(fmaxf(sacc[0], sacc[1]), fmaxf(sacc[2], sacc[3])));
    *(LAS float*)(L + a_mxw) = mloc;
    TS(15);
    BAR();
    TS(16);
    const f32x4 m0 = *(LAS f32x4*)(L + a_mxr), m1 = *(LAS f32x4*)(L + a_mxr + 16);
    const float m = fmaxf(fmaxf(fmaxf(m0[0], m0[1]), fmaxf(m0[2], m0[3])), fmaxf(fmaxf(m1[0], m1[1]), fmaxf(m1[2], m1[3])));
    const unsigned d0 = pk2(__builtin_amdgcn_exp2f(sacc[0] - m), __builtin_amdgcn_exp2f(sacc[1] - m)), d1 = pk2(__builtin_amdgcn_exp2f(sacc[2] - m), __builtin_amdgcn_exp2f(sacc[3] - m));
    *(LAS v2u*)(L + a_pw) = v2u{d0, d1};
    BAR();
    TS(17);
    f32x4 o[4], lacc = {0.f, 0.f, 0.f, 0.f};
#pragma unroll
    for (int t = 0; t < 4; ++t) o[t] = f32x4{0.f, 0.f, 0.f, 0.f};
    v4u pf[4];
#pragma unroll
    for (int s = 0; s < 4; ++s) pf[s] = *(LAS v4u*)(L + a_pr + s * 1024);
    PIN();
#pragma unroll
    for (int s = 0; s < 4; ++s)
#pragma unroll
      for (int t = 0; t < 4; ++t) o[t] = mfma16(pv[s * 4 + t], pf[s], o[t]);
    st16(v4u{pk2(o[0][0], o[0][1]), pk2(o[0][2], o[0][3]), pk2(o[1][0], o[1][1]), pk2(o[1][2], o[1][3])}, o_st);
    st16(v4u{pk2(o[2][0], o[2][1]), pk2(o[2][2], o[2][3]), pk2(o[3][0], o[3][1]), pk2(o[3][2], o[3][3])}, o_st + 64);
    if (w == 0) {
      const v4u ones = {0x3F803F80u, 0x3F803F80u, 0x3F803F80u, 0x3F803F80u};
#pragma unroll
      for (int s = 0; s < 4; ++s) lacc = mfma16(ones, pf[s], lacc);
      if (g == 0) st8(v2u{__float_as_uint(m), __float_as_uint(lacc[0])}, o_ss);
    }
    TS(18);
    PIN();
    hook_post();
    PIN();
    if (w == 6) {
      const unsigned tag = TAG();
      if (!__all((lf[0] == tag) & (lf[1] == tag) & (lf[2] == tag) & (lf[3] == tag))) check_flags(OFF_FLAGS, 4);
    }
    BAR();
    TS(19);
  };

  if (Ltype) {
    const int hh = idx >> 2, sub = idx & 3;
    const bool t1role = idx < 40;
    const int mg = idx % 10, pp1 = idx / 10;
    v4u uk[4];
    auto load_t3 = [&]() {
      const u16* src = p.wuk + ((size_t)(128 * sub + 16 * w + r16) * NH + hh) * DH + 8 * g;
#pragma unroll
      for (int s = 0; s < 4; ++s) uk[s] = ldnt(src + 32 * s);
    };
    auto stage_t3 = [&]() {
      if (CKV_B > 0) dma_ckv(16 * w, CKV_B);
      const int kq = (r16 & 7) >> 1, b = r16 & 1;
      int off = X2 + ((hh * 4 + kq) * 2 + b) * 256 + 16 * g;
      int a_w = L_RED + ((g & 1) * 128 + 16 * w + r16) * 2;
      int o_x3 = X3 + ((l >> 4) * NH + hh) * 1024 + sub * 256 + (l & 15) * 16;
      KEEP(off); KEEP(a_w); KEEP(o_x3);
      PIN();
      v4u a[4];
      for (unsigned sp = 0; sp < SPIN_MAX; ++sp) {
        asm volatile("" ::: "memory");
#pragma unroll
        for (int s = 0; s < 4; ++s) a[s] = ld16(off + 64 * s);
        if (__all(umax4a(a[3], umax4a(a[2], umax4a(a[1], umax4(a[0])))) != SENT)) break;
      }
      TS(9);
      f32x4 acc = mfma16(a[0], uk[0], f32x4{0.f, 0.f, 0.f, 0.f}), acc2 = mfma16(a[1], uk[1], f32x4{0.f, 0.f, 0.f, 0.f});
      acc = mfma16(a[2], uk[2], acc); acc2 = mfma16(a[3], uk[3], acc2);
      acc += acc2;
      const float sc = 0.08838834764831845f * 1.4426950408889634f;
      const float tot = rs16(acc[0] + acc[2], acc[1] + acc[3]) * sc;
      if (g < 2) *(LAS u16*)(L + a_w) = (u16)pk2(tot, tot);
      BAR();
      if (w == 0 && l < 32) {
        st16(*(LAS v4u*)(L + L_RED + l * 16), o_x3);
        TS(10);
      }
    };
    if (w < 5) {
      v4u wv2[8];
      const u16* src2 = p.wq + (size_t)(160 * sub + 32 * w + 8 * g) * (NH * DH) + hh * DH + 8 * r16;
      if (t1role) {
        v4u wv[8];
        const int col = (r16 < 8) ? (64 * mg + 8 * r16) : (DM + 64 * mg + 8 * (r16 - 8));
        const int k0 = 160 * pp1 + 32 * w + 8 * g;
        const v4u cv = *(const GAS v4u*)(p.c + (r16 & 1) * DM + k0);
        const unsigned xd = *(const GAS unsigned*)(p.x + (l >> 5) * DM + 64 * mg + 2 * (l & 31));
        const u16* src = p.wmod + (size_t)k0 * (6 * DM) + col;
#pragma unroll
        for (int j = 0; j < 8; ++j) wv[j] = ldnt(src + (size_t)j * (6 * DM));
        int a_w = L_RED + ((w * 2) * 128 + 8 * r16) * 4;
        int a_r = L_RED + ((l >> 5) * 128 + 2 * (l & 31)) * 4;
        int o_x1 = OFF_X1 + (((pp1 * 2 + (l >> 5)) * 320) + 32 * mg + (l & 31)) * 8;
        KEEP(a_w); KEEP(a_r); KEEP(o_x1);
        PIN();
        v4u a = silu8(cv);
        float xlo = bflo(xd), xhi = bfhi(xd);
        unsigned tag = TAG();
        KEEP(a); KEEP(xlo); KEEP(xhi); KEEP(tag);
        PIN();
        v4u f[8]; tr8(wv, f);
        KEEP8(f);
        TS(1);
        PIN();
#pragma unroll
        for (int j = 0; j < 8; ++j) wv2[j] = ldnt(src2 + (size_t)j * (NH * DH));
        PIN();
        f32x4 acc[8];
#pragma unroll
        for (int i = 0; i < 8; ++i) acc[i] = mfma16(a, f[i], f32x4{0.f, 0.f, 0.f, 0.f});
        if (g == 0) {
#pragma unroll
          for (int b = 0; b < 2; ++b) {
            *(LAS f32x4*)(L + a_w + b * 512) = f32x4{acc[0][b], acc[1][b], acc[2][b], acc[3][b]};
            *(LAS f32x4*)(L + a_w + b * 512 + 16) = f32x4{acc[4][b], acc[5][b], acc[6][b], acc[7][b]};
          }
        }
        TS(2);
        BAR();
        TS(3);
        if (w == 0) {
          f32x2 s[5], c2[5];
#pragma unroll
          for (int ww = 0; ww < 5; ++ww) { s[ww] = *(LAS f32x2*)(L + a_r + ww * 1024); c2[ww] = *(LAS f32x2*)(L + a_r + ww * 1024 + 256); }
          const f32x2 mr = *(LAS f32x2*)(L + L_MISC + (l >> 5) * 8);
          const float xn0 = (xlo - mr[0]) * mr[1], xn1 = (xhi - mr[0]) * mr[1];
          const float one = pp1 == 0 ? 1.f : 0.f;
          const float o0 = xn0 * (sum5(c2[0][0], c2[1][0], c2[2][0], c2[3][0], c2[4][0]) + one) + sum5(s[0][0], s[1][0], s[2][0], s[3][0], s[4][0]);
          const float o1 = xn1 * (sum5(c2[0][1], c2[1][1], c2[2][1], c2[3][1], c2[4][1]) + one) + sum5(s[0][1], s[1][1], s[2][1], s[3][1], s[4][1]);
          st8(v2u{pk2(o0, o1), tag}, o_x1);
          TS(4);
        }
      } else {
#pragma unroll
        for (int j = 0; j < 8; ++j) wv2[j] = ldnt(src2 + (size_t)j * (NH * DH));
      }
      load_t3();
      PIN();
      {
        unsigned tag = TAG();
        v4u f[8]; tr8(wv2, f);
        const int pp = (r16 & 7) >> 1, b = r16 & 1;
        int off = OFF_X1 + ((pp * 2 + b) * 320 + (160 * sub + 32 * w + 8 * g) / 2) * 8;
        int a_w = L_QIMG + (((w * 2 + (g & 1)) * 2) * 128 + 8 * r16) * 4;
        int a_r = L_QIMG + ((w & 1) * 128 + 2 * l) * 4, o_x2 = X2 + ((hh * 4 + sub) * 2 + (w & 1)) * 256 + l * 4;
        KEEP8(f); KEEP(off); KEEP(a_w); KEEP(tag); KEEP(a_r); KEEP(o_x2);
        TS(5);
        PIN();
        v4u a;
        for (unsigned sp = 0; sp < SPIN_MAX; ++sp) {
          asm volatile("" ::: "memory");
          const v4u u0 = ld16(off), u1 = ld16(off + 16);
          a = v4u{u0.x, u0.z, u1.x, u1.z};
          if (__all(((u0.y ^ tag) | (u0.w ^ tag) | (u1.y ^ tag) | (u1.w ^ tag)) == 0u)) break;
        }
        TS(6);
        f32x4 acc[8];
#pragma unroll
        for (int i = 0; i < 8; ++i) acc[i] = mfma16(a, f[i], f32x4{0.f, 0.f, 0.f, 0.f});
        if (g < 2) {
#pragma unroll
          for (int b2 = 0; b2 < 2; ++b2) {
            *(LAS f32x4*)(L + a_w + b2 * 512) = f32x4{acc[0][b2] + acc[0][b2 + 2], acc[1][b2] + acc[1][b2 + 2], acc[2][b2] + acc[2][b2 + 2], acc[3][b2] + acc[3][b2 + 2]};
            *(LAS f32x4*)(L + a_w + b2 * 512 + 16) = f32x4{acc[4][b2] + acc[4][b2 + 2], acc[5][b2] + acc[5][b2 + 2], acc[6][b2] + acc[6][b2 + 2], acc[7][b2] + acc[7][b2 + 2]};
          }
        }
        BAR();
        TS(7);
        if (w < 2) {
          f32x2 v[10];
#pragma unroll
          for (int k = 0; k < 10; ++k) v[k] = *(LAS f32x2*)(L + a_r + k * 1024);
          const float s0 = sum5(v[0][0], v[1][0], v[2][0], v[3][0], v[4][0]) + sum5(v[5][0], v[6][0], v[7][0], v[8][0], v[9][0]);
          const float s1 = sum5(v[0][1], v[1][1], v[2][1], v[3][1], v[4][1]) + sum5(v[5][1], v[6][1], v[7][1], v[8][1], v[9][1]);
          st4(pk2(s0, s1), o_x2);
          TS(8);
        }
      }
      stage_t3();
    } else {
      if (w == 7) {
        if (l < 32) st16(SENT4, X2 + (hh * 4 + sub) * 512 + l * 16);
        else st16(SENT4, X3 + (((l - 32) >> 4) * NH + hh) * 1024 + sub * 256 + (l & 15) * 16);
        DRAIN();
        if (l == 0) st4(TAG(), OFF_EFLAGS + idx * 128);
      } else if (t1role) {
        const int b = w - 5;
        float v[10]; float s = 0.f;
#pragma unroll
        for (int k = 0; k < 5; ++k) { const unsigned d = *(const GAS unsigned*)(p.x + b * DM + 2 * (l + 64 * k)); v[2 * k] = bflo(d); v[2 * k + 1] = bfhi(d); s += v[2 * k] + v[2 * k + 1]; }
        const float mean = wave_sum(s) * (1.f / DM);
        float q = 0.f;
#pragma unroll
        for (int k = 0; k < 10; ++k) { const float d = v[k] - mean; q += d * d; }
        const float var = wave_sum(q) * (1.f / DM);
        *(LAS f32x2*)(L + L_MISC + b * 8) = f32x2{mean, __builtin_amdgcn_rsqf(var + 1e-5f)};
      }
      if (t1role) BAR();
      {
        const int h0 = (w - 5) * 6, nh = w == 7 ? 4 : 6;
        for (int k = 0; k < nh; ++k) st16(SENT4, X4 + ((ab * NH + 16 * hg + h0 + k) * 64 + aj) * 1024 + l * 16);
        if (w == 7) {
          if (l < 8) st16(SENT4, X4S + ((ab * 64 + aj) * NH + 16 * hg) * 8 + l * 16);
          else if (l >= 16 && l < 32) {
            const int u = l - 16;
            if (idx < 80) st16(SENT4, X6 + (((idx / 10) * 2 + (u >> 3)) * DM + 64 * (idx % 10)) * 2 + (u & 7) * 16);
            else if (idx < 120) st16(SENT4, XM + ((u >> 3) * 2560 + 64 * (idx - 80)) * 2 + (u & 7) * 16);
          }
        }
      }
      load_t3();
      if (w == 6) check_flags(OFF_EFLAGS, 2);
      BAR();
      stage_t3();
    }
    dma_ckv(16 * w + CKV_B, 16 - CKV_B);
    v4u wpre[3][8];
    attention([&]() {
      if (w < 4) {
        const int kk = (l >> 3) & 1, ch = l & 7;
        if (idx < 80) {
          const int pp = idx / 10, n = idx % 10;
          const u16* src = p.wo + (size_t)(512 * pp + 64 * w + 32 * kk + 8 * g) * DM + 64 * n + 8 * ch;
#pragma unroll
          for (int k = 0; k < 2; ++k)
#pragma unroll
            for (int j = 0; j < 8; ++j) wpre[k][j] = ldnt(src + (size_t)(256 * k + j) * DM);
        } else if (idx < 120) {
          const int tb = idx - 80;
          const u16* src = p.wmod + (size_t)(64 * w + 32 * kk + 8 * g) * (6 * DM) + 2 * DM + 64 * tb + 8 * ch;
#pragma unroll
          for (int k = 0; k < 2; ++k)
#pragma unroll
            for (int j = 0; j < 8; ++j) wpre[k][j] = ldnt(src + (size_t)(256 * k + j) * (6 * DM));
          if (w < 2) {
#pragma unroll
            for (int j = 0; j < 8; ++j) wpre[2][j] = ldnt(src + (size_t)(512 + j) * (6 * DM));
          }
        }
      }
    });
    LAS float* red = (LAS float*)(L + L_RED);
    if (idx < 80) {
      const int pp = idx / 10, n = idx % 10;
      if (w < 4) {
        v4u f0[8], f1[8]; tr8(wpre[0], f0); tr8(wpre[1], f1);
        const int rho = r16 & 7, cq = rho >> 1, b = rho & 1, kk2 = r16 >> 3;
        int off = X5 + (((4 * pp + (w >> 1)) * 4 + cq) * 2 + b) * 256 + (64 * (w & 1) + 32 * kk2 + 8 * g) * 2;
        int a_w = L_RED + (((w * 2 + (g >> 1)) * 2 + (g & 1)) * 64 + 8 * (r16 & 7)) * 4;
        KEEP8(f0); KEEP8(f1); KEEP(off); KEEP(a_w);
        PIN();
        v4u a0, a1;
        for (unsigned sp = 0; sp < SPIN_MAX; ++sp) {
          asm volatile("" ::: "memory");
          a0 = ld16(off); a1 = ld16(off + 2 * 4 * 2 * 256);
          if (__all(umax4a(a1, umax4(a0)) != SENT)) break;
        }
        TS(24);
        f32x4 d[8];
#pragma unroll
        for (int i = 0; i < 8; ++i) d[i] = mfma16(a0, f0[i], f32x4{0.f, 0.f, 0.f, 0.f});
#pragma unroll
        for (int i = 0; i < 8; ++i) d[i] = mfma16(a1, f1[i], d[i]);
        float s[8];
#pragma unroll
        for (int i = 0; i < 8; ++i) s[i] = rs16(d[i][0] + d[i][2], d[i][1] + d[i][3]);
        if ((g >> 1) == (r16 >> 3)) {
          *(LAS f32x4*)(L + a_w) = f32x4{s[0], s[1], s[2], s[3]}; *(LAS f32x4*)(L + a_w + 16) = f32x4{s[4], s[5], s[6], s[7]};
        }
      }
      int a_r = L_RED + ((l >> 5) * 64 + 2 * (l & 31)) * 4, o_x6 = X6 + ((pp * 2 + (l >> 5)) * DM + 64 * n + 2 * (l & 31)) * 2;
      KEEP(a_r); KEEP(o_x6);
      BAR();
      TS(25);
      if (w == 0) {
        f32x2 v[8];
#pragma unroll
        for (int k = 0; k < 8; ++k) v[k] = *(LAS f32x2*)(L + a_r + k * 512);
        const float s0 = ((v[0][0] + v[1][0]) + (v[2][0] + v[3][0])) + ((v[4][0] + v[5][0]) + (v[6][0] + v[7][0]));
        const float s1 = ((v[0][1] + v[1][1]) + (v[2][1] + v[3][1])) + ((v[4][1] + v[5][1]) + (v[6][1] + v[7][1]));
        st4(pk2(s0, s1), o_x6);
        TS(29);
      }
    } else if (idx < 120) {
      const int tb = idx - 80;
      if (w < 4) {
        f32x4 acc[8];
#pragma unroll
        for (int i = 0; i < 8; ++i) acc[i] = f32x4{0.f, 0.f, 0.f, 0.f};
#pragma unroll
        for (int k = 0; k < 3; ++k) {
          if (k < 2 || w < 2) {
            const v4u cv = *(const GAS v4u*)(p.c + (r16 & 1) * DM + 64 * (w + 4 * k) + 32 * (r16 >> 3) + 8 * g);
            const v4u a = silu8(cv);
            v4u f[8];
            tr8(wpre[k], f);
#pragma unroll
            for (int i = 0; i < 8; ++i) acc[i] = mfma16(a, f[i], acc[i]);
          }
        }
        if ((g == 0 && r16 < 8) || (g == 2 && r16 >= 8)) {
#pragma unroll
          for (int b2 = 0; b2 < 2; ++b2) {
            LAS float* d = red + ((w * 2 + (g >> 1)) * 2 + b2) * 64 + 8 * (r16 & 7);
            *(LAS f32x4*)d = f32x4{acc[0][b2], acc[1][b2], acc[2][b2], acc[3][b2]}; *(LAS f32x4*)(d + 4) = f32x4{acc[4][b2], acc[5][b2], acc[6][b2], acc[7][b2]};
          }
        }
      }
      BAR();
      if (w == 0) {
        const int b = l >> 5, pr = l & 31;
        float s0 = 0.f, s1 = 0.f;
#pragma unroll
        for (int k = 0; k < 8; ++k) { const f32x2 v = *(LAS f32x2*)(red + (k * 2 + b) * 64 + 2 * pr); s0 += v[0]; s1 += v[1]; }
        st4(pk2(s0, s1), XM + (b * 2560 + 64 * tb + 2 * pr) * 2);
        TS(29);
      }
    }
  } else {
    fill_x4();
    if (w == 1 && l < 32) st16(SENT4, X5 + idx * 512 + l * 16);
    if (idx < 54 && w >= 2 && w < 5) { const int u = (w - 2) * 64 + l; if (u < 160) st16(SENT4, X7 + idx * 2560 + u * 16); }
    dma_ckv(16 * w, 16);
    if (w == 6) check_flags(OFF_EFLAGS, 2);
    const int h5 = idx >> 2, cq = idx & 3;
    v4u wpre[8];
    attention([&]() {
      if (w < 4) {
        const u16* src = p.wuv + ((size_t)(128 * cq + 32 * w + 8 * g) * NH + h5) * DH + 8 * r16;
#pragma unroll
        for (int j = 0; j < 8; ++j) wpre[j] = ldnt(src + (size_t)j * (NH * DH));
      }
    });
    {
      const int b = w >> 2;
      int off = X4 + (((b * NH + h5) * 64 + 16 * (w & 3) + g) * 512 + 128 * cq + 8 * r16) * 2;
      int offs = X4S + ((b * 64 + 16 * (w & 3) + g) * NH + h5) * 8;
      const int cofs = 8 * r16 + 4 * (l >> 5) + 2 * ((l >> 4) & 1);
      int a_w = L_RED + w * 256 + (((cofs >> 3) ^ w) * 16) + (cofs & 7) * 2;
      int a_s = L_MISC + 1024 + w * 8;
      KEEP(off); KEEP(offs); KEEP(a_w); KEEP(a_s);
      PIN();
      v4u a[4]; v2u sv[4];
      for (unsigned sp = 0; sp < SPIN_MAX; ++sp) {
        asm volatile("" ::: "memory");
#pragma unroll
        for (int k = 0; k < 4; ++k) { sv[k] = ld8(offs + k * 1024); a[k] = ld16(off + k * 4096); }
        unsigned m = umax3(umax3(sv[0].x, sv[0].y, sv[1].x), umax3(sv[1].y, sv[2].x, sv[2].y), umax3(sv[3].x, sv[3].y, 0u));
        m = umax4a(a[1], umax4a(a[0], m)); m = umax4a(a[3], umax4a(a[2], m));
        if (__all(m != SENT)) break;
      }
      TS(20);
      float mw = fmaxf(fmaxf(__uint_as_float(sv[0].x), __uint_as_float(sv[1].x)), fmaxf(__uint_as_float(sv[2].x), __uint_as_float(sv[3].x)));
      mw = x16_max(mw);
      float acc[8] = {0.f, 0.f, 0.f, 0.f, 0.f, 0.f, 0.f, 0.f}; float lw = 0.f;
#pragma unroll
      for (int k = 0; k < 4; ++k) {
        const float e = __builtin_amdgcn_exp2f(__uint_as_float(sv[k].x) - mw);
        const unsigned wlo = pk2(e, 0.f), whi = pk2(0.f, e);
        lw += __uint_as_float(whi) * __uint_as_float(sv[k].y);
#pragma unroll
        for (int d = 0; d < 4; ++d) { acc[2 * d] = dot2(a[k][d], wlo, acc[2 * d]); acc[2 * d + 1] = dot2(a[k][d], whi, acc[2 * d + 1]); }
      }
      const float u0 = rs16(rs32(acc[0], acc[4]), rs32(acc[2], acc[6])), u1 = rs16(rs32(acc[1], acc[5]), rs32(acc[3], acc[7]));
      *(LAS unsigned*)(L + a_w) = pk2(u0, u1);
      lw = x16_sum(lw);
      *(LAS f32x2*)(L + a_s) = f32x2{mw, lw};
    }
    {
      v4u f[8];
      if (w < 4) { tr8(wpre, f); KEEP8(f); }
      const int row = r16 & 7;
      int a_a = L_RED + row * 256 + (((4 * w + g) ^ row) * 16);
      int a_st = L_MISC + 1024 + (g & 1) * 32;
      int a_w2 = L_QIMG + ((w * 2 + (g & 1)) * 128 + 8 * r16) * 4;
      int a_r2 = L_QIMG + ((w & 1) * 128 + 2 * l) * 4, o_x5 = X5 + (idx * 2 + (w & 1)) * 256 + l * 4;
      KEEP(a_a); KEEP(a_st); KEEP(a_w2); KEEP(a_r2); KEEP(o_x5);
      BAR();
      TS(21);
      if (w < 4) {
        const v4u a = *(LAS v4u*)(L + a_a);
        const f32x4 s01 = *(LAS f32x4*)(L + a_st), s23 = *(LAS f32x4*)(L + a_st + 16);
        f32x4 d[8];
#pragma unroll
        for (int i = 0; i < 8; ++i) d[i] = mfma16(a, f[i], f32x4{0.f, 0.f, 0.f, 0.f});
        const float mb = fmaxf(fmaxf(s01[0], s01[2]), fmaxf(s23[0], s23[2]));
        float f0 = __builtin_amdgcn_exp2f(s01[0] - mb), f1 = __builtin_amdgcn_exp2f(s01[2] - mb), f2 = __builtin_amdgcn_exp2f(s23[0] - mb), f3 = __builtin_amdgcn_exp2f(s23[2] - mb);
        const float inv = __builtin_amdgcn_rcpf((f0 * s01[1] + f1 * s01[3]) + (f2 * s23[1] + f3 * s23[3]));
        f0 *= inv; f1 *= inv; f2 *= inv; f3 *= inv;
        float v[8];
#pragma unroll
        for (int i = 0; i < 8; ++i) v[i] = (d[i][0] * f0 + d[i][1] * f1) + (d[i][2] * f2 + d[i][3] * f3);
        if (g < 2) { *(LAS f32x4*)(L + a_w2) = f32x4{v[0], v[1], v[2], v[3]}; *(LAS f32x4*)(L + a_w2 + 16) = f32x4{v[4], v[5], v[6], v[7]}; }
      }
      BAR();
      TS(22);
      if (w < 2) {
        f32x2 v[4];
#pragma unroll
        for (int k = 0; k < 4; ++k) v[k] = *(LAS f32x2*)(L + a_r2 + k * 1024);
        st4(pk2((v[0][0] + v[1][0]) + (v[2][0] + v[3][0]), (v[0][1] + v[1][1]) + (v[2][1] + v[3][1])), o_x5);
        TS(23);
      }
    }
    if (idx < 54) {
      const int kk = (l >> 3) & 1, ch = l & 7;
      v4u wa[3][8], wdn[8];
      if (w < 4) {
        const u16* wsrc = (ch < 4 ? p.wg : p.wu) + 32 * idx + 8 * (ch & 3) + (size_t)(64 * w + 32 * kk + 8 * g) * DF;
#pragma unroll
        for (int k = 0; k < 2; ++k)
#pragma unroll
          for (int j = 0; j < 8; ++j) wa[k][j] = ldnt(wsrc + (size_t)(256 * k + j) * DF);
        if (w < 2) {
#pragma unroll
          for (int j = 0; j < 8; ++j) wa[2][j] = ldnt(wsrc + (size_t)(512 + j) * DF);
        }
      }
      if (w < 5) {
        const u16* src = p.wd + (size_t)(32 * idx + 8 * g) * DM + 128 * w + 8 * r16;
#pragma unroll
        for (int j = 0; j < 8; ++j) wdn[j] = ldnt(src + (size_t)j * DM);
      }
      PIN();
      const int b = w / 3, cc = 32 * (w % 3) + (l & 31), half = l >> 5;
      const bool act_lane = (w < 6) && (cc < 80);
      float x1[4] = {0.f, 0.f, 0.f, 0.f}, cb4[4], sb4[4];
      int a_h2w = L_RED + (b * DM + 8 * cc + 4 * half) * 2;
      if (w < 6) {
        const int ccc = cc < 80 ? cc : 79;
        const int mo = 8 * ccc + 4 * half;
        v2u gav, sbv, cbv;
        for (unsigned sp = 0; sp < SPIN_MAX; ++sp) {
          asm volatile("" ::: "memory");
          gav = ld8(XM + (b * 2560 + mo) * 2); sbv = ld8(XM + (b * 2560 + 640 + mo) * 2); cbv = ld8(XM + (b * 2560 + 1280 + mo) * 2);
          if (__all(umax3(umax3(gav.x, gav.y, sbv.x), umax3(sbv.y, cbv.x, cbv.y), 0u) != SENT)) break;
        }
        const v2u xv = *(const GAS v2u*)(p.x + b * DM + mo);
        float xf[4] = {bflo(xv.x), bfhi(xv.x), bflo(xv.y), bfhi(xv.y)}, ga4[4] = {bflo(gav.x), bfhi(gav.x), bflo(gav.y), bfhi(gav.y)};
        cb4[0] = 1.f + bflo(cbv.x); cb4[1] = 1.f + bfhi(cbv.x); cb4[2] = 1.f + bflo(cbv.y); cb4[3] = 1.f + bfhi(cbv.y);
        sb4[0] = bflo(sbv.x); sb4[1] = bfhi(sbv.x); sb4[2] = bflo(sbv.y); sb4[3] = bfhi(sbv.y);
        if (!act_lane) { ga4[0] = ga4[1] = ga4[2] = ga4[3] = 0.f; xf[0] = xf[1] = xf[2] = xf[3] = 0.f; }
        int off = X6 + ((4 * half * 2 + b) * DM + 8 * ccc) * 2;
        int a_st = L_MISC + w * 8;
        unsigned one_lo = 0x00003F80u, one_hi = 0x3F800000u;
        KEEP(one_lo); KEEP(one_hi);
        KEEP4(xf); KEEP4(ga4); KEEP4(cb4); KEEP4(sb4); KEEP(off); KEEP(a_st); KEEP(a_h2w);
        PIN();
        v4u a[4];
        for (unsigned sp = 0; sp < SPIN_MAX; ++sp) {
          asm volatile("" ::: "memory");
#pragma unroll
          for (int k = 0; k < 4; ++k) a[k] = ld16(off + k * 2560);
          if (__all(umax4a(a[3], umax4a(a[2], umax4a(a[1], umax4(a[0])))) != SENT)) break;
        }
        TS(24);
        float acc[8];
#pragma unroll
        for (int d = 0; d < 4; ++d) {
          acc[2 * d] = dot2(a[3][d], one_lo, dot2(a[2][d], one_lo, dot2(a[1][d], one_lo, bflo(a[0][d]))));
          acc[2 * d + 1] = dot2(a[3][d], one_hi, dot2(a[2][d], one_hi, dot2(a[1][d], one_hi, bfhi(a[0][d]))));
        }
#pragma unroll
        for (int e = 0; e < 4; ++e) x1[e] = xf[e] + ga4[e] * rs32(acc[e], acc[e + 4]);
        const float s1 = wave_sum((x1[0] + x1[1]) + (x1[2] + x1[3]));
        const float s2 = wave_sum((x1[0] * x1[0] + x1[1] * x1[1]) + (x1[2] * x1[2] + x1[3] * x1[3]));
        *(LAS f32x2*)(L + a_st) = f32x2{s1, s2};
      }
      v4u fa[3][8], fd[8];
      if (w < 4) { tr8(wa[0], fa[0]); tr8(wa[1], fa[1]); if (w < 2) tr8(wa[2], fa[2]); }
      int a_st2 = L_MISC + b * 24;
      int a_ar = L_RED + ((r16 & 1) * DM + 64 * w + 32 * (r16 >> 3) + 8 * g) * 2;
      int a_rw = L_QIMG + (((w * 2 + (g >> 1)) * 2) * 64 + 8 * (r16 & 7)) * 4;
      KEEP(a_st2); KEEP(a_ar); KEEP(a_rw);
      BAR();
      TS(25);
      if (act_lane) {
        const f32x2 t0 = *(LAS f32x2*)(L + a_st2), t1 = *(LAS f32x2*)(L + a_st2 + 8), t2 = *(LAS f32x2*)(L + a_st2 + 16);
        const float mean = (t0[0] + t1[0] + t2[0]) * (1.f / DM);
        const float var = fmaxf((t0[1] + t1[1] + t2[1]) * (1.f / DM) - mean * mean, 0.f);
        const float rstd = __builtin_amdgcn_rsqf(var + 1e-5f);
        float hv[4];
#pragma unroll
        for (int e = 0; e < 4; ++e) hv[e] = (x1[e] - mean) * rstd * cb4[e] + sb4[e];
        *(LAS v2u*)(L + a_h2w) = v2u{pk2(hv[0], hv[1]), pk2(hv[2], hv[3])};
      }
      BAR();
      TS(26);
      if (w < 4) {
        const v4u a0 = *(LAS v4u*)(L + a_ar), a1 = *(LAS v4u*)(L + a_ar + 512);
        v4u a2 = a0;
        if (w < 2) a2 = *(LAS v4u*)(L + a_ar + 1024);
        f32x4 acc[8];
#pragma unroll
        for (int i = 0; i < 8; ++i) acc[i] = mfma16(a0, fa[0][i], f32x4{0.f, 0.f, 0.f, 0.f});
#pragma unroll
        for (int i = 0; i < 8; ++i) acc[i] = mfma16(a1, fa[1][i], acc[i]);
        if (w < 2) {
#pragma unroll
          for (int i = 0; i < 8; ++i) acc[i] = mfma16(a2, fa[2][i], acc[i]);
        }
        if ((g == 0 && r16 < 8) || (g == 2 && r16 >= 8)) {
#pragma unroll
          for (int b2 = 0; b2 < 2; ++b2) {
            *(LAS f32x4*)(L + a_rw + b2 * 256) = f32x4{acc[0][b2], acc[1][b2], acc[2][b2], acc[3][b2]};
            *(LAS f32x4*)(L + a_rw + b2 * 256 + 16) = f32x4{acc[4][b2], acc[5][b2], acc[6][b2], acc[7][b2]};
          }
        }
      }
      if (w < 5) { tr8(wdn, fd); KEEP8(fd); }
      int a_gr = L_QIMG + ((l >> 5) * 64 + (l & 31)) * 4, a_aw = L_MISC + 512 + l * 2, a_acr = L_MISC + 512 + ((r16 & 1) * 32 + 8 * g) * 2;
      int o_x7 = X7 + (idx * 2 * DM + 128 * w + 8 * r16) * 2;
      KEEP(a_gr); KEEP(a_aw); KEEP(a_acr); KEEP(o_x7);
      BAR();
      TS(27);
      if (w == 0) {
        float gv[8], uv[8];
#pragma unroll
        for (int k = 0; k < 8; ++k) { gv[k] = *(LAS float*)(L + a_gr + k * 512); uv[k] = *(LAS float*)(L + a_gr + k * 512 + 128); }
        const float gs = ((gv[0] + gv[1]) + (gv[2] + gv[3])) + ((gv[4] + gv[5]) + (gv[6] + gv[7]));
        const float us = ((uv[0] + uv[1]) + (uv[2] + uv[3])) + ((uv[4] + uv[5]) + (uv[6] + uv[7]));
        *(LAS u16*)(L + a_aw) = (u16)pk2(silu(gs) * us, 0.f);
      }
      BAR();
      TS(28);
      if (w < 5) {
        const v4u a = *(LAS v4u*)(L + a_acr);
        f32x4 d[8];
#pragma unroll
        for (int i = 0; i < 8; ++i) d[i] = mfma16(a, fd[i], f32x4{0.f, 0.f, 0.f, 0.f});
        if (g == 0) {
#pragma unroll
          for (int b2 = 0; b2 < 2; ++b2)
            st16(v4u{pk2(d[0][b2], d[1][b2]), pk2(d[2][b2], d[3][b2]), pk2(d[4][b2], d[5][b2]), pk2(d[6][b2], d[7][b2])}, o_x7 + b2 * DM * 2);
        }
        TS(29);
      }
    } else if (idx >= 64 && idx < 84) {
      const int n = idx - 64;
      float base0 = 0.f, base1 = 0.f, gb0 = 0.f, gb1 = 0.f;
      if (w == 7 && l < 32) {
        const int b = l >> 4, pr = l & 15, m = 32 * n + 2 * pr;
        unsigned ga = 0, gb = 0, ap[8];
        for (unsigned sp = 0; sp < SPIN_MAX; ++sp) {
          asm volatile("" ::: "memory");
          ga = ld4(XM + (b * 2560 + m) * 2); gb = ld4(XM + (b * 2560 + 1920 + m) * 2);
#pragma unroll
          for (int k = 0; k < 8; ++k) ap[k] = ld4(X6 + ((k * 2 + b) * DM + m) * 2);
          if (__all(umax3(umax3(umax3(ap[0], ap[1], ap[2]), umax3(ap[3], ap[4], ap[5]), umax3(ap[6], ap[7], ga)), gb, 0u) != SENT)) break;
        }
        const unsigned xd = *(const GAS unsigned*)(p.x + b * DM + m);
        float s0 = 0.f, s1 = 0.f;
#pragma unroll
        for (int k = 0; k < 8; ++k) { s0 += bflo(ap[k]); s1 += bfhi(ap[k]); }
        base0 = bflo(xd) + bflo(ga) * s0; base1 = bfhi(xd) + bfhi(ga) * s1; gb0 = bflo(gb); gb1 = bfhi(gb);
        KEEP(base0); KEEP(base1); KEEP(gb0); KEEP(gb1);
      }
      int a_fr = L_RED + ((l >> 4) * 32 + 2 * (l & 15)) * 4;
      KEEP(a_fr);
      if (w < 7) {
        const int task = 8 * w + (l >> 3), b = (l >> 2) & 1, ch = l & 3;
        const bool valid = task < 54;
        int off = X7 + (((valid ? task : 0) * 2 + b) * DM + 32 * n + 8 * ch) * 2;
        int a_w = L_RED + (w * 64 + b * 32 + 8 * ch + 4 * (l >> 5) + 2 * ((l >> 4) & 1)) * 4;
        unsigned msk = valid ? 0xFFFFFFFFu : 0u;
        KEEP(off); KEEP(a_w); KEEP(msk);
        PIN();
        v4u a;
        for (unsigned sp = 0; sp < SPIN_MAX; ++sp) {
          asm volatile("" ::: "memory");
          a = ld16(off);
          if (__all(umax4(a) != SENT)) break;
        }
        TS(29);
        float v[8];
#pragma unroll
        for (int d = 0; d < 4; ++d) { const unsigned x = a[d] & msk; v[2 * d] = bflo(x); v[2 * d + 1] = bfhi(x); }
        float t0 = rs16(rs32(v[0], v[4]), rs32(v[2], v[6])), t1 = rs16(rs32(v[1], v[5]), rs32(v[3], v[7]));
        t0 += dppf<0x128>(t0); t1 += dppf<0x128>(t1);
        *(LAS f32x2*)(L + a_w) = f32x2{t0, t1};
      }
      BAR();
      if (w == 7 && l < 32) {
        f32x2 v[7];
#pragma unroll
        for (int k = 0; k < 7; ++k) v[k] = *(LAS f32x2*)(L + a_fr + k * 256);
        const float s0 = ((v[0][0] + v[1][0]) + (v[2][0] + v[3][0])) + ((v[4][0] + v[5][0]) + v[6][0]);
        const float s1 = ((v[0][1] + v[1][1]) + (v[2][1] + v[3][1])) + ((v[4][1] + v[5][1]) + v[6][1]);
        *(GAS unsigned*)(p.out + (l >> 4) * DM + 32 * n + 2 * (l & 15)) = pk2(base0 + gb0 * s0, base1 + gb1 * s1);
        if (n == 0 && l == 0) st4(TAG(), OFF_EPOCH);
      }
    }
  }
done: ;
  if (bid == 64) { u64 t0, t1; asm volatile("s_memtime %0\n\ts_waitcnt lgkmcnt(0)" : "=s"(t0) :: "memory"); do { asm volatile("s_memtime %0\n\ts_waitcnt lgkmcnt(0)" : "=s"(t1) :: "memory"); } while (t1 - t0 < 46000ull); }
}

extern "C" void kernel_launch(void* const* d_in, const int* in_sizes, int n_in, void* d_out, int out_size, void* d_ws, size_t ws_size, hipStream_t stream) {
  Params p;
  p.x = (const u16*)d_in[0]; p.ckv = (const u16*)d_in[1]; p.wq = (const u16*)d_in[2]; p.wuk = (const u16*)d_in[3]; p.wuv = (const u16*)d_in[4]; p.wo = (const u16*)d_in[5];
  p.wg = (const u16*)d_in[7]; p.wu = (const u16*)d_in[8]; p.wd = (const u16*)d_in[9]; p.c = (const u16*)d_in[10]; p.wmod = (const u16*)d_in[11];
  p.out = (u16*)d_out; p.ws = (unsigned char*)d_ws;
  mega<<<256, 512, 0, stream>>>(p);
}
```

```cpp
#include <hip/hip_runtime.h>
#include <stdint.h>
#define MEAS 0
#define MEAS_BLK 64
#define WBL2 256

#define GAS __attribute__((address_space(1)))
#define LAS __attribute__((address_space(3)))
typedef unsigned v4u __attribute__((ext_vector_type(4)));
typedef unsigned v2u __attribute__((ext_vector_type(2)));
typedef float f32x4 __attribute__((ext_vector_type(4)));
typedef float f32x2 __attribute__((ext_vector_type(2)));
typedef __bf16 bf16x8 __attribute__((ext_vector_type(8)));
typedef __bf16 bf16x2 __attribute__((ext_vector_type(2)));
typedef short s16x4 __attribute__((ext_vector_type(4)));
typedef unsigned short u16;
typedef unsigned long long u64;

constexpr int NB = 2, NH = 32, SK = 8192, DH = 128, DM = 640, DC = 512, DF = 1728;
constexpr int OFF_EPOCH = 0;
constexpr int OFF_FLAGS = 4096;
constexpr int OFF_EFLAGS = OFF_FLAGS + 256 * 128;
constexpr int OFF_X1 = OFF_EFLAGS + 128 * 128;
constexpr int OFF_R = 81920;
constexpr int X2 = OFF_R;
constexpr int X3 = X2 + 65536;
constexpr int X4 = X3 + 65536;
constexpr int X4S = X4 + 4194304;
constexpr int X5 = X4S + 32768;
constexpr int X6 = X5 + 65536;
constexpr int X7 = X6 + 20480;
constexpr int XM = X7 + 138240;
constexpr int R_END = XM + 10240;
static_assert(OFF_X1 + 20480 <= OFF_R, "map");
constexpr int WS_USED = R_END;
#define CKV_A 0
#define CKV_B 6
constexpr unsigned SENT = 0xFFFFFFFFu;
constexpr int AUX_SC1 = 16;
constexpr unsigned SPIN_MAX = 1u << 18;
constexpr int L_CKV = 0;
constexpr int L_QIMG = 131072;
constexpr int L_PIMG = L_QIMG + 16384;
constexpr int L_RED = L_PIMG + 4096;
constexpr int L_MISC = L_RED + 8192;
constexpr int LDS_BYTES = L_MISC + 2048;
static_assert(LDS_BYTES <= 163840, "LDS");

#define TS(k) do { if ((k) == MEAS) { asm volatile("s_memrealtime %0\n\ts_waitcnt lgkmcnt(0)" : "=s"(meas_t) :: "memory"); } } while (0)

struct Params {
  const u16 *x, *ckv, *wq, *wuk, *wuv, *wo, *wg, *wu, *wd, *c, *wmod;
  u16* out;
  unsigned char* ws;
};

#define BAR() do { asm volatile("s_waitcnt lgkmcnt(0)" ::: "memory"); __builtin_amdgcn_s_barrier(); asm volatile("" ::: "memory"); } while (0)
#define DRAIN() asm volatile("s_waitcnt vmcnt(0)" ::: "memory")
#define PIN() __builtin_amdgcn_sched_barrier(0)
#define KEEP(x) asm volatile("" : "+v"(x))
#define KEEP4(a) do { KEEP(a[0]); KEEP(a[1]); KEEP(a[2]); KEEP(a[3]); } while (0)
#define KEEP8(a) do { KEEP(a[0]); KEEP(a[1]); KEEP(a[2]); KEEP(a[3]); KEEP(a[4]); KEEP(a[5]); KEEP(a[6]); KEEP(a[7]); } while (0)

__device__ __forceinline__ float bflo(unsigned d) { return __uint_as_float(d << 16); }
__device__ __forceinline__ float bfhi(unsigned d) { return __uint_as_float(d & 0xffff0000u); }
__device__ __forceinline__ unsigned pk2(float lo, float hi) { f32x2 f = {lo, hi}; return __builtin_bit_cast(unsigned, __builtin_convertvector(f, bf16x2)); }
__device__ __forceinline__ f32x4 mfma16(v4u a, v4u b, f32x4 c) { return __builtin_amdgcn_mfma_f32_16x16x32_bf16(__builtin_bit_cast(bf16x8, a), __builtin_bit_cast(bf16x8, b), c, 0, 0, 0); }
__device__ __forceinline__ float dot2(unsigned a, unsigned b, float c) { return __builtin_amdgcn_fdot2_f32_bf16(__builtin_bit_cast(bf16x2, a), __builtin_bit_cast(bf16x2, b), c, false); }
__device__ __forceinline__ v4u ldnt(const u16* p) { return __builtin_nontemporal_load((const GAS v4u*)p); }
__device__ __forceinline__ unsigned umax3(unsigned a, unsigned b, unsigned c) { unsigned t = a > b ? a : b; return t > c ? t : c; }
__device__ __forceinline__ unsigned umax4(v4u v) { return umax3(umax3(v.x, v.y, v.z), v.w, 0u); }
__device__ __forceinline__ unsigned umax4a(v4u v, unsigned m) { return umax3(umax3(v.x, v.y, v.z), v.w, m); }
__device__ __forceinline__ float x16_max(float x) {
  auto s = __builtin_amdgcn_permlane16_swap(__float_as_uint(x), __float_as_uint(x), false, false);
  x = fmaxf(__uint_as_float(s[0]), __uint_as_float(s[1]));
  auto t = __builtin_amdgcn_permlane32_swap(__float_as_uint(x), __float_as_uint(x), false, false);
  return fmaxf(__uint_as_float(t[0]), __uint_as_float(t[1]));
}
__device__ __forceinline__ float x16_sum(float x) {
  auto s = __builtin_amdgcn_permlane16_swap(__float_as_uint(x), __float_as_uint(x), false, false);
  x = __uint_as_float(s[0]) + __uint_as_float(s[1]);
  auto t = __builtin_amdgcn_permlane32_swap(__float_as_uint(x), __float_as_uint(x), false, false);
  return __uint_as_float(t[0]) + __uint_as_float(t[1]);
}
__device__ __forceinline__ float rs32(float a, float b) { auto t = __builtin_amdgcn_permlane32_swap(__float_as_uint(a), __float_as_uint(b), false, false); return __uint_as_float(t[0]) + __uint_as_float(t[1]); }
__device__ __forceinline__ float rs16(float a, float b) { auto t = __builtin_amdgcn_permlane16_swap(__float_as_uint(a), __float_as_uint(b), false, false); return __uint_as_float(t[0]) + __uint_as_float(t[1]); }
template <int CTRL> __device__ __forceinline__ float dppf(float x) { return __builtin_bit_cast(float, __builtin_amdgcn_mov_dpp(__builtin_bit_cast(int, x), CTRL, 0xf, 0xf, true)); }
__device__ __forceinline__ float row16_sum(float x) { x += dppf<0xB1>(x); x += dppf<0x4E>(x); x += dppf<0x141>(x); x += dppf<0x128>(x); return x; }
__device__ __forceinline__ float wave_sum(float x) { return x16_sum(row16_sum(x)); }
__device__ __forceinline__ float silu(float v) { return v * __builtin_amdgcn_rcpf(1.f + __builtin_amdgcn_exp2f(v * -1.4426950408889634f)); }

__device__ __forceinline__ void tr8(const v4u (&w)[8], v4u (&f)[8]) {
#pragma unroll
  for (int d = 0; d < 4; ++d) {
#pragma unroll
    for (int i2 = 0; i2 < 4; ++i2) {
      f[2 * i2][d] = __builtin_amdgcn_perm(w[2 * d + 1][i2], w[2 * d][i2], 0x05040100u);
      f[2 * i2 + 1][d] = __builtin_amdgcn_perm(w[2 * d + 1][i2], w[2 * d][i2], 0x07060302u);
    }
  }
}
__device__ __forceinline__ v4u silu8(v4u c) {
  v4u r;
#pragma unroll
  for (int d = 0; d < 4; ++d) r[d] = pk2(silu(bflo(c[d])), silu(bfhi(c[d])));
  return r;
}
__device__ __forceinline__ float sum5(float a, float b, float c, float d, float e) { return ((a + b) + (c + d)) + e; }

__global__ void __launch_bounds__(512, 2) mega(Params p) {
  __shared__ __attribute__((aligned(16))) unsigned char smem[LDS_BYTES];
  LAS unsigned char* L = (LAS unsigned char*)smem;
  const int tid = threadIdx.x, l = tid & 63, w = __builtin_amdgcn_readfirstlane(tid >> 6);
  const int bid = blockIdx.x;
  const int g = l >> 4, r16 = l & 15;
  u64 meas_t0, meas_t; asm volatile("s_memrealtime %0\n\ts_waitcnt lgkmcnt(0)" : "=s"(meas_t0) :: "memory"); meas_t = meas_t0;
  TS(0);
  const __amdgpu_buffer_rsrc_t rs = __builtin_amdgcn_make_buffer_rsrc((void*)p.ws, 0, WS_USED, 0x00020000);
  const bool Ltype = bid >= 128;
  const int idx = bid & 127;
  const int ab = idx >> 6, aj = idx & 63, hg = bid >> 7;
  const unsigned epoch_raw = __builtin_amdgcn_raw_buffer_load_b32(rs, OFF_EPOCH, 0, AUX_SC1);
  auto TAG = [&]() { unsigned t = epoch_raw; asm volatile("" : "+v"(t)); return t + 1u; };

  auto ld16 = [&](int off) { return __builtin_amdgcn_raw_buffer_load_b128(rs, off, 0, AUX_SC1); };
  auto ld8 = [&](int off) { return __builtin_amdgcn_raw_buffer_load_b64(rs, off, 0, AUX_SC1); };
  auto ld4 = [&](int off) { return __builtin_amdgcn_raw_buffer_load_b32(rs, off, 0, AUX_SC1); };
  auto st16 = [&](v4u v, int off) { __builtin_amdgcn_raw_buffer_store_b128(v, rs, off, 0, AUX_SC1); };
  auto st8 = [&](v2u v, int off) { __builtin_amdgcn_raw_buffer_store_b64(v, rs, off, 0, AUX_SC1); };
  auto st4 = [&](unsigned v, int off) { __builtin_amdgcn_raw_buffer_store_b32(v, rs, off, 0, AUX_SC1); };
  auto dma_ckv = [&](int row0, int nrows) {
    const u16* base = p.ckv + ((size_t)ab * SK + aj * 128) * DC;
    for (int rr = 0; rr < nrows; ++rr) {
      const int row = row0 + rr;
      const int sw = ((row & 3) << 2) | ((row >> 2) & 3);
      const u16* src = base + row * DC + ((l ^ sw) * 8);
      unsigned keep; const unsigned dst = (unsigned)(L_CKV + row * 1024) + (unsigned)(size_t)L;
      asm volatile("s_mov_b32 %0, m0\n\ts_mov_b32 m0, %2\n\ts_nop 0\n\tglobal_load_lds_dwordx4 %1, off nt\n\ts_mov_b32 m0, %0" : "=&s"(keep) : "v"(src), "s"(dst) : "memory");
    }
  };
  const v4u SENT4 = {SENT, SENT, SENT, SENT};
  auto fill_x4 = [&]() {
#pragma unroll
    for (int k = 0; k < 2; ++k) st16(SENT4, X4 + ((ab * NH + 16 * hg + 2 * w + k) * 64 + aj) * 1024 + l * 16);
    if (w == 0 && l < 8) st16(SENT4, X4S + ((ab * 64 + aj) * NH + 16 * hg) * 8 + l * 16);
  };
  auto check_flags = [&](int off, int nper) {
    const unsigned tag = TAG();
    for (unsigned sp = 0; sp < SPIN_MAX; ++sp) {
      asm volatile("" ::: "memory");
      bool ok = true;
      for (int k = 0; k < nper; ++k) ok &= ld4(off + (k * 64 + l) * 128) == tag;
      if (__all(ok)) break;
      __builtin_amdgcn_s_sleep(1);
    }
  };

  auto attention = [&](auto&& hook_post) {
    DRAIN();
    BAR();
    TS(11);
    if (w == 7 && l == 0) st4(TAG(), OFF_FLAGS + bid * 128);
    v4u qk[16], pv[16];
    {
      const int key = 16 * w + r16;
      const int sw = ((key & 3) << 2) | ((key >> 2) & 3);
#pragma unroll
      for (int s = 0; s < 16; ++s) qk[s] = *(LAS v4u*)(L + L_CKV + key * 1024 + (s >> 2) * 256 + (((4 * (s & 3) + g) ^ sw) * 16));
      const int q4 = r16 >> 2, p4 = l & 3;
#pragma unroll
      for (int s = 0; s < 4; ++s)
#pragma unroll
        for (int t = 0; t < 4; ++t) {
          v2u h2[2];
#pragma unroll
          for (int hf = 0; hf < 2; ++hf) {
            const int ky = 32 * s + 16 * hf + 4 * g + q4;
            const int sw2 = (q4 << 2) | g;
            const int ch = 8 * w + 4 * (t >> 1) + p4;
            const s16x4 v = __builtin_amdgcn_ds_read_tr16_b64_v4i16((LAS s16x4*)(L + L_CKV + ky * 1024 + ((ch ^ sw2) * 16) + 8 * (t & 1)));
            h2[hf] = __builtin_bit_cast(v2u, v);
          }
          pv[s * 4 + t] = v4u{h2[0].x, h2[0].y, h2[1].x, h2[1].y};
        }
    }
    int a_qw0 = L_QIMG + w * 1024 + ((l ^ w) * 16), a_qw1 = L_QIMG + (8 + w) * 1024 + ((l ^ (8 + w)) * 16);
    int a_qr[4];
#pragma unroll
    for (int t = 0; t < 4; ++t) a_qr[t] = L_QIMG + r16 * 1024 + (((4 * t + g) ^ r16) * 16);
    int a_mxw = L_MISC + r16 * 32 + w * 4, a_mxr = L_MISC + r16 * 32;
    int a_pw = L_PIMG + (w >> 1) * 1024 + l * 16 + (w & 1) * 8, a_pr = L_PIMG + l * 16;
    const int hd = 16 * hg + r16;
    int o_st = X4 + (((ab * NH + hd) * 64 + aj) * 512 + 64 * w + 8 * g) * 2, o_ss = X4S + ((ab * 64 + aj) * NH + hd) * 8;
    int o_q = X3 + (ab * NH + 16 * hg) * 1024 + tid * 16;
    KEEP(a_qw0); KEEP(a_qw1); KEEP4(a_qr); KEEP(a_mxw); KEEP(a_mxr); KEEP(a_pw); KEEP(a_pr); KEEP(o_st); KEEP(o_ss); KEEP(o_q);
#pragma unroll
    for (int s = 0; s < 16; ++s) { KEEP(qk[s]); KEEP(pv[s]); }
    TS(12);
    PIN();
    {
      v4u a0, a1;
      for (unsigned sp = 0; sp < SPIN_MAX; ++sp) {
        asm volatile("" ::: "memory");
        a0 = ld16(o_q); a1 = ld16(o_q + 8192);
        if (__all(umax4a(a1, umax4(a0)) != SENT)) break;
      }
      *(LAS v4u*)(L + a_qw0) = a0; *(LAS v4u*)(L + a_qw1) = a1;
    }
    TS(13);
    unsigned lf[4] = {0u, 0u, 0u, 0u};
    if (w == 6) {
#pragma unroll
      for (int k = 0; k < 4; ++k) lf[k] = ld4(OFF_FLAGS + (k * 64 + l) * 128);
    }
    PIN();
    BAR();
    TS(14);
    f32x4 sacc = {0.f, 0.f, 0.f, 0.f}, sacc2 = {0.f, 0.f, 0.f, 0.f};
    {
      v4u qf[16];
#pragma unroll
      for (int s = 0; s < 16; ++s) qf[s] = *(LAS v4u*)(L + a_qr[s & 3] + (s >> 2) * 256);
      PIN();
#pragma unroll
      for (int s = 0; s < 16; s += 2) { sacc = mfma16(qk[s], qf[s], sacc); sacc2 = mfma16(qk[s + 1], qf[s + 1], sacc2); }
    }
    sacc += sacc2;
    const float mloc = x16_max(fmaxf(fmaxf(sacc[0], sacc[1]), fmaxf(sacc[2], sacc[3])));
    *(LAS float*)(L + a_mxw) = mloc;
    TS(15);
    BAR();
    TS(16);
    const f32x4 m0 = *(LAS f32x4*)(L + a_mxr), m1 = *(LAS f32x4*)(L + a_mxr + 16);
    const float m = fmaxf(fmaxf(fmaxf(m0[0], m0[1]), fmaxf(m0[2], m0[3])), fmaxf(fmaxf(m1[0], m1[1]), fmaxf(m1[2], m1[3])));
    const unsigned d0 = pk2(__builtin_amdgcn_exp2f(sacc[0] - m), __builtin_amdgcn_exp2f(sacc[1] - m)), d1 = pk2(__builtin_amdgcn_exp2f(sacc[2] - m), __builtin_amdgcn_exp2f(sacc[3] - m));
    *(LAS v2u*)(L + a_pw) = v2u{d0, d1};
    BAR();
    TS(17);
    f32x4 o[4], lacc = {0.f, 0.f, 0.f, 0.f};
#pragma unroll
    for (int t = 0; t < 4; ++t) o[t] = f32x4{0.f, 0.f, 0.f, 0.f};
    v4u pf[4];
#pragma unroll
    for (int s = 0; s < 4; ++s) pf[s] = *(LAS v4u*)(L + a_pr + s * 1024);
    PIN();
#pragma unroll
    for (int s = 0; s < 4; ++s)
#pragma unroll
      for (int t = 0; t < 4; ++t) o[t] = mfma16(pv[s * 4 + t], pf[s], o[t]);
    st16(v4u{pk2(o[0][0], o[0][1]), pk2(o[0][2], o[0][3]), pk2(o[1][0], o[1][1]), pk2(o[1][2], o[1][3])}, o_st);
    st16(v4u{pk2(o[2][0], o[2][1]), pk2(o[2][2], o[2][3]), pk2(o[3][0], o[3][1]), pk2(o[3][2], o[3][3])}, o_st + 64);
    if (w == 0) {
      const v4u ones = {0x3F803F80u, 0x3F803F80u, 0x3F803F80u, 0x3F803F80u};
#pragma unroll
      for (int s = 0; s < 4; ++s) lacc = mfma16(ones, pf[s], lacc);
      if (g == 0) st8(v2u{__float_as_uint(m), __float_as_uint(lacc[0])}, o_ss);
    }
    TS(18);
    PIN();
    hook_post();
    PIN();
    if (w == 6) {
      const unsigned tag = TAG();
      if (!__all((lf[0] == tag) & (lf[1] == tag) & (lf[2] == tag) & (lf[3] == tag))) check_flags(OFF_FLAGS, 4);
    }
    BAR();
    TS(19);
  };

  if (Ltype) {
    const int hh = idx >> 2, sub = idx & 3;
    const bool t1role = idx < 40;
    const int mg = idx % 10, pp1 = idx / 10;
    v4u uk[4];
    auto load_t3 = [&]() {
      const u16* src = p.wuk + ((size_t)(128 * sub + 16 * w + r16) * NH + hh) * DH + 8 * g;
#pragma unroll
      for (int s = 0; s < 4; ++s) uk[s] = ldnt(src + 32 * s);
    };
    auto stage_t3 = [&]() {
      if (CKV_B > 0) dma_ckv(16 * w, CKV_B);
      const int kq = (r16 & 7) >> 1, b = r16 & 1;
      int off = X2 + ((hh * 4 + kq) * 2 + b) * 256 + 16 * g;
      int a_w = L_RED + ((g & 1) * 128 + 16 * w + r16) * 2;
      int o_x3 = X3 + ((l >> 4) * NH + hh) * 1024 + sub * 256 + (l & 15) * 16;
      KEEP(off); KEEP(a_w); KEEP(o_x3);
      PIN();
      v4u a[4];
      for (unsigned sp = 0; sp < SPIN_MAX; ++sp) {
        asm volatile("" ::: "memory");
#pragma unroll
        for (int s = 0; s < 4; ++s) a[s] = ld16(off + 64 * s);
        if (__all(umax4a(a[3], umax4a(a[2], umax4a(a[1], umax4(a[0])))) != SENT)) break;
      }
      TS(9);
      f32x4 acc = mfma16(a[0], uk[0], f32x4{0.f, 0.f, 0.f, 0.f}), acc2 = mfma16(a[1], uk[1], f32x4{0.f, 0.f, 0.f, 0.f});
      acc = mfma16(a[2], uk[2], acc); acc2 = mfma16(a[3], uk[3], acc2);
      acc += acc2;
      const float sc = 0.08838834764831845f * 1.4426950408889634f;
      const float tot = rs16(acc[0] + acc[2], acc[1] + acc[3]) * sc;
      if (g < 2) *(LAS u16*)(L + a_w) = (u16)pk2(tot, tot);
      BAR();
      if (w == 0 && l < 32) {
        st16(*(LAS v4u*)(L + L_RED + l * 16), o_x3);
        TS(10);
      }
    };
    if (w < 5) {
      v4u wv2[8];
      const u16* src2 = p.wq + (size_t)(160 * sub + 32 * w + 8 * g) * (NH * DH) + hh * DH + 8 * r16;
      if (t1role) {
        v4u wv[8];
        const int col = (r16 < 8) ? (64 * mg + 8 * r16) : (DM + 64 * mg + 8 * (r16 - 8));
        const int k0 = 160 * pp1 + 32 * w + 8 * g;
        const v4u cv = *(const GAS v4u*)(p.c + (r16 & 1) * DM + k0);
        const unsigned xd = *(const GAS unsigned*)(p.x + (l >> 5) * DM + 64 * mg + 2 * (l & 31));
        const u16* src = p.wmod + (size_t)k0 * (6 * DM) + col;
#pragma unroll
        for (int j = 0; j < 8; ++j) wv[j] = ldnt(src + (size_t)j * (6 * DM));
        int a_w = L_RED + ((w * 2) * 128 + 8 * r16) * 4;
        int a_r = L_RED + ((l >> 5) * 128 + 2 * (l & 31)) * 4;
        int o_x1 = OFF_X1 + (((pp1 * 2 + (l >> 5)) * 320) + 32 * mg + (l & 31)) * 8;
        KEEP(a_w); KEEP(a_r); KEEP(o_x1);
        PIN();
        v4u a = silu8(cv);
        float xlo = bflo(xd), xhi = bfhi(xd);
        unsigned tag = TAG();
        KEEP(a); KEEP(xlo); KEEP(xhi); KEEP(tag);
        PIN();
        v4u f[8]; tr8(wv, f);
        KEEP8(f);
        TS(1);
        PIN();
#pragma unroll
        for (int j = 0; j < 8; ++j) wv2[j] = ldnt(src2 + (size_t)j * (NH * DH));
        PIN();
        f32x4 acc[8];
#pragma unroll
        for (int i = 0; i < 8; ++i) acc[i] = mfma16(a, f[i], f32x4{0.f, 0.f, 0.f, 0.f});
        if (g == 0) {
#pragma unroll
          for (int b = 0; b < 2; ++b) {
            *(LAS f32x4*)(L + a_w + b * 512) = f32x4{acc[0][b], acc[1][b], acc[2][b], acc[3][b]};
            *(LAS f32x4*)(L + a_w + b * 512 + 16) = f32x4{acc[4][b], acc[5][b], acc[6][b], acc[7][b]};
          }
        }
        TS(2);
        BAR();
        TS(3);
        if (w == 0) {
          f32x2 s[5], c2[5];
#pragma unroll
          for (int ww = 0; ww < 5; ++ww) { s[ww] = *(LAS f32x2*)(L + a_r + ww * 1024); c2[ww] = *(LAS f32x2*)(L + a_r + ww * 1024 + 256); }
          const f32x2 mr = *(LAS f32x2*)(L + L_MISC + (l >> 5) * 8);
          const float xn0 = (xlo - mr[0]) * mr[1], xn1 = (xhi - mr[0]) * mr[1];
          const float one = pp1 == 0 ? 1.f : 0.f;
          const float o0 = xn0 * (sum5(c2[0][0], c2[1][0], c2[2][0], c2[3][0], c2[4][0]) + one) + sum5(s[0][0], s[1][0], s[2][0], s[3][0], s[4][0]);
          const float o1 = xn1 * (sum5(c2[0][1], c2[1][1], c2[2][1], c2[3][1], c2[4][1]) + one) + sum5(s[0][1], s[1][1], s[2][1], s[3][1], s[4][1]);
          st8(v2u{pk2(o0, o1), tag}, o_x1);
          TS(4);
        }
      } else {
#pragma unroll
        for (int j = 0; j < 8; ++j) wv2[j] = ldnt(src2 + (size_t)j * (NH * DH));
      }
      load_t3();
      PIN();
      {
        unsigned tag = TAG();
        v4u f[8]; tr8(wv2, f);
        const int pp = (r16 & 7) >> 1, b = r16 & 1;
        int off = OFF_X1 + ((pp * 2 + b) * 320 + (160 * sub + 32 * w + 8 * g) / 2) * 8;
        int a_w = L_QIMG + (((w * 2 + (g & 1)) * 2) * 128 + 8 * r16) * 4;
        int a_r = L_QIMG + ((w & 1) * 128 + 2 * l) * 4, o_x2 = X2 + ((hh * 4 + sub) * 2 + (w & 1)) * 256 + l * 4;
        KEEP8(f); KEEP(off); KEEP(a_w); KEEP(tag); KEEP(a_r); KEEP(o_x2);
        TS(5);
        PIN();
        v4u a;
        for (unsigned sp = 0; sp < SPIN_MAX; ++sp) {
          asm volatile("" ::: "memory");
          const v4u u0 = ld16(off), u1 = ld16(off + 16);
          a = v4u{u0.x, u0.z, u1.x, u1.z};
          if (__all(((u0.y ^ tag) | (u0.w ^ tag) | (u1.y ^ tag) | (u1.w ^ tag)) == 0u)) break;
        }
        TS(6);
        f32x4 acc[8];
#pragma unroll
        for (int i = 0; i < 8; ++i) acc[i] = mfma16(a, f[i], f32x4{0.f, 0.f, 0.f, 0.f});
        if (g < 2) {
#pragma unroll
          for (int b2 = 0; b2 < 2; ++b2) {
            *(LAS f32x4*)(L + a_w + b2 * 512) = f32x4{acc[0][b2] + acc[0][b2 + 2], acc[1][b2] + acc[1][b2 + 2], acc[2][b2] + acc[2][b2 + 2], acc[3][b2] + acc[3][b2 + 2]};
            *(LAS f32x4*)(L + a_w + b2 * 512 + 16) = f32x4{acc[4][b2] + acc[4][b2 + 2], acc[5][b2] + acc[5][b2 + 2], acc[6][b2] + acc[6][b2 + 2], acc[7][b2] + acc[7][b2 + 2]};
          }
        }
        BAR();
        TS(7);
        if (w < 2) {
          f32x2 v[10];
#pragma unroll
          for (int k = 0; k < 10; ++k) v[k] = *(LAS f32x2*)(L + a_r + k * 1024);
          const float s0 = sum5(v[0][0], v[1][0], v[2][0], v[3][0], v[4][0]) + sum5(v[5][0], v[6][0], v[7][0], v[8][0], v[9][0]);
          const float s1 = sum5(v[0][1], v[1][1], v[2][1], v[3][1], v[4][1]) + sum5(v[5][1], v[6][1], v[7][1], v[8][1], v[9][1]);
          st4(pk2(s0, s1), o_x2);
          TS(8);
        }
      }
      stage_t3();
    } else {
      if (w == 7) {
        if (l < 32) st16(SENT4, X2 + (hh * 4 + sub) * 512 + l * 16);
        else st16(SENT4, X3 + (((l - 32) >> 4) * NH + hh) * 1024 + sub * 256 + (l & 15) * 16);
        DRAIN();
        if (l == 0) st4(TAG(), OFF_EFLAGS + idx * 128);
      } else if (t1role) {
        const int b = w - 5;
        float v[10]; float s = 0.f;
#pragma unroll
        for (int k = 0; k < 5; ++k) { const unsigned d = *(const GAS unsigned*)(p.x + b * DM + 2 * (l + 64 * k)); v[2 * k] = bflo(d); v[2 * k + 1] = bfhi(d); s += v[2 * k] + v[2 * k + 1]; }
        const float mean = wave_sum(s) * (1.f / DM);
        float q = 0.f;
#pragma unroll
        for (int k = 0; k < 10; ++k) { const float d = v[k] - mean; q += d * d; }
        const float var = wave_sum(q) * (1.f / DM);
        *(LAS f32x2*)(L + L_MISC + b * 8) = f32x2{mean, __builtin_amdgcn_rsqf(var + 1e-5f)};
      }
      if (t1role) BAR();
      {
        const int h0 = (w - 5) * 6, nh = w == 7 ? 4 : 6;
        for (int k = 0; k < nh; ++k) st16(SENT4, X4 + ((ab * NH + 16 * hg + h0 + k) * 64 + aj) * 1024 + l * 16);
        if (w == 7) {
          if (l < 8) st16(SENT4, X4S + ((ab * 64 + aj) * NH + 16 * hg) * 8 + l * 16);
          else if (l >= 16 && l < 32) {
            const int u = l - 16;
            if (idx < 80) st16(SENT4, X6 + (((idx / 10) * 2 + (u >> 3)) * DM + 64 * (idx % 10)) * 2 + (u & 7) * 16);
            else if (idx < 120) st16(SENT4, XM + ((u >> 3) * 2560 + 64 * (idx - 80)) * 2 + (u & 7) * 16);
          }
        }
      }
      load_t3();
      if (w == 6) check_flags(OFF_EFLAGS, 2);
      BAR();
      stage_t3();
    }
    dma_ckv(16 * w + CKV_B, 16 - CKV_B);
    v4u wpre[3][8];
    attention([&]() {
      if (w < 4) {
        const int kk = (l >> 3) & 1, ch = l & 7;
        if (idx < 80) {
          const int pp = idx / 10, n = idx % 10;
          const u16* src = p.wo + (size_t)(512 * pp + 64 * w + 32 * kk + 8 * g) * DM + 64 * n + 8 * ch;
#pragma unroll
          for (int k = 0; k < 2; ++k)
#pragma unroll
            for (int j = 0; j < 8; ++j) wpre[k][j] = ldnt(src + (size_t)(256 * k + j) * DM);
        } else if (idx < 120) {
          const int tb = idx - 80;
          const u16* src = p.wmod + (size_t)(64 * w + 32 * kk + 8 * g) * (6 * DM) + 2 * DM + 64 * tb + 8 * ch;
#pragma unroll
          for (int k = 0; k < 2; ++k)
#pragma unroll
            for (int j = 0; j < 8; ++j) wpre[k][j] = ldnt(src + (size_t)(256 * k + j) * (6 * DM));
          if (w < 2) {
#pragma unroll
            for (int j = 0; j < 8; ++j) wpre[2][j] = ldnt(src + (size_t)(512 + j) * (6 * DM));
          }
        }
      }
    });
    LAS float* red = (LAS float*)(L + L_RED);
    if (idx < 80) {
      const int pp = idx / 10, n = idx % 10;
      if (w < 4) {
        v4u f0[8], f1[8]; tr8(wpre[0], f0); tr8(wpre[1], f1);
        const int rho = r16 & 7, cq = rho >> 1, b = rho & 1, kk2 = r16 >> 3;
        int off = X5 + (((4 * pp + (w >> 1)) * 4 + cq) * 2 + b) * 256 + (64 * (w & 1) + 32 * kk2 + 8 * g) * 2;
        int a_w = L_RED + (((w * 2 + (g >> 1)) * 2 + (g & 1)) * 64 + 8 * (r16 & 7)) * 4;
        KEEP8(f0); KEEP8(f1); KEEP(off); KEEP(a_w);
        PIN();
        v4u a0, a1;
        for (unsigned sp = 0; sp < SPIN_MAX; ++sp) {
          asm volatile("" ::: "memory");
          a0 = ld16(off); a1 = ld16(off + 2 * 4 * 2 * 256);
          if (__all(umax4a(a1, umax4(a0)) != SENT)) break;
        }
        TS(24);
        f32x4 d[8];
#pragma unroll
        for (int i = 0; i < 8; ++i) d[i] = mfma16(a0, f0[i], f32x4{0.f, 0.f, 0.f, 0.f});
#pragma unroll
        for (int i = 0; i < 8; ++i) d[i] = mfma16(a1, f1[i], d[i]);
        float s[8];
#pragma unroll
        for (int i = 0; i < 8; ++i) s[i] = rs16(d[i][0] + d[i][2], d[i][1] + d[i][3]);
        if ((g >> 1) == (r16 >> 3)) {
          *(LAS f32x4*)(L + a_w) = f32x4{s[0], s[1], s[2], s[3]}; *(LAS f32x4*)(L + a_w + 16) = f32x4{s[4], s[5], s[6], s[7]};
        }
      }
      int a_r = L_RED + ((l >> 5) * 64 + 2 * (l & 31)) * 4, o_x6 = X6 + ((pp * 2 + (l >> 5)) * DM + 64 * n + 2 * (l & 31)) * 2;
      KEEP(a_r); KEEP(o_x6);
      BAR();
      TS(25);
      if (w == 0) {
        f32x2 v[8];
#pragma unroll
        for (int k = 0; k < 8; ++k) v[k] = *(LAS f32x2*)(L + a_r + k * 512);
        const float s0 = ((v[0][0] + v[1][0]) + (v[2][0] + v[3][0])) + ((v[4][0] + v[5][0]) + (v[6][0] + v[7][0]));
        const float s1 = ((v[0][1] + v[1][1]) + (v[2][1] + v[3][1])) + ((v[4][1] + v[5][1]) + (v[6][1] + v[7][1]));
        st4(pk2(s0, s1), o_x6);
        TS(29);
      }
    } else if (idx < 120) {
      const int tb = idx - 80;
      if (w < 4) {
        f32x4 acc[8];
#pragma unroll
        for (int i = 0; i < 8; ++i) acc[i] = f32x4{0.f, 0.f, 0.f, 0.f};
#pragma unroll
        for (int k = 0; k < 3; ++k) {
          if (k < 2 || w < 2) {
            const v4u cv = *(const GAS v4u*)(p.c + (r16 & 1) * DM + 64 * (w + 4 * k) + 32 * (r16 >> 3) + 8 * g);
            const v4u a = silu8(cv);
            v4u f[8];
            tr8(wpre[k], f);
#pragma unroll
            for (int i = 0; i < 8; ++i) acc[i] = mfma16(a, f[i], acc[i]);
          }
        }
        if ((g == 0 && r16 < 8) || (g == 2 && r16 >= 8)) {
#pragma unroll
          for (int b2 = 0; b2 < 2; ++b2) {
            LAS float* d = red + ((w * 2 + (g >> 1)) * 2 + b2) * 64 + 8 * (r16 & 7);
            *(LAS f32x4*)d = f32x4{acc[0][b2], acc[1][b2], acc[2][b2], acc[3][b2]}; *(LAS f32x4*)(d + 4) = f32x4{acc[4][b2], acc[5][b2], acc[6][b2], acc[7][b2]};
          }
        }
      }
      BAR();
      if (w == 0) {
        const int b = l >> 5, pr = l & 31;
        float s0 = 0.f, s1 = 0.f;
#pragma unroll
        for (int k = 0; k < 8; ++k) { const f32x2 v = *(LAS f32x2*)(red + (k * 2 + b) * 64 + 2 * pr); s0 += v[0]; s1 += v[1]; }
        st4(pk2(s0, s1), XM + (b * 2560 + 64 * tb + 2 * pr) * 2);
        TS(29);
      }
    }
  } else {
    fill_x4();
    if (w == 1 && l < 32) st16(SENT4, X5 + idx * 512 + l * 16);
    if (idx < 54 && w >= 2 && w < 5) { const int u = (w - 2) * 64 + l; if (u < 160) st16(SENT4, X7 + idx * 2560 + u * 16); }
    dma_ckv(16 * w, 16);
    if (w == 6) check_flags(OFF_EFLAGS, 2);
    const int h5 = idx >> 2, cq = idx & 3;
    v4u wpre[8];
    attention([&]() {
      if (w < 4) {
        const u16* src = p.wuv + ((size_t)(128 * cq + 32 * w + 8 * g) * NH + h5) * DH + 8 * r16;
#pragma unroll
        for (int j = 0; j < 8; ++j) wpre[j] = ldnt(src + (size_t)j * (NH * DH));
      }
    });
    {
      const int b = w >> 2;
      int off = X4 + (((b * NH + h5) * 64 + 16 * (w & 3) + g) * 512 + 128 * cq + 8 * r16) * 2;
      int offs = X4S + ((b * 64 + 16 * (w & 3) + g) * NH + h5) * 8;
      const int cofs = 8 * r16 + 4 * (l >> 5) + 2 * ((l >> 4) & 1);
      int a_w = L_RED + w * 256 + (((cofs >> 3) ^ w) * 16) + (cofs & 7) * 2;
      int a_s = L_MISC + 1024 + w * 8;
      KEEP(off); KEEP(offs); KEEP(a_w); KEEP(a_s);
      PIN();
      v4u a[4]; v2u sv[4];
      for (unsigned sp = 0; sp < SPIN_MAX; ++sp) {
        asm volatile("" ::: "memory");
#pragma unroll
        for (int k = 0; k < 4; ++k) { sv[k] = ld8(offs + k * 1024); a[k] = ld16(off + k * 4096); }
        unsigned m = umax3(umax3(sv[0].x, sv[0].y, sv[1].x), umax3(sv[1].y, sv[2].x, sv[2].y), umax3(sv[3].x, sv[3].y, 0u));
        m = umax4a(a[1], umax4a(a[0], m)); m = umax4a(a[3], umax4a(a[2], m));
        if (__all(m != SENT)) break;
      }
      TS(20);
      float mw = fmaxf(fmaxf(__uint_as_float(sv[0].x), __uint_as_float(sv[1].x)), fmaxf(__uint_as_float(sv[2].x), __uint_as_float(sv[3].x)));
      mw = x16_max(mw);
      float acc[8] = {0.f, 0.f, 0.f, 0.f, 0.f, 0.f, 0.f, 0.f}; float lw = 0.f;
#pragma unroll
      for (int k = 0; k < 4; ++k) {
        const float e = __builtin_amdgcn_exp2f(__uint_as_float(sv[k].x) - mw);
        const unsigned wlo = pk2(e, 0.f), whi = pk2(0.f, e);
        lw += __uint_as_float(whi) * __uint_as_float(sv[k].y);
#pragma unroll
        for (int d = 0; d < 4; ++d) { acc[2 * d] = dot2(a[k][d], wlo, acc[2 * d]); acc[2 * d + 1] = dot2(a[k][d], whi, acc[2 * d + 1]); }
      }
      const float u0 = rs16(rs32(acc[0], acc[4]), rs32(acc[2], acc[6])), u1 = rs16(rs32(acc[1], acc[5]), rs32(acc[3], acc[7]));
      *(LAS unsigned*)(L + a_w) = pk2(u0, u1);
      lw = x16_sum(lw);
      *(LAS f32x2*)(L + a_s) = f32x2{mw, lw};
    }
    {
      v4u f[8];
      if (w < 4) { tr8(wpre, f); KEEP8(f); }
      const int row = r16 & 7;
      int a_a = L_RED + row * 256 + (((4 * w + g) ^ row) * 16);
      int a_st = L_MISC + 1024 + (g & 1) * 32;
      int a_w2 = L_QIMG + ((w * 2 + (g & 1)) * 128 + 8 * r16) * 4;
      int a_r2 = L_QIMG + ((w & 1) * 128 + 2 * l) * 4, o_x5 = X5 + (idx * 2 + (w & 1)) * 256 + l * 4;
      KEEP(a_a); KEEP(a_st); KEEP(a_w2); KEEP(a_r2); KEEP(o_x5);
      BAR();
      TS(21);
      if (w < 4) {
        const v4u a = *(LAS v4u*)(L + a_a);
        const f32x4 s01 = *(LAS f32x4*)(L + a_st), s23 = *(LAS f32x4*)(L + a_st + 16);
        f32x4 d[8];
#pragma unroll
        for (int i = 0; i < 8; ++i) d[i] = mfma16(a, f[i], f32x4{0.f, 0.f, 0.f, 0.f});
        const float mb = fmaxf(fmaxf(s01[0], s01[2]), fmaxf(s23[0], s23[2]));
        float f0 = __builtin_amdgcn_exp2f(s01[0] - mb), f1 = __builtin_amdgcn_exp2f(s01[2] - mb), f2 = __builtin_amdgcn_exp2f(s23[0] - mb), f3 = __builtin_amdgcn_exp2f(s23[2] - mb);
        const float inv = __builtin_amdgcn_rcpf((f0 * s01[1] + f1 * s01[3]) + (f2 * s23[1] + f3 * s23[3]));
        f0 *= inv; f1 *= inv; f2 *= inv; f3 *= inv;
        float v[8];
#pragma unroll
        for (int i = 0; i < 8; ++i) v[i] = (d[i][0] * f0 + d[i][1] * f1) + (d[i][2] * f2 + d[i][3] * f3);
        if (g < 2) { *(LAS f32x4*)(L + a_w2) = f32x4{v[0], v[1], v[2], v[3]}; *(LAS f32x4*)(L + a_w2 + 16) = f32x4{v[4], v[5], v[6], v[7]}; }
      }
      BAR();
      TS(22);
      if (w < 2) {
        f32x2 v[4];
#pragma unroll
        for (int k = 0; k < 4; ++k) v[k] = *(LAS f32x2*)(L + a_r2 + k * 1024);
        st4(pk2((v[0][0] + v[1][0]) + (v[2][0] + v[3][0]), (v[0][1] + v[1][1]) + (v[2][1] + v[3][1])), o_x5);
        TS(23);
      }
    }
    if (idx < 54) {
      const int kk = (l >> 3) & 1, ch = l & 7;
      v4u wa[3][8], wdn[8];
      if (w < 4) {
        const u16* wsrc = (ch < 4 ? p.wg : p.wu) + 32 * idx + 8 * (ch & 3) + (size_t)(64 * w + 32 * kk + 8 * g) * DF;
#pragma unroll
        for (int k = 0; k < 2; ++k)
#pragma unroll
          for (int j = 0; j < 8; ++j) wa[k][j] = ldnt(wsrc + (size_t)(256 * k + j) * DF);
        if (w < 2) {
#pragma unroll
          for (int j = 0; j < 8; ++j) wa[2][j] = ldnt(wsrc + (size_t)(512 + j) * DF);
        }
      }
      if (w < 5) {
        const u16* src = p.wd + (size_t)(32 * idx + 8 * g) * DM + 128 * w + 8 * r16;
#pragma unroll
        for (int j = 0; j < 8; ++j) wdn[j] = ldnt(src + (size_t)j * DM);
      }
      PIN();
      const int b = w / 3, cc = 32 * (w % 3) + (l & 31), half = l >> 5;
      const bool act_lane = (w < 6) && (cc < 80);
      float x1[4] = {0.f, 0.f, 0.f, 0.f}, cb4[4], sb4[4];
      int a_h2w = L_RED + (b * DM + 8 * cc + 4 * half) * 2;
      if (w < 6) {
        const int ccc = cc < 80 ? cc : 79;
        const int mo = 8 * ccc + 4 * half;
        v2u gav, sbv, cbv;
        for (unsigned sp = 0; sp < SPIN_MAX; ++sp) {
          asm volatile("" ::: "memory");
          gav = ld8(XM + (b * 2560 + mo) * 2); sbv = ld8(XM + (b * 2560 + 640 + mo) * 2); cbv = ld8(XM + (b * 2560 + 1280 + mo) * 2);
          if (__all(umax3(umax3(gav.x, gav.y, sbv.x), umax3(sbv.y, cbv.x, cbv.y), 0u) != SENT)) break;
        }
        const v2u xv = *(const GAS v2u*)(p.x + b * DM + mo);
        float xf[4] = {bflo(xv.x), bfhi(xv.x), bflo(xv.y), bfhi(xv.y)}, ga4[4] = {bflo(gav.x), bfhi(gav.x), bflo(gav.y), bfhi(gav.y)};
        cb4[0] = 1.f + bflo(cbv.x); cb4[1] = 1.f + bfhi(cbv.x); cb4[2] = 1.f + bflo(cbv.y); cb4[3] = 1.f + bfhi(cbv.y);
        sb4[0] = bflo(sbv.x); sb4[1] = bfhi(sbv.x); sb4[2] = bflo(sbv.y); sb4[3] = bfhi(sbv.y);
        if (!act_lane) { ga4[0] = ga4[1] = ga4[2] = ga4[3] = 0.f; xf[0] = xf[1] = xf[2] = xf[3] = 0.f; }
        int off = X6 + ((4 * half * 2 + b) * DM + 8 * ccc) * 2;
        int a_st = L_MISC + w * 8;
        unsigned one_lo = 0x00003F80u, one_hi = 0x3F800000u;
        KEEP(one_lo); KEEP(one_hi);
        KEEP4(xf); KEEP4(ga4); KEEP4(cb4); KEEP4(sb4); KEEP(off); KEEP(a_st); KEEP(a_h2w);
        PIN();
        v4u a[4];
        for (unsigned sp = 0; sp < SPIN_MAX; ++sp) {
          asm volatile("" ::: "memory");
#pragma unroll
          for (int k = 0; k < 4; ++k) a[k] = ld16(off + k * 2560);
          if (__all(umax4a(a[3], umax4a(a[2], umax4a(a[1], umax4(a[0])))) != SENT)) break;
        }
        TS(24);
        float acc[8];
#pragma unroll
        for (int d = 0; d < 4; ++d) {
          acc[2 * d] = dot2(a[3][d], one_lo, dot2(a[2][d], one_lo, dot2(a[1][d], one_lo, bflo(a[0][d]))));
          acc[2 * d + 1] = dot2(a[3][d], one_hi, dot2(a[2][d], one_hi, dot2(a[1][d], one_hi, bfhi(a[0][d]))));
        }
#pragma unroll
        for (int e = 0; e < 4; ++e) x1[e] = xf[e] + ga4[e] * rs32(acc[e], acc[e + 4]);
        const float s1 = wave_sum((x1[0] + x1[1]) + (x1[2] + x1[3]));
        const float s2 = wave_sum((x1[0] * x1[0] + x1[1] * x1[1]) + (x1[2] * x1[2] + x1[3] * x1[3]));
        *(LAS f32x2*)(L + a_st) = f32x2{s1, s2};
      }
      v4u fa[3][8], fd[8];
      if (w < 4) { tr8(wa[0], fa[0]); tr8(wa[1], fa[1]); if (w < 2) tr8(wa[2], fa[2]); }
      int a_st2 = L_MISC + b * 24;
      int a_ar = L_RED + ((r16 & 1) * DM + 64 * w + 32 * (r16 >> 3) + 8 * g) * 2;
      int a_rw = L_QIMG + (((w * 2 + (g >> 1)) * 2) * 64 + 8 * (r16 & 7)) * 4;
      KEEP(a_st2); KEEP(a_ar); KEEP(a_rw);
      BAR();
      TS(25);
      if (act_lane) {
        const f32x2 t0 = *(LAS f32x2*)(L + a_st2), t1 = *(LAS f32x2*)(L + a_st2 + 8), t2 = *(LAS f32x2*)(L + a_st2 + 16);
        const float mean = (t0[0] + t1[0] + t2[0]) * (1.f / DM);
        const float var = fmaxf((t0[1] + t1[1] + t2[1]) * (1.f / DM) - mean * mean, 0.f);
        const float rstd = __builtin_amdgcn_rsqf(var + 1e-5f);
        float hv[4];
#pragma unroll
        for (int e = 0; e < 4; ++e) hv[e] = (x1[e] - mean) * rstd * cb4[e] + sb4[e];
        *(LAS v2u*)(L + a_h2w) = v2u{pk2(hv[0], hv[1]), pk2(hv[2], hv[3])};
      }
      BAR();
      TS(26);
      if (w < 4) {
        const v4u a0 = *(LAS v4u*)(L + a_ar), a1 = *(LAS v4u*)(L + a_ar + 512);
        v4u a2 = a0;
        if (w < 2) a2 = *(LAS v4u*)(L + a_ar + 1024);
        f32x4 acc[8];
#pragma unroll
        for (int i = 0; i < 8; ++i) acc[i] = mfma16(a0, fa[0][i], f32x4{0.f, 0.f, 0.f, 0.f});
#pragma unroll
        for (int i = 0; i < 8; ++i) acc[i] = mfma16(a1, fa[1][i], acc[i]);
        if (w < 2) {
#pragma unroll
          for (int i = 0; i < 8; ++i) acc[i] = mfma16(a2, fa[2][i], acc[i]);
        }
        if ((g == 0 && r16 < 8) || (g == 2 && r16 >= 8)) {
#pragma unroll
          for (int b2 = 0; b2 < 2; ++b2) {
            *(LAS f32x4*)(L + a_rw + b2 * 256) = f32x4{acc[0][b2], acc[1][b2], acc[2][b2], acc[3][b2]};
            *(LAS f32x4*)(L + a_rw + b2 * 256 + 16) = f32x4{acc[4][b2], acc[5][b2], acc[6][b2], acc[7][b2]};
          }
        }
      }
      if (w < 5) { tr8(wdn, fd); KEEP8(fd); }
      int a_gr = L_QIMG + ((l >> 5) * 64 + (l & 31)) * 4, a_aw = L_MISC + 512 + l * 2, a_acr = L_MISC + 512 + ((r16 & 1) * 32 + 8 * g) * 2;
      int o_x7 = X7 + (idx * 2 * DM + 128 * w + 8 * r16) * 2;
      KEEP(a_gr); KEEP(a_aw); KEEP(a_acr); KEEP(o_x7);
      BAR();
      TS(27);
      if (w == 0) {
        float gv[8], uv[8];
#pragma unroll
        for (int k = 0; k < 8; ++k) { gv[k] = *(LAS float*)(L + a_gr + k * 512); uv[k] = *(LAS float*)(L + a_gr + k * 512 + 128); }
        const float gs = ((gv[0] + gv[1]) + (gv[2] + gv[3])) + ((gv[4] + gv[5]) + (gv[6] + gv[7]));
        const float us = ((uv[0] + uv[1]) + (uv[2] + uv[3])) + ((uv[4] + uv[5]) + (uv[6] + uv[7]));
        *(LAS u16*)(L + a_aw) = (u16)pk2(silu(gs) * us, 0.f);
      }
      BAR();
      TS(28);
      if (w < 5) {
        const v4u a = *(LAS v4u*)(L + a_acr);
        f32x4 d[8];
#pragma unroll
        for (int i = 0; i < 8; ++i) d[i] = mfma16(a, fd[i], f32x4{0.f, 0.f, 0.f, 0.f});
        if (g == 0) {
#pragma unroll
          for (int b2 = 0; b2 < 2; ++b2)
            st16(v4u{pk2(d[0][b2], d[1][b2]), pk2(d[2][b2], d[3][b2]), pk2(d[4][b2], d[5][b2]), pk2(d[6][b2], d[7][b2])}, o_x7 + b2 * DM * 2);
        }
        TS(29);
      }
    } else if (idx >= 64 && idx < 84) {
      const int n = idx - 64;
      float base0 = 0.f, base1 = 0.f, gb0 = 0.f, gb1 = 0.f;
      if (w == 7 && l < 32) {
        const int b = l >> 4, pr = l & 15, m = 32 * n + 2 * pr;
        unsigned ga = 0, gb = 0, ap[8];
        for (unsigned sp = 0; sp < SPIN_MAX; ++sp) {
          asm volatile("" ::: "memory");
          ga = ld4(XM + (b * 2560 + m) * 2); gb = ld4(XM + (b * 2560 + 1920 + m) * 2);
#pragma unroll
          for (int k = 0; k < 8; ++k) ap[k] = ld4(X6 + ((k * 2 + b) * DM + m) * 2);
          if (__all(umax3(umax3(umax3(ap[0], ap[1], ap[2]), umax3(ap[3], ap[4], ap[5]), umax3(ap[6], ap[7], ga)), gb, 0u) != SENT)) break;
        }
        const unsigned xd = *(const GAS unsigned*)(p.x + b * DM + m);
        float s0 = 0.f, s1 = 0.f;
#pragma unroll
        for (int k = 0; k < 8; ++k) { s0 += bflo(ap[k]); s1 += bfhi(ap[k]); }
        base0 = bflo(xd) + bflo(ga) * s0; base1 = bfhi(xd) + bfhi(ga) * s1; gb0 = bflo(gb); gb1 = bfhi(gb);
        KEEP(base0); KEEP(base1); KEEP(gb0); KEEP(gb1);
      }
      int a_fr = L_RED + ((l >> 4) * 32 + 2 * (l & 15)) * 4;
      KEEP(a_fr);
      if (w < 7) {
        const int task = 8 * w + (l >> 3), b = (l >> 2) & 1, ch = l & 3;
        const bool valid = task < 54;
        int off = X7 + (((valid ? task : 0) * 2 + b) * DM + 32 * n + 8 * ch) * 2;
        int a_w = L_RED + (w * 64 + b * 32 + 8 * ch + 4 * (l >> 5) + 2 * ((l >> 4) & 1)) * 4;
        unsigned msk = valid ? 0xFFFFFFFFu : 0u;
        KEEP(off); KEEP(a_w); KEEP(msk);
        PIN();
        v4u a;
        for (unsigned sp = 0; sp < SPIN_MAX; ++sp) {
          asm volatile("" ::: "memory");
          a = ld16(off);
          if (__all(umax4(a) != SENT)) break;
        }
        TS(29);
        float v[8];
#pragma unroll
        for (int d = 0; d < 4; ++d) { const unsigned x = a[d] & msk; v[2 * d] = bflo(x); v[2 * d + 1] = bfhi(x); }
        float t0 = rs16(rs32(v[0], v[4]), rs32(v[2], v[6])), t1 = rs16(rs32(v[1], v[5]), rs32(v[3], v[7]));
        t0 += dppf<0x128>(t0); t1 += dppf<0x128>(t1);
        *(LAS f32x2*)(L + a_w) = f32x2{t0, t1};
      }
      BAR();
      if (w == 7 && l < 32) {
        f32x2 v[7];
#pragma unroll
        for (int k = 0; k < 7; ++k) v[k] = *(LAS f32x2*)(L + a_fr + k * 256);
        const float s0 = ((v[0][0] + v[1][0]) + (v[2][0] + v[3][0])) + ((v[4][0] + v[5][0]) + v[6][0]);
        const float s1 = ((v[0][1] + v[1][1]) + (v[2][1] + v[3][1])) + ((v[4][1] + v[5][1]) + v[6][1]);
        *(GAS unsigned*)(p.out + (l >> 4) * DM + 32 * n + 2 * (l & 15)) = pk2(base0 + gb0 * s0, base1 + gb1 * s1);
        if (n == 0 && l == 0) st4(TAG(), OFF_EPOCH);
      }
    }
  }
done: ;
  if (bid < WBL2 && w == 1) { asm volatile("buffer_wbl2 sc1\n\ts_waitcnt vmcnt(0)" ::: "memory"); }
  if (bid == MEAS_BLK && w == 0) { u64 t1; do { asm volatile("s_memrealtime %0\n\ts_waitcnt lgkmcnt(0)" : "=s"(t1) :: "memory"); } while (t1 < meas_t + 6000ull); }
}

extern "C" void kernel_launch(void* const* d_in, const int* in_sizes, int n_in, void* d_out, int out_size, void* d_ws, size_t ws_size, hipStream_t stream) {
  Params p;
  p.x = (const u16*)d_in[0]; p.ckv = (const u16*)d_in[1]; p.wq = (const u16*)d_in[2]; p.wuk = (const u16*)d_in[3]; p.wuv = (const u16*)d_in[4]; p.wo = (const u16*)d_in[5];
  p.wg = (const u16*)d_in[7]; p.wu = (const u16*)d_in[8]; p.wd = (const u16*)d_in[9]; p.c = (const u16*)d_in[10]; p.wmod = (const u16*)d_in[11];
  p.out = (u16*)d_out; p.ws = (unsigned char*)d_ws;
  mega<<<256, 512, 0, stream>>>(p);
}
```

```cpp
#include <hip/hip_runtime.h>
#include <stdint.h>
#define E_DELAY 80
#define E_DEPTH 10
#define L_DELAY 2

#define GAS __attribute__((address_space(1)))
#define LAS __attribute__((address_space(3)))
typedef unsigned v4u __attribute__((ext_vector_type(4)));
typedef unsigned v2u __attribute__((ext_vector_type(2)));
typedef float f32x4 __attribute__((ext_vector_type(4)));
typedef float f32x2 __attribute__((ext_vector_type(2)));
typedef __bf16 bf16x8 __attribute__((ext_vector_type(8)));
typedef __bf16 bf16x2 __attribute__((ext_vector_type(2)));
typedef short s16x4 __attribute__((ext_vector_type(4)));
typedef unsigned short u16;
typedef unsigned long long u64;

constexpr int NB = 2, NH = 32, SK = 8192, DH = 128, DM = 640, DC = 512, DF = 1728;
constexpr int OFF_EPOCH = 0;
constexpr int FSTR = 32;
constexpr int OFF_FLAGS = 4096;
constexpr int OFF_EFLAGS = OFF_FLAGS + 256 * FSTR;
constexpr int OFF_L2FLAGS = OFF_FLAGS + 128 * FSTR;
constexpr int OFF_X1 = OFF_EFLAGS + 128 * FSTR;
constexpr int OFF_R = 81920;
constexpr int X2 = OFF_R;
constexpr int X3 = X2 + 131072;
constexpr int X4 = X3 + 65536;
constexpr int X4S = X4 + 4194304;
constexpr int X5 = X4S + 65536;
constexpr int X6 = X5 + 65536;
constexpr int X7 = X6 + 20480;
constexpr int XM = X7 + 138240;
constexpr int R_END = XM + 10240;
static_assert(OFF_X1 + 20480 <= OFF_R, "map");
constexpr int WS_USED = R_END;
#define F_DELAY 30
#define T3_DELAY 0
#define B_ROWS 63
#define S5_SLEEP 2
#define Q_SLEEP 0
#define P_SLEEP 0
#define LATE_SLEEP 0
#define T1_EARLY 12
#define A_DELAY 30
constexpr unsigned SENT = 0xFFFFFFFFu;
constexpr int AUX_SC1 = 16;
constexpr unsigned SPIN_MAX = 1u << 18;
constexpr int L_CKV = 0;
constexpr int L_QIMG = 131072;
constexpr int LDS_BYTES = 163840;
constexpr int L_T1RED = L_QIMG, L_T1MISC = L_QIMG + 8192;
constexpr int L_MX = 0;
constexpr int L_PIMG = 4096;
constexpr int L_RED = 16384;
constexpr int L_RED2 = 32768;
constexpr int L_MISC = 49152;
constexpr int L_UKT = 65536;
constexpr int L_X2W = 81920;
constexpr int L_X1W = 90112;
constexpr int L_X5W = 98304;

#define TS(k) do { } while (0)

struct Params {
  const u16 *x, *ckv, *wq, *wuk, *wuv, *wo, *wg, *wu, *wd, *c, *wmod;
  u16* out;
  unsigned char* ws;
};

#define BAR() do { asm volatile("s_waitcnt lgkmcnt(0)" ::: "memory"); __builtin_amdgcn_s_barrier(); asm volatile("" ::: "memory"); } while (0)
#define DRAIN() asm volatile("s_waitcnt vmcnt(0)" ::: "memory")
#define PIN() __builtin_amdgcn_sched_barrier(0)
#define KEEP(x) asm volatile("" : "+v"(x))
#define KEEP4(a) do { KEEP(a[0]); KEEP(a[1]); KEEP(a[2]); KEEP(a[3]); } while (0)
#define KEEP8(a) do { KEEP(a[0]); KEEP(a[1]); KEEP(a[2]); KEEP(a[3]); KEEP(a[4]); KEEP(a[5]); KEEP(a[6]); KEEP(a[7]); } while (0)

__device__ __forceinline__ float bflo(unsigned d) { return __uint_as_float(d << 16); }
__device__ __forceinline__ float bfhi(unsigned d) { return __uint_as_float(d & 0xffff0000u); }
__device__ __forceinline__ unsigned pk2(float lo, float hi) { f32x2 f = {lo, hi}; return __builtin_bit_cast(unsigned, __builtin_convertvector(f, bf16x2)); }
__device__ __forceinline__ f32x4 mfma16(v4u a, v4u b, f32x4 c) { return __builtin_amdgcn_mfma_f32_16x16x32_bf16(__builtin_bit_cast(bf16x8, a), __builtin_bit_cast(bf16x8, b), c, 0, 0, 0); }
__device__ __forceinline__ float dot2(unsigned a, unsigned b, float c) { return __builtin_amdgcn_fdot2_f32_bf16(__builtin_bit_cast(bf16x2, a), __builtin_bit_cast(bf16x2, b), c, false); }
__device__ __forceinline__ v4u ldnt(const u16* p) { return __builtin_nontemporal_load((const GAS v4u*)p); }
__device__ __forceinline__ unsigned umax3(unsigned a, unsigned b, unsigned c) { unsigned t = a > b ? a : b; return t > c ? t : c; }
__device__ __forceinline__ unsigned umax4(v4u v) { return umax3(umax3(v.x, v.y, v.z), v.w, 0u); }
__device__ __forceinline__ unsigned umax4a(v4u v, unsigned m) { return umax3(umax3(v.x, v.y, v.z), v.w, m); }
__device__ __forceinline__ float x16_max(float x) {
  auto s = __builtin_amdgcn_permlane16_swap(__float_as_uint(x), __float_as_uint(x), false, false);
  x = fmaxf(__uint_as_float(s[0]), __uint_as_float(s[1]));
  auto t = __builtin_amdgcn_permlane32_swap(__float_as_uint(x), __float_as_uint(x), false, false);
  return fmaxf(__uint_as_float(t[0]), __uint_as_float(t[1]));
}
__device__ __forceinline__ float x16_sum(float x) {
  auto s = __builtin_amdgcn_permlane16_swap(__float_as_uint(x), __float_as_uint(x), false, false);
  x = __uint_as_float(s[0]) + __uint_as_float(s[1]);
  auto t = __builtin_amdgcn_permlane32_swap(__float_as_uint(x), __float_as_uint(x), false, false);
  return __uint_as_float(t[0]) + __uint_as_float(t[1]);
}
__device__ __forceinline__ float rs32(float a, float b) { auto t = __builtin_amdgcn_permlane32_swap(__float_as_uint(a), __float_as_uint(b), false, false); return __uint_as_float(t[0]) + __uint_as_float(t[1]); }
__device__ __forceinline__ float rs16(float a, float b) { auto t = __builtin_amdgcn_permlane16_swap(__float_as_uint(a), __float_as_uint(b), false, false); return __uint_as_float(t[0]) + __uint_as_float(t[1]); }
template <int CTRL> __device__ __forceinline__ float dppf(float x) { return __builtin_bit_cast(float, __builtin_amdgcn_mov_dpp(__builtin_bit_cast(int, x), CTRL, 0xf, 0xf, true)); }
__device__ __forceinline__ float row16_sum(float x) { x += dppf<0xB1>(x); x += dppf<0x4E>(x); x += dppf<0x141>(x); x += dppf<0x128>(x); return x; }
__device__ __forceinline__ float wave_sum(float x) { return x16_sum(row16_sum(x)); }
__device__ __forceinline__ float silu(float v) { return v * __builtin_amdgcn_rcpf(1.f + __builtin_amdgcn_exp2f(v * -1.4426950408889634f)); }

__device__ __forceinline__ void tr8(const v4u (&w)[8], v4u (&f)[8]) {
#pragma unroll
  for (int d = 0; d < 4; ++d) {
#pragma unroll
    for (int i2 = 0; i2 < 4; ++i2) {
      f[2 * i2][d] = __builtin_amdgcn_perm(w[2 * d + 1][i2], w[2 * d][i2], 0x05040100u);
      f[2 * i2 + 1][d] = __builtin_amdgcn_perm(w[2 * d + 1][i2], w[2 * d][i2], 0x07060302u);
    }
  }
}
__device__ __forceinline__ v4u silu8(v4u c) {
  v4u r;
#pragma unroll
  for (int d = 0; d < 4; ++d) r[d] = pk2(silu(bflo(c[d])), silu(bfhi(c[d])));
  return r;
}
template <int CTRL> __device__ __forceinline__ unsigned dppu(unsigned x) { return (unsigned)__builtin_amdgcn_mov_dpp((int)x, CTRL, 0xf, 0xf, true); }
__device__ __forceinline__ v4u quad4(unsigned d) { return v4u{dppu<0x00>(d), dppu<0x55>(d), dppu<0xAA>(d), dppu<0xFF>(d)}; }
__device__ __forceinline__ v4u pair2(unsigned a, unsigned b) { return v4u{dppu<0xA0>(a), dppu<0xA0>(b), dppu<0xF5>(a), dppu<0xF5>(b)}; }
template <bool V> struct BoolC { static constexpr bool value = V; };
__device__ __forceinline__ float sum5(float a, float b, float c, float d, float e) { return ((a + b) + (c + d)) + e; }

__global__ void __launch_bounds__(512, 2) mega(Params p) {
  __shared__ __attribute__((aligned(16))) unsigned char smem[LDS_BYTES];
  LAS unsigned char* L = (LAS unsigned char*)smem;
  const int tid = threadIdx.x, l = tid & 63, w = __builtin_amdgcn_readfirstlane(tid >> 6);
  const int bid = blockIdx.x;
  const int g = l >> 4, r16 = l & 15;
  TS(0);
  const __amdgpu_buffer_rsrc_t rs = __builtin_amdgcn_make_buffer_rsrc((void*)p.ws, 0, WS_USED, 0x00020000);
  const bool Ltype = bid >= 128;
  const int idx = bid & 127;
  const int ab = idx >> 6, aj = idx & 63;
  unsigned epoch_raw = 0;
  auto LOAD_EPOCH = [&]() { epoch_raw = __builtin_amdgcn_raw_buffer_load_b32(rs, OFF_EPOCH, 0, AUX_SC1); };
  auto TAG = [&]() { unsigned t = epoch_raw; asm volatile("" : "+v"(t)); return t + 1u; };

  auto ld16 = [&](int off) { return __builtin_amdgcn_raw_buffer_load_b128(rs, off, 0, AUX_SC1); };
  auto ld8 = [&](int off) { return __builtin_amdgcn_raw_buffer_load_b64(rs, off, 0, AUX_SC1); };
  auto ld4 = [&](int off) { return __builtin_amdgcn_raw_buffer_load_b32(rs, off, 0, AUX_SC1); };
  auto st16 = [&](v4u v, int off) { __builtin_amdgcn_raw_buffer_store_b128(v, rs, off, 0, AUX_SC1); };
  auto st8 = [&](v2u v, int off) { __builtin_amdgcn_raw_buffer_store_b64(v, rs, off, 0, AUX_SC1); };
  auto st4 = [&](unsigned v, int off) { __builtin_amdgcn_raw_buffer_store_b32(v, rs, off, 0, AUX_SC1); };
  auto dma_ckv = [&](int row0, int nrows, bool paced = false, int rstride = 1) {
    const u16* base = p.ckv + ((size_t)ab * SK + aj * 128) * DC;
    for (int rr = 0; rr < nrows; ++rr) {
      if (paced) asm volatile("s_waitcnt vmcnt(%0)" :: "i"(E_DEPTH - 1) : "memory");
      const int row = row0 + rr * rstride;
      const int sw = ((row & 3) << 2) | ((row >> 2) & 3);
      const u16* src = base + row * DC + ((l ^ sw) * 8);
      unsigned keep; const unsigned dst = (unsigned)(L_CKV + row * 1024) + (unsigned)(size_t)L;
      asm volatile("s_mov_b32 %0, m0\n\ts_mov_b32 m0, %2\n\ts_nop 0\n\tglobal_load_lds_dwordx4 %1, off nt\n\ts_mov_b32 m0, %0" : "=&s"(keep) : "v"(src), "s"(dst) : "memory");
    }
  };
  const v4u SENT4 = {SENT, SENT, SENT, SENT};
  auto wait_flags = [&](int off) {
    const unsigned tag = TAG();
    for (unsigned sp = 0; sp < SPIN_MAX; ++sp) {
      asm volatile("" ::: "memory");
      if (__all(ld4(off) == tag)) break;
      __builtin_amdgcn_s_sleep(2);
    }
  };

  if (Ltype) {
    const int hh = idx >> 2, sub = idx & 3;
    if (w < 5) {
      v4u wv2[8], uk[8];
      const u16* src2 = p.wq + (size_t)(160 * sub + 32 * w + 8 * g) * (NH * DH) + hh * DH + 8 * r16;
      auto load_t3 = [&]() {
        const u16* src = p.wuk + ((size_t)(128 * sub + 32 * (w & 3) + g) * NH + hh) * DH + 8 * r16;
#pragma unroll
        for (int t = 0; t < 2; ++t)
#pragma unroll
          for (int i = 0; i < 4; ++i) uk[4 * t + i] = ldnt(src + (size_t)(16 * t + 4 * i) * NH * DH);
      };
      auto load_t23 = [&]() {
#pragma unroll
        for (int j = 0; j < 8; ++j) wv2[j] = ldnt(src2 + (size_t)j * (NH * DH));
        __builtin_amdgcn_s_sleep(T3_DELAY);
        if (w < 4) load_t3();
      };
      __builtin_amdgcn_s_sleep(L_DELAY);
      load_t23();
      LOAD_EPOCH();
      PIN();
      {
        unsigned tag = TAG();
        v4u f[8]; tr8(wv2, f);
        KEEP8(f);
        TS(2);
        int off = OFF_X1 + ((l >> 3) * 320 + (160 * sub + 32 * w) / 2 + 2 * (l & 7)) * 8;
        int a_gw = L_X1W + w * 512 + (l >> 3) * 64 + (l & 7) * 8, a_gr = L_X1W + w * 512 + (r16 & 7) * 64 + g * 16;
        int a_w = L_RED2 + (((w * 2 + (g & 1)) * 2) * 128 + 8 * r16) * 4;
        int a_r = L_RED2 + ((w & 1) * 128 + 2 * l) * 4, o_x2 = X2 + ((hh * 4 + sub) * 2 + (w & 1)) * 512 + l * 8;
        KEEP8(f); KEEP(off); KEEP(a_w); KEEP(tag); KEEP(a_r); KEEP(o_x2); KEEP(a_gw); KEEP(a_gr);
        if (w < 4) {
#pragma unroll
          for (int t = 0; t < 2; ++t) {
#pragma unroll
            for (int i = 0; i < 4; ++i) *(LAS v4u*)(L + L_UKT + w * 4096 + (4 * i + g) * 256 + ((r16 ^ (4 * i + g)) * 16)) = uk[4 * t + i];
#pragma unroll
            for (int s = 0; s < 4; ++s) uk[4 * t + s] = *(LAS v4u*)(L + L_UKT + w * 4096 + r16 * 256 + (((4 * s + g) ^ r16) * 16));
            KEEP(uk[4 * t]); KEEP(uk[4 * t + 1]); KEEP(uk[4 * t + 2]); KEEP(uk[4 * t + 3]);
          }
          TS(1);
        }
        TS(5);
        PIN();
        v4u u0;
        for (unsigned sp = 0; sp < SPIN_MAX; ++sp) {
          asm volatile("" ::: "memory");
          u0 = ld16(off);
          if (__all(((u0.y ^ tag) | (u0.w ^ tag)) == 0u)) break;
          if (P_SLEEP) __builtin_amdgcn_s_sleep(P_SLEEP);
        }
        *(LAS v2u*)(L + a_gw) = v2u{u0.x, u0.z};
        const v4u a = *(LAS v4u*)(L + a_gr);
        TS(6);
        f32x4 acc[8];
#pragma unroll
        for (int i = 0; i < 8; ++i) acc[i] = mfma16(a, f[i], f32x4{0.f, 0.f, 0.f, 0.f});
        if (g < 2) {
#pragma unroll
          for (int b2 = 0; b2 < 2; ++b2) {
            *(LAS f32x4*)(L + a_w + b2 * 512) = f32x4{acc[0][b2] + acc[0][b2 + 2], acc[1][b2] + acc[1][b2 + 2], acc[2][b2] + acc[2][b2 + 2], acc[3][b2] + acc[3][b2 + 2]};
            *(LAS f32x4*)(L + a_w + b2 * 512 + 16) = f32x4{acc[4][b2] + acc[4][b2 + 2], acc[5][b2] + acc[5][b2 + 2], acc[6][b2] + acc[6][b2 + 2], acc[7][b2] + acc[7][b2 + 2]};
          }
        }
        BAR();
        TS(7);
        if (w < 2) {
          f32x2 v[10];
#pragma unroll
          for (int k = 0; k < 10; ++k) v[k] = *(LAS f32x2*)(L + a_r + k * 1024);
          const float s0 = sum5(v[0][0], v[1][0], v[2][0], v[3][0], v[4][0]) + sum5(v[5][0], v[6][0], v[7][0], v[8][0], v[9][0]);
          const float s1 = sum5(v[0][1], v[1][1], v[2][1], v[3][1], v[4][1]) + sum5(v[5][1], v[6][1], v[7][1], v[8][1], v[9][1]);
          { const v4u v = pair2(pk2(s0, s1), tag); if ((l & 1) == 0) st16(v, o_x2); }
          TS(8);
        }
      }
      {
        int off = X2 + hh * 4096 + l * 16;
        int a_gw[4];
#pragma unroll
        for (int k = 0; k < 4; ++k) { const int row = 2 * k + (l >> 5), pc = l & 31; a_gw[k] = L_X2W + (w & 3) * 2048 + row * 256 + (((pc >> 1) ^ row) * 16) + (pc & 1) * 8; }
        int a_gr[4];
#pragma unroll
        for (int s = 0; s < 4; ++s) a_gr[s] = L_X2W + (w & 3) * 2048 + (r16 & 7) * 256 + (((4 * s + g) ^ (r16 & 7)) * 16);
        int a_w = L_RED + ((g & 1) * 128 + 32 * (w & 3) + r16) * 2;
        int o_x3 = X3 + ((l >> 4) * NH + hh) * 1024 + sub * 256 + (l & 15) * 16;
        unsigned tag = TAG();
        KEEP(off); KEEP(a_w); KEEP(o_x3); KEEP4(a_gw); KEEP4(a_gr); KEEP(tag);
        PIN();
        if (w < 4) {
          v4u u[4];
          for (unsigned sp = 0; sp < SPIN_MAX; ++sp) {
            asm volatile("" ::: "memory");
#pragma unroll
            for (int k = 0; k < 4; ++k) u[k] = ld16(off + k * 1024);
            const unsigned bad = (((u[0].y ^ tag) | (u[0].w ^ tag)) | ((u[1].y ^ tag) | (u[1].w ^ tag))) | (((u[2].y ^ tag) | (u[2].w ^ tag)) | ((u[3].y ^ tag) | (u[3].w ^ tag)));
            if (__all(bad == 0u)) break;
            if (P_SLEEP) __builtin_amdgcn_s_sleep(P_SLEEP);
          }
#pragma unroll
          for (int k = 0; k < 4; ++k) *(LAS v2u*)(L + a_gw[k]) = v2u{u[k].x, u[k].z};
          v4u a[4];
#pragma unroll
          for (int s = 0; s < 4; ++s) a[s] = *(LAS v4u*)(L + a_gr[s]);
          TS(9);
          const float sc = 0.08838834764831845f * 1.4426950408889634f;
          f32x4 acc[2][2];
#pragma unroll
          for (int t = 0; t < 2; ++t) { acc[t][0] = mfma16(a[0], uk[4 * t], f32x4{0.f, 0.f, 0.f, 0.f}); acc[t][1] = mfma16(a[1], uk[4 * t + 1], f32x4{0.f, 0.f, 0.f, 0.f}); }
#pragma unroll
          for (int t = 0; t < 2; ++t) { acc[t][0] = mfma16(a[2], uk[4 * t + 2], acc[t][0]); acc[t][1] = mfma16(a[3], uk[4 * t + 3], acc[t][1]); }
#pragma unroll
          for (int t = 0; t < 2; ++t) {
            const f32x4 v = acc[t][0] + acc[t][1];
            const float tot = rs16(v[0] + v[2], v[1] + v[3]) * sc;
            if (g < 2) *(LAS u16*)(L + a_w + 32 * t) = (u16)pk2(tot, tot);
          }
        }
        BAR();
        if (w == 0 && l < 32) {
          st16(*(LAS v4u*)(L + L_RED + l * 16), o_x3);
          TS(10);
        }
      }
    } else {
      if (w == 7) {
        if (l >= 32) st16(SENT4, X3 + (((l - 32) >> 4) * NH + hh) * 1024 + sub * 256 + (l & 15) * 16);
        if (l < 32) st16(SENT4, X5 + idx * 512 + l * 16);
        else if (l < 48 && idx < 80) st16(SENT4, X6 + (((idx / 10) * 2 + ((l - 32) >> 3)) * DM + 64 * (idx % 10)) * 2 + (l & 7) * 16);
        else if (l < 48 && idx < 120) st16(SENT4, XM + (((l - 32) >> 3) * 2560 + 64 * (idx - 80)) * 2 + (l & 7) * 16);
        LOAD_EPOCH();
        DRAIN();
        if (l == 0) st4(TAG(), OFF_EFLAGS + idx * FSTR);
      } else {
        __builtin_amdgcn_s_sleep(20);
        LOAD_EPOCH();
      }
      BAR();
      BAR();
    }
    const int h5 = idx >> 2, cq = idx & 3;
    v4u w5[8], w6[2][8];
    if (w < 4) {
      const u16* src = p.wuv + ((size_t)(128 * cq + 32 * w + 8 * g) * NH + h5) * DH + 8 * r16;
#pragma unroll
      for (int j = 0; j < 8; ++j) w5[j] = ldnt(src + (size_t)j * (NH * DH));
      if (idx < 80) {
        const int kk = (l >> 3) & 1, ch = l & 7, pp = idx / 10, n = idx % 10;
        const u16* src6 = p.wo + (size_t)(512 * pp + 64 * w + 32 * kk + 8 * g) * DM + 64 * n + 8 * ch;
#pragma unroll
        for (int k = 0; k < 2; ++k)
#pragma unroll
          for (int j = 0; j < 8; ++j) w6[k][j] = ldnt(src6 + (size_t)(256 * k + j) * DM);
      }
    }
    PIN();
    if (idx >= 80 && idx < 120) {
      const int tb = idx - 80, wv = w & 3;
      if (w >= 4) {
        v4u wpre[3][8], cv[3];
        const int kk = (l >> 3) & 1, ch = l & 7;
#pragma unroll
        for (int k = 0; k < 3; ++k) cv[k] = *(const GAS v4u*)(p.c + (r16 & 1) * DM + 64 * (k < 2 || wv < 2 ? wv + 4 * k : wv) + 32 * (r16 >> 3) + 8 * g);
        const u16* src = p.wmod + (size_t)(64 * wv + 32 * kk + 8 * g) * (6 * DM) + 2 * DM + 64 * tb + 8 * ch;
#pragma unroll
        for (int k = 0; k < 2; ++k)
#pragma unroll
          for (int j = 0; j < 8; ++j) wpre[k][j] = ldnt(src + (size_t)(256 * k + j) * (6 * DM));
        if (wv < 2) {
#pragma unroll
          for (int j = 0; j < 8; ++j) wpre[2][j] = ldnt(src + (size_t)(512 + j) * (6 * DM));
        }
        PIN();
        v4u a[3];
#pragma unroll
        for (int k = 0; k < 3; ++k) a[k] = silu8(cv[k]);
        f32x4 acc[8];
#pragma unroll
        for (int i = 0; i < 8; ++i) acc[i] = f32x4{0.f, 0.f, 0.f, 0.f};
#pragma unroll
        for (int k = 0; k < 3; ++k) {
          if (k < 2 || wv < 2) {
            v4u f[8];
            tr8(wpre[k], f);
#pragma unroll
            for (int i = 0; i < 8; ++i) acc[i] = mfma16(a[k], f[i], acc[i]);
          }
        }
        if ((g == 0 && r16 < 8) || (g == 2 && r16 >= 8)) {
#pragma unroll
          for (int b2 = 0; b2 < 2; ++b2) {
            LAS float* d = (LAS float*)(L + L_X5W) + ((wv * 2 + (g >> 1)) * 2 + b2) * 64 + 8 * (r16 & 7);
            *(LAS f32x4*)d = f32x4{acc[0][b2], acc[1][b2], acc[2][b2], acc[3][b2]}; *(LAS f32x4*)(d + 4) = f32x4{acc[4][b2], acc[5][b2], acc[6][b2], acc[7][b2]};
          }
        }
      }
      BAR();
      if (w == 4) {
        const int b = l >> 5, pr = l & 31;
        float s0 = 0.f, s1 = 0.f;
#pragma unroll
        for (int k = 0; k < 8; ++k) { const f32x2 v = *(LAS f32x2*)((LAS float*)(L + L_X5W) + (k * 2 + b) * 64 + 2 * pr); s0 += v[0]; s1 += v[1]; }
        { const v4u q = quad4(pk2(s0, s1)); if ((l & 3) == 0) st16(q, XM + (b * 2560 + 64 * tb + 2 * pr) * 2); }
        TS(29);
      }
    }
    wait_flags(OFF_EFLAGS + l * FSTR); wait_flags(OFF_EFLAGS + (64 + l) * FSTR);
    {
      const int b = w >> 2;
      int off = X4 + (((b * NH + h5) * 64 + 16 * (w & 3) + g) * 512 + 128 * cq + 8 * r16) * 2;
      int offs = X4S + ((b * 64 + 16 * (w & 3) + g) * NH + h5) * 16;
      const int cofs = 8 * r16 + 4 * (l >> 5) + 2 * ((l >> 4) & 1);
      int a_w = L_RED + w * 256 + (((cofs >> 3) ^ w) * 16) + (cofs & 7) * 2;
      int a_s = L_MISC + 1024 + w * 8;
      unsigned tag = TAG();
      KEEP(off); KEEP(offs); KEEP(a_w); KEEP(a_s); KEEP(tag);
      PIN();
      v4u a[4], sv[4];
      for (unsigned sp = 0; sp < SPIN_MAX; ++sp) {
        asm volatile("" ::: "memory");
#pragma unroll
        for (int k = 0; k < 4; ++k) sv[k] = ld16(offs + k * 2048);
        if (__all(((((sv[0].y ^ tag) | (sv[0].w ^ tag)) | ((sv[1].y ^ tag) | (sv[1].w ^ tag))) | (((sv[2].y ^ tag) | (sv[2].w ^ tag)) | ((sv[3].y ^ tag) | (sv[3].w ^ tag)))) == 0u)) break;
        __builtin_amdgcn_s_sleep(S5_SLEEP);
      }
      asm volatile("" ::: "memory");
      TS(19);
#pragma unroll
      for (int k = 0; k < 4; ++k) a[k] = ld16(off + k * 4096);
      TS(20);
      float mw = fmaxf(fmaxf(__uint_as_float(sv[0].x), __uint_as_float(sv[1].x)), fmaxf(__uint_as_float(sv[2].x), __uint_as_float(sv[3].x)));
      mw = x16_max(mw);
      float acc[8] = {0.f, 0.f, 0.f, 0.f, 0.f, 0.f, 0.f, 0.f}; float lw = 0.f;
#pragma unroll
      for (int k = 0; k < 4; ++k) {
        const float e = __builtin_amdgcn_exp2f(__uint_as_float(sv[k].x) - mw);
        const unsigned wlo = pk2(e, 0.f), whi = pk2(0.f, e);
        lw += __uint_as_float(whi) * __uint_as_float(sv[k].z);
#pragma unroll
        for (int d = 0; d < 4; ++d) { acc[2 * d] = dot2(a[k][d], wlo, acc[2 * d]); acc[2 * d + 1] = dot2(a[k][d], whi, acc[2 * d + 1]); }
      }
      const float u0 = rs16(rs32(acc[0], acc[4]), rs32(acc[2], acc[6])), u1 = rs16(rs32(acc[1], acc[5]), rs32(acc[3], acc[7]));
      *(LAS unsigned*)(L + a_w) = pk2(u0, u1);
      lw = x16_sum(lw);
      *(LAS f32x2*)(L + a_s) = f32x2{mw, lw};
    }
    {
      v4u f[8];
      if (w < 4) { tr8(w5, f); KEEP8(f); }
      const int row = r16 & 7;
      int a_a = L_RED + row * 256 + (((4 * w + g) ^ row) * 16);
      int a_st = L_MISC + 1024 + (g & 1) * 32;
      int a_w2 = L_RED2 + ((w * 2 + (g & 1)) * 128 + 8 * r16) * 4;
      int a_r2 = L_RED2 + ((w & 1) * 128 + 2 * l) * 4, o_x5 = X5 + (idx * 2 + (w & 1)) * 256 + l * 4;
      KEEP(a_a); KEEP(a_st); KEEP(a_w2); KEEP(a_r2); KEEP(o_x5);
      BAR();
      TS(21);
      if (w < 4) {
        const v4u a = *(LAS v4u*)(L + a_a);
        const f32x4 s01 = *(LAS f32x4*)(L + a_st), s23 = *(LAS f32x4*)(L + a_st + 16);
        f32x4 d[8];
#pragma unroll
        for (int i = 0; i < 8; ++i) d[i] = mfma16(a, f[i], f32x4{0.f, 0.f, 0.f, 0.f});
        const float mb = fmaxf(fmaxf(s01[0], s01[2]), fmaxf(s23[0], s23[2]));
        float f0 = __builtin_amdgcn_exp2f(s01[0] - mb), f1 = __builtin_amdgcn_exp2f(s01[2] - mb), f2 = __builtin_amdgcn_exp2f(s23[0] - mb), f3 = __builtin_amdgcn_exp2f(s23[2] - mb);
        const float inv = __builtin_amdgcn_rcpf((f0 * s01[1] + f1 * s01[3]) + (f2 * s23[1] + f3 * s23[3]));
        f0 *= inv; f1 *= inv; f2 *= inv; f3 *= inv;
        float v[8];
#pragma unroll
        for (int i = 0; i < 8; ++i) v[i] = (d[i][0] * f0 + d[i][1] * f1) + (d[i][2] * f2 + d[i][3] * f3);
        if (g < 2) { *(LAS f32x4*)(L + a_w2) = f32x4{v[0], v[1], v[2], v[3]}; *(LAS f32x4*)(L + a_w2 + 16) = f32x4{v[4], v[5], v[6], v[7]}; }
      }
      BAR();
      TS(22);
      if (w < 2) {
        f32x2 v[4];
#pragma unroll
        for (int k = 0; k < 4; ++k) v[k] = *(LAS f32x2*)(L + a_r2 + k * 1024);
        { const v4u q = quad4(pk2((v[0][0] + v[1][0]) + (v[2][0] + v[3][0]), (v[0][1] + v[1][1]) + (v[2][1] + v[3][1]))); if ((l & 3) == 0) st16(q, o_x5); }
        TS(23);
      }
    }
    if (idx < 80) {
      const int pp = idx / 10, n = idx % 10;
      if (w < 4) {
        v4u f0[8], f1[8]; tr8(w6[0], f0); tr8(w6[1], f1);
        int off = X5 + ((4 * pp + (w >> 1)) * 8 + (l >> 3)) * 256 + 128 * (w & 1) + (l & 7) * 16;
        int a_gw = L_X5W + w * 2048 + (l >> 3) * 128 + (l & 7) * 16, a_gr = L_X5W + w * 2048 + (r16 & 7) * 128 + (r16 >> 3) * 64 + g * 16;
        int a_w = L_RED + (((w * 2 + (g >> 1)) * 2 + (g & 1)) * 64 + 8 * (r16 & 7)) * 4;
        KEEP8(f0); KEEP8(f1); KEEP(off); KEEP(a_w); KEEP(a_gw); KEEP(a_gr);
        PIN();
        v4u u0, u1;
        for (unsigned sp = 0; sp < SPIN_MAX; ++sp) {
          asm volatile("" ::: "memory");
          u0 = ld16(off); u1 = ld16(off + 2 * 2048);
          if (__all(umax4a(u1, umax4(u0)) != SENT)) break;
          __builtin_amdgcn_s_sleep(1);
        }
        *(LAS v4u*)(L + a_gw) = u0; *(LAS v4u*)(L + a_gw + 1024) = u1;
        const v4u a0 = *(LAS v4u*)(L + a_gr), a1 = *(LAS v4u*)(L + a_gr + 1024);
        TS(24);
        f32x4 d[8];
#pragma unroll
        for (int i = 0; i < 8; ++i) d[i] = mfma16(a0, f0[i], f32x4{0.f, 0.f, 0.f, 0.f});
#pragma unroll
        for (int i = 0; i < 8; ++i) d[i] = mfma16(a1, f1[i], d[i]);
        float s[8];
#pragma unroll
        for (int i = 0; i < 8; ++i) s[i] = rs16(d[i][0] + d[i][2], d[i][1] + d[i][3]);
        if ((g >> 1) == (r16 >> 3)) {
          *(LAS f32x4*)(L + a_w) = f32x4{s[0], s[1], s[2], s[3]}; *(LAS f32x4*)(L + a_w + 16) = f32x4{s[4], s[5], s[6], s[7]};
        }
      }
      int a_r = L_RED + ((l >> 5) * 64 + 2 * (l & 31)) * 4, o_x6 = X6 + ((pp * 2 + (l >> 5)) * DM + 64 * n + 2 * (l & 31)) * 2;
      KEEP(a_r); KEEP(o_x6);
      BAR();
      TS(25);
      if (w == 0) {
        f32x2 v[8];
#pragma unroll
        for (int k = 0; k < 8; ++k) v[k] = *(LAS f32x2*)(L + a_r + k * 512);
        const float s0 = ((v[0][0] + v[1][0]) + (v[2][0] + v[3][0])) + ((v[4][0] + v[5][0]) + (v[6][0] + v[7][0]));
        const float s1 = ((v[0][1] + v[1][1]) + (v[2][1] + v[3][1])) + ((v[4][1] + v[5][1]) + (v[6][1] + v[7][1]));
        { const v4u q = quad4(pk2(s0, s1)); if ((l & 3) == 0) st16(q, o_x6); }
        TS(29);
      }
    }
  } else {
    const bool t1role = idx < 40;
    auto fills = [&]() {
      if (t1role) __builtin_amdgcn_s_sleep(4);
      if (idx < 54) {
#pragma unroll
        for (int k = 0; k < 3; ++k) { const int u = k * 64 + l; if (u < 160) st16(SENT4, X7 + idx * 2560 + u * 16); }
      }
      DRAIN();
    };
    if (t1role) {
      const int mg = idx % 10, pp1 = idx / 10;
      if (w < 5) {
        v4u wv[8];
        const int col = (r16 < 8) ? (64 * mg + 8 * r16) : (DM + 64 * mg + 8 * (r16 - 8));
        const int k0 = 160 * pp1 + 32 * w + 8 * g;
        const v4u cv = *(const GAS v4u*)(p.c + (r16 & 1) * DM + k0);
        const u16* src = p.wmod + (size_t)k0 * (6 * DM) + col;
#pragma unroll
        for (int j = 0; j < 8; ++j) wv[j] = ldnt(src + (size_t)j * (6 * DM));
        const unsigned xd = *(const GAS unsigned*)(p.x + (l >> 5) * DM + 64 * mg + 2 * (l & 31));
        LOAD_EPOCH();
        PIN();
        int a_w = L_T1RED + ((w * 2) * 128 + 8 * r16) * 4;
        int a_r = L_T1RED + ((l >> 5) * 128 + 2 * (l & 31)) * 4;
        int o_x1 = OFF_X1 + (((pp1 * 2 + (l >> 5)) * 320) + 32 * mg + (l & 31)) * 8;
        KEEP(a_w); KEEP(a_r); KEEP(o_x1);
        v4u a = silu8(cv);
        KEEP(a);
        PIN();
        v4u f[8]; tr8(wv, f);
        KEEP8(f);
        float xlo = bflo(xd), xhi = bfhi(xd);
        unsigned tag = TAG();
        KEEP(xlo); KEEP(xhi); KEEP(tag);
        TS(1);
        PIN();
        f32x4 acc[8];
#pragma unroll
        for (int i = 0; i < 8; ++i) acc[i] = mfma16(a, f[i], f32x4{0.f, 0.f, 0.f, 0.f});
        if (g == 0) {
#pragma unroll
          for (int b = 0; b < 2; ++b) {
            *(LAS f32x4*)(L + a_w + b * 512) = f32x4{acc[0][b], acc[1][b], acc[2][b], acc[3][b]};
            *(LAS f32x4*)(L + a_w + b * 512 + 16) = f32x4{acc[4][b], acc[5][b], acc[6][b], acc[7][b]};
          }
        }
        TS(2);
        BAR();
        TS(3);
        if (w == 0) {
          f32x2 s[5], c2[5];
#pragma unroll
          for (int ww = 0; ww < 5; ++ww) { s[ww] = *(LAS f32x2*)(L + a_r + ww * 1024); c2[ww] = *(LAS f32x2*)(L + a_r + ww * 1024 + 256); }
          const f32x2 mr = *(LAS f32x2*)(L + L_T1MISC + (l >> 5) * 8);
          const float xn0 = (xlo - mr[0]) * mr[1], xn1 = (xhi - mr[0]) * mr[1];
          const float one = pp1 == 0 ? 1.f : 0.f;
          const float o0 = xn0 * (sum5(c2[0][0], c2[1][0], c2[2][0], c2[3][0], c2[4][0]) + one) + sum5(s[0][0], s[1][0], s[2][0], s[3][0], s[4][0]);
          const float o1 = xn1 * (sum5(c2[0][1], c2[1][1], c2[2][1], c2[3][1], c2[4][1]) + one) + sum5(s[0][1], s[1][1], s[2][1], s[3][1], s[4][1]);
          { const v4u v = pair2(pk2(o0, o1), tag); if ((l & 1) == 0) st16(v, o_x1); }
          TS(4);
        }
        dma_ckv(T1_EARLY * 3 + w, (128 - T1_EARLY * 3 - w + 7) / 8, true, 8);
      } else if (w < 7) {
        const int b = w - 5;
        LOAD_EPOCH();
        float v[10]; float s = 0.f;
#pragma unroll
        for (int k = 0; k < 5; ++k) { const unsigned d = *(const GAS unsigned*)(p.x + b * DM + 2 * (l + 64 * k)); v[2 * k] = bflo(d); v[2 * k + 1] = bfhi(d); s += v[2 * k] + v[2 * k + 1]; }
        const float mean = wave_sum(s) * (1.f / DM);
        float q = 0.f;
#pragma unroll
        for (int k = 0; k < 10; ++k) { const float d = v[k] - mean; q += d * d; }
        const float var = wave_sum(q) * (1.f / DM);
        *(LAS f32x2*)(L + L_T1MISC + b * 8) = f32x2{mean, __builtin_amdgcn_rsqf(var + 1e-5f)};
        __builtin_amdgcn_s_sleep(E_DELAY / 2);
        dma_ckv(b, T1_EARLY, false, 3);
        BAR();
        dma_ckv(T1_EARLY * 3 + w, (128 - T1_EARLY * 3 - w + 7) / 8, true, 8);
      } else {
        LOAD_EPOCH();
        fills();
        if (l == 0) st4(TAG(), OFF_FLAGS + idx * FSTR);
        dma_ckv(2, T1_EARLY, false, 3);
        BAR();
        dma_ckv(T1_EARLY * 3 + w, (128 - T1_EARLY * 3 - w + 7) / 8, true, 8);
      }
    } else if (w < 7) {
      __builtin_amdgcn_s_sleep(E_DELAY);
      LOAD_EPOCH();
      dma_ckv(w, 17, true, 7);
    } else {
      LOAD_EPOCH();
      fills();
      if (l == 0) st4(TAG(), OFF_FLAGS + idx * FSTR);
      dma_ckv(119, 9, true);
    }
    {
      const unsigned ef0 = ld4(OFF_EFLAGS + l * FSTR), ef1 = ld4(OFF_EFLAGS + (64 + l) * FSTR);
      int a_qw[2], a_qr[4], a_kr[4];
      const int key = 16 * w + r16;
#pragma unroll
      for (int u = 0; u < 2; ++u) a_qw[u] = L_QIMG + (8 * u + w) * 1024 + ((l ^ (8 * u + w)) * 16);
#pragma unroll
      for (int t = 0; t < 4; ++t) { a_qr[t] = L_QIMG + r16 * 1024 + (((4 * t + g) ^ r16) * 16); a_kr[t] = L_CKV + key * 1024 + (((4 * t + g) ^ (((key & 3) << 2) | ((key >> 2) & 3))) * 16); }
      int a_vr[2];
#pragma unroll
      for (int u = 0; u < 2; ++u) a_vr[u] = L_CKV + (4 * g + (r16 >> 2)) * 1024 + (((8 * w + 4 * u + (l & 3)) ^ (((r16 >> 2) << 2) | g)) * 16);
      int a_mxw = L_MX + r16 * 32 + w * 4, a_mxr = L_MX + r16 * 32;
      int a_pw = L_PIMG + (w >> 1) * 1024 + l * 16 + (w & 1) * 8, a_pr = L_PIMG + l * 16;
      int a_ow[2], a_or[4];
#pragma unroll
      for (int u = 0; u < 2; ++u) a_ow[u] = L_QIMG + r16 * 1024 + (((8 * w + 4 * u + g) ^ r16) * 16);
#pragma unroll
      for (int k = 0; k < 4; ++k) a_or[k] = L_QIMG + (4 * w + k) * 1024 + ((l ^ ((4 * w + k) & 15)) * 16);
      KEEP(a_ow[0]); KEEP(a_ow[1]); KEEP4(a_or);
      int o_st = X4 + ((ab * NH + 4 * w) * 64 + aj) * 1024 + l * 16, o_ss = X4S + ((ab * 64 + aj) * NH + 16 * (w & 1) + r16) * 16;
      int o_q = X3 + ab * 32768 + tid * 16;
      KEEP(a_qw[0]); KEEP(a_qw[1]); KEEP4(a_qr); KEEP4(a_kr); KEEP(a_vr[0]); KEEP(a_vr[1]); KEEP(a_mxw); KEEP(a_mxr); KEEP(a_pw); KEEP(a_pr); KEEP(o_st); KEEP(o_ss); KEEP(o_q);
      DRAIN();
      { const unsigned tag = TAG(); if (!__all((ef0 == tag) & (ef1 == tag))) { wait_flags(OFF_EFLAGS + l * FSTR); wait_flags(OFF_EFLAGS + (64 + l) * FSTR); } }
      BAR();
      TS(11);
      v4u qa[4];
#pragma unroll
      for (int k = 0; k < 4; ++k) qa[k] = ld16(o_q + k * 8192);
      PIN();
      v4u qk[16], pv[16];
#pragma unroll
      for (int s = 0; s < 16; ++s) qk[s] = *(LAS v4u*)(L + a_kr[s & 3] + (s >> 2) * 256);
#pragma unroll
      for (int s = 0; s < 4; ++s)
#pragma unroll
        for (int t = 0; t < 4; ++t) {
          v2u h2[2];
#pragma unroll
          for (int hf = 0; hf < 2; ++hf)
            h2[hf] = __builtin_bit_cast(v2u, __builtin_amdgcn_ds_read_tr16_b64_v4i16((LAS s16x4*)(L + a_vr[t >> 1] + (32 * s + 16 * hf) * 1024 + 8 * (t & 1))));
          pv[s * 4 + t] = v4u{h2[0].x, h2[0].y, h2[1].x, h2[1].y};
        }
#pragma unroll
      for (int s = 0; s < 16; ++s) { KEEP(qk[s]); KEEP(pv[s]); }
      TS(12);
      PIN();
      for (unsigned sp = 0; sp < SPIN_MAX; ++sp) {
        if (__all(umax4a(qa[3], umax4a(qa[2], umax4a(qa[1], umax4(qa[0])))) != SENT)) break;
        if (Q_SLEEP) __builtin_amdgcn_s_sleep(Q_SLEEP);
        asm volatile("" ::: "memory");
#pragma unroll
        for (int k = 0; k < 4; ++k) qa[k] = ld16(o_q + k * 8192);
      }
#pragma unroll
      for (int k = 0; k < 4; ++k) *(LAS v4u*)(L + a_qw[k & 1] + (k >> 1) * 16384) = qa[k];
      TS(13);
      BAR();
      TS(14);
      f32x4 sacc[2][2];
#pragma unroll
      for (int ht = 0; ht < 2; ++ht) { sacc[ht][0] = f32x4{0.f, 0.f, 0.f, 0.f}; sacc[ht][1] = f32x4{0.f, 0.f, 0.f, 0.f}; }
      {
        v4u fa[8], fb[8];
#pragma unroll
        for (int s = 0; s < 8; ++s) fa[s] = *(LAS v4u*)(L + a_qr[s & 3] + (s >> 2) * 256);
#pragma unroll
        for (int s = 0; s < 8; ++s) fb[s] = *(LAS v4u*)(L + a_qr[s & 3] + (2 + (s >> 2)) * 256);
        PIN();
#pragma unroll
        for (int s = 0; s < 8; ++s) sacc[0][s & 1] = mfma16(qk[s], fa[s], sacc[0][s & 1]);
        PIN();
#pragma unroll
        for (int s = 0; s < 8; ++s) fa[s] = *(LAS v4u*)(L + a_qr[s & 3] + 16384 + (s >> 2) * 256);
        PIN();
#pragma unroll
        for (int s = 0; s < 8; ++s) sacc[0][s & 1] = mfma16(qk[8 + s], fb[s], sacc[0][s & 1]);
        PIN();
#pragma unroll
        for (int s = 0; s < 8; ++s) fb[s] = *(LAS v4u*)(L + a_qr[s & 3] + 16384 + (2 + (s >> 2)) * 256);
        PIN();
#pragma unroll
        for (int s = 0; s < 8; ++s) sacc[1][s & 1] = mfma16(qk[s], fa[s], sacc[1][s & 1]);
#pragma unroll
        for (int s = 0; s < 8; ++s) sacc[1][s & 1] = mfma16(qk[8 + s], fb[s], sacc[1][s & 1]);
      }
      PIN();
      unsigned lf[2] = {0u, 0u};
      if (w == 6) lf[0] = ld4(OFF_FLAGS + l * FSTR);
      f32x4 sc[2];
#pragma unroll
      for (int ht = 0; ht < 2; ++ht) {
        sc[ht] = sacc[ht][0] + sacc[ht][1];
        *(LAS float*)(L + a_mxw + ht * 512) = x16_max(fmaxf(fmaxf(sc[ht][0], sc[ht][1]), fmaxf(sc[ht][2], sc[ht][3])));
      }
      TS(15);
      BAR();
      TS(16);
      float m[2];
#pragma unroll
      for (int ht = 0; ht < 2; ++ht) {
        const f32x4 m0 = *(LAS f32x4*)(L + a_mxr + ht * 512), m1 = *(LAS f32x4*)(L + a_mxr + ht * 512 + 16);
        m[ht] = fmaxf(fmaxf(fmaxf(m0[0], m0[1]), fmaxf(m0[2], m0[3])), fmaxf(fmaxf(m1[0], m1[1]), fmaxf(m1[2], m1[3])));
        *(LAS v2u*)(L + a_pw + ht * 4096) = v2u{pk2(__builtin_amdgcn_exp2f(sc[ht][0] - m[ht]), __builtin_amdgcn_exp2f(sc[ht][1] - m[ht])), pk2(__builtin_amdgcn_exp2f(sc[ht][2] - m[ht]), __builtin_amdgcn_exp2f(sc[ht][3] - m[ht]))};
      }
      BAR();
      v4u pf[2][4];
#pragma unroll
      for (int ht = 0; ht < 2; ++ht)
#pragma unroll
        for (int s = 0; s < 4; ++s) pf[ht][s] = *(LAS v4u*)(L + a_pr + ht * 4096 + s * 1024);
      PIN();
#pragma unroll
      for (int ht = 0; ht < 2; ++ht) {
        f32x4 o[4];
#pragma unroll
        for (int t = 0; t < 4; ++t) o[t] = f32x4{0.f, 0.f, 0.f, 0.f};
#pragma unroll
        for (int s = 0; s < 4; ++s)
#pragma unroll
          for (int t = 0; t < 4; ++t) o[t] = mfma16(pv[s * 4 + t], pf[ht][s], o[t]);
        *(LAS v4u*)(L + a_ow[0] + ht * 16384) = v4u{pk2(o[0][0], o[0][1]), pk2(o[0][2], o[0][3]), pk2(o[1][0], o[1][1]), pk2(o[1][2], o[1][3])};
        *(LAS v4u*)(L + a_ow[1] + ht * 16384) = v4u{pk2(o[2][0], o[2][1]), pk2(o[2][2], o[2][3]), pk2(o[3][0], o[3][1]), pk2(o[3][2], o[3][3])};
      }
      float lsum = 0.f;
      if (w < 2) {
        const v4u ones = {0x3F803F80u, 0x3F803F80u, 0x3F803F80u, 0x3F803F80u};
        f32x4 lacc = {0.f, 0.f, 0.f, 0.f};
        if (w == 0) {
#pragma unroll
          for (int s = 0; s < 4; ++s) lacc = mfma16(ones, pf[0][s], lacc);
        } else {
#pragma unroll
          for (int s = 0; s < 4; ++s) lacc = mfma16(ones, pf[1][s], lacc);
        }
        lsum = lacc[0];
      }
      BAR();
#pragma unroll
      for (int k = 0; k < 4; ++k) st16(*(LAS v4u*)(L + a_or[k]), o_st + k * 65536);
      TS(18);
      DRAIN();
      BAR();
      if (w < 2 && g == 0) { const unsigned tag = TAG(); st16(v4u{__float_as_uint(w == 0 ? m[0] : m[1]), tag, __float_as_uint(lsum), tag}, o_ss); }
      TS(17);
      if (w == 6) {
        const unsigned tag = TAG();
        if (!__all((lf[0] == tag) | (l >= 54))) wait_flags(OFF_FLAGS + (l < 54 ? l : 0) * FSTR);
      }
      BAR();
      TS(19);
    }
    if (idx < 54) {
      v4u wa0[8], wa1[8], wa2[8], wdn[8];
      if (w >= 4) {
        const u16* wsrc78 = (((l & 7) < 4) ? p.wg : p.wu) + 32 * idx + 8 * (l & 3) + (size_t)(64 * (w & 3) + 32 * ((l >> 3) & 1) + 8 * g) * DF;
#pragma unroll
        for (int j = 0; j < 8; ++j) wa0[j] = ldnt(wsrc78 + (size_t)j * DF);
#pragma unroll
        for (int j = 0; j < 8; ++j) wa1[j] = ldnt(wsrc78 + (size_t)(256 + j) * DF);
        if (w < 6) {
#pragma unroll
          for (int j = 0; j < 8; ++j) wa2[j] = ldnt(wsrc78 + (size_t)(512 + j) * DF);
        }
      }
      if (w >= 3) {
        const u16* src = p.wd + (size_t)(32 * idx + 8 * g) * DM + 128 * (w - 3) + 8 * r16;
#pragma unroll
        for (int j = 0; j < 8; ++j) wdn[j] = ldnt(src + (size_t)j * DM);
      }
      PIN();
      const int b = w / 3, cc = 32 * (w % 3) + (l & 31), half = l >> 5;
      const bool act_lane = (w < 6) && (cc < 80);
      float x1[4] = {0.f, 0.f, 0.f, 0.f}, cb4[4], sb4[4];
      int a_h2w = L_RED + (b * DM + 8 * cc + 4 * half) * 2;
      if (w < 6) {
        const int ccc = cc < 80 ? cc : 79;
        const int mo = 8 * ccc + 4 * half;
        v2u gav, sbv, cbv;
        for (unsigned sp = 0; sp < SPIN_MAX; ++sp) {
          asm volatile("" ::: "memory");
          gav = ld8(XM + (b * 2560 + mo) * 2); sbv = ld8(XM + (b * 2560 + 640 + mo) * 2); cbv = ld8(XM + (b * 2560 + 1280 + mo) * 2);
          if (__all(umax3(umax3(gav.x, gav.y, sbv.x), umax3(sbv.y, cbv.x, cbv.y), 0u) != SENT)) break;
          __builtin_amdgcn_s_sleep(8);
        }
        const v2u xv = *(const GAS v2u*)(p.x + b * DM + mo);
        float xf[4] = {bflo(xv.x), bfhi(xv.x), bflo(xv.y), bfhi(xv.y)}, ga4[4] = {bflo(gav.x), bfhi(gav.x), bflo(gav.y), bfhi(gav.y)};
        cb4[0] = 1.f + bflo(cbv.x); cb4[1] = 1.f + bfhi(cbv.x); cb4[2] = 1.f + bflo(cbv.y); cb4[3] = 1.f + bfhi(cbv.y);
        sb4[0] = bflo(sbv.x); sb4[1] = bfhi(sbv.x); sb4[2] = bflo(sbv.y); sb4[3] = bfhi(sbv.y);
        if (!act_lane) { ga4[0] = ga4[1] = ga4[2] = ga4[3] = 0.f; xf[0] = xf[1] = xf[2] = xf[3] = 0.f; }
        int off = X6 + ((4 * half * 2 + b) * DM + 8 * ccc) * 2;
        int a_st = L_MISC + w * 8;
        unsigned one_lo = 0x00003F80u, one_hi = 0x3F800000u;
        KEEP(one_lo); KEEP(one_hi);
        KEEP4(xf); KEEP4(ga4); KEEP4(cb4); KEEP4(sb4); KEEP(off); KEEP(a_st); KEEP(a_h2w);
        PIN();
        v4u a[4];
        for (unsigned sp = 0; sp < SPIN_MAX; ++sp) {
          asm volatile("" ::: "memory");
#pragma unroll
          for (int k = 0; k < 4; ++k) a[k] = ld16(off + k * 2560);
          if (__all(umax4a(a[3], umax4a(a[2], umax4a(a[1], umax4(a[0])))) != SENT)) break;
          if (LATE_SLEEP) __builtin_amdgcn_s_sleep(LATE_SLEEP);
        }
        TS(24);
        float acc[8];
#pragma unroll
        for (int d = 0; d < 4; ++d) {
          acc[2 * d] = dot2(a[3][d], one_lo, dot2(a[2][d], one_lo, dot2(a[1][d], one_lo, bflo(a[0][d]))));
          acc[2 * d + 1] = dot2(a[3][d], one_hi, dot2(a[2][d], one_hi, dot2(a[1][d], one_hi, bfhi(a[0][d]))));
        }
#pragma unroll
        for (int e = 0; e < 4; ++e) x1[e] = xf[e] + ga4[e] * rs32(acc[e], acc[e + 4]);
        const float s1 = wave_sum((x1[0] + x1[1]) + (x1[2] + x1[3]));
        const float s2 = wave_sum((x1[0] * x1[0] + x1[1] * x1[1]) + (x1[2] * x1[2] + x1[3] * x1[3]));
        *(LAS f32x2*)(L + a_st) = f32x2{s1, s2};
      }
      v4u fa[3][8], fd[8];
      if (w >= 4) { tr8(wa0, fa[0]); tr8(wa1, fa[1]); if (w < 6) tr8(wa2, fa[2]); KEEP8(fa[0]); KEEP8(fa[1]); }
      int a_st2 = L_MISC + b * 24;
      int a_ar = L_RED + ((r16 & 1) * DM + 64 * (w & 3) + 32 * (r16 >> 3) + 8 * g) * 2;
      int a_rw = L_RED2 + ((((w & 3) * 2 + (g >> 1)) * 2) * 64 + 8 * (r16 & 7)) * 4;
      KEEP(a_st2); KEEP(a_ar); KEEP(a_rw);
      BAR();
      TS(25);
      if (act_lane) {
        const f32x2 t0 = *(LAS f32x2*)(L + a_st2), t1 = *(LAS f32x2*)(L + a_st2 + 8), t2 = *(LAS f32x2*)(L + a_st2 + 16);
        const float mean = (t0[0] + t1[0] + t2[0]) * (1.f / DM);
        const float var = fmaxf((t0[1] + t1[1] + t2[1]) * (1.f / DM) - mean * mean, 0.f);
        const float rstd = __builtin_amdgcn_rsqf(var + 1e-5f);
        float hv[4];
#pragma unroll
        for (int e = 0; e < 4; ++e) hv[e] = (x1[e] - mean) * rstd * cb4[e] + sb4[e];
        *(LAS v2u*)(L + a_h2w) = v2u{pk2(hv[0], hv[1]), pk2(hv[2], hv[3])};
      }
      BAR();
      TS(26);
      if (w >= 4) {
        const v4u a0 = *(LAS v4u*)(L + a_ar), a1 = *(LAS v4u*)(L + a_ar + 512);
        v4u a2 = a0;
        if (w < 6) a2 = *(LAS v4u*)(L + a_ar + 1024);
        f32x4 acc[8];
#pragma unroll
        for (int i = 0; i < 8; ++i) acc[i] = mfma16(a0, fa[0][i], f32x4{0.f, 0.f, 0.f, 0.f});
#pragma unroll
        for (int i = 0; i < 8; ++i) acc[i] = mfma16(a1, fa[1][i], acc[i]);
        if (w < 6) {
#pragma unroll
          for (int i = 0; i < 8; ++i) acc[i] = mfma16(a2, fa[2][i], acc[i]);
        }
        if ((g == 0 && r16 < 8) || (g == 2 && r16 >= 8)) {
#pragma unroll
          for (int b2 = 0; b2 < 2; ++b2) {
            *(LAS f32x4*)(L + a_rw + b2 * 256) = f32x4{acc[0][b2], acc[1][b2], acc[2][b2], acc[3][b2]};
            *(LAS f32x4*)(L + a_rw + b2 * 256 + 16) = f32x4{acc[4][b2], acc[5][b2], acc[6][b2], acc[7][b2]};
          }
        }
      }
      if (w >= 3) { tr8(wdn, fd); KEEP8(fd); }
      int a_gr = L_RED2 + ((l >> 5) * 64 + (l & 31)) * 4, a_aw = L_MISC + 512 + l * 2, a_acr = L_MISC + 512 + ((r16 & 1) * 32 + 8 * g) * 2;
      int o_x7 = X7 + (idx * 2 * DM + 128 * (w - 3) + 8 * r16) * 2;
      KEEP(a_gr); KEEP(a_aw); KEEP(a_acr); KEEP(o_x7);
      BAR();
      TS(27);
      if (w == 0) {
        float gv[8], uv[8];
#pragma unroll
        for (int k = 0; k < 8; ++k) { gv[k] = *(LAS float*)(L + a_gr + k * 512); uv[k] = *(LAS float*)(L + a_gr + k * 512 + 128); }
        const float gs = ((gv[0] + gv[1]) + (gv[2] + gv[3])) + ((gv[4] + gv[5]) + (gv[6] + gv[7]));
        const float us = ((uv[0] + uv[1]) + (uv[2] + uv[3])) + ((uv[4] + uv[5]) + (uv[6] + uv[7]));
        *(LAS u16*)(L + a_aw) = (u16)pk2(silu(gs) * us, 0.f);
      }
      BAR();
      TS(28);
      if (w >= 3) {
        const v4u a = *(LAS v4u*)(L + a_acr);
        f32x4 d[8];
#pragma unroll
        for (int i = 0; i < 8; ++i) d[i] = mfma16(a, fd[i], f32x4{0.f, 0.f, 0.f, 0.f});
        if (g == 0) {
#pragma unroll
          for (int b2 = 0; b2 < 2; ++b2)
            st16(v4u{pk2(d[0][b2], d[1][b2]), pk2(d[2][b2], d[3][b2]), pk2(d[4][b2], d[5][b2]), pk2(d[6][b2], d[7][b2])}, o_x7 + b2 * DM * 2);
        }
        TS(29);
      }
    } else if (idx >= 94 && idx < 114) {
      const int n = idx - 94;
      float base0 = 0.f, base1 = 0.f, gb0 = 0.f, gb1 = 0.f;
      if (w == 7 && l < 32) {
        const int b = l >> 4, pr = l & 15, m = 32 * n + 2 * pr;
        unsigned ga = 0, gb = 0, ap[8];
        for (unsigned sp = 0; sp < SPIN_MAX; ++sp) {
          asm volatile("" ::: "memory");
          ga = ld4(XM + (b * 2560 + m) * 2); gb = ld4(XM + (b * 2560 + 1920 + m) * 2);
#pragma unroll
          for (int k = 0; k < 8; ++k) ap[k] = ld4(X6 + ((k * 2 + b) * DM + m) * 2);
          if (__all(umax3(umax3(umax3(ap[0], ap[1], ap[2]), umax3(ap[3], ap[4], ap[5]), umax3(ap[6], ap[7], ga)), gb, 0u) != SENT)) break;
          __builtin_amdgcn_s_sleep(8);
        }
        const unsigned xd = *(const GAS unsigned*)(p.x + b * DM + m);
        float s0 = 0.f, s1 = 0.f;
#pragma unroll
        for (int k = 0; k < 8; ++k) { s0 += bflo(ap[k]); s1 += bfhi(ap[k]); }
        base0 = bflo(xd) + bflo(ga) * s0; base1 = bfhi(xd) + bfhi(ga) * s1; gb0 = bflo(gb); gb1 = bfhi(gb);
        KEEP(base0); KEEP(base1); KEEP(gb0); KEEP(gb1);
      }
      int a_fr = L_RED + ((l >> 4) * 32 + 2 * (l & 15)) * 4;
      KEEP(a_fr);
      if (w < 7) {
        const int task = 8 * w + (l >> 3), b = (l >> 2) & 1, ch = l & 3;
        const bool valid = task < 54;
        int off = X7 + (((valid ? task : 0) * 2 + b) * DM + 32 * n + 8 * ch) * 2;
        int a_w = L_RED + (w * 64 + b * 32 + 8 * ch + 4 * (l >> 5) + 2 * ((l >> 4) & 1)) * 4;
        unsigned msk = valid ? 0xFFFFFFFFu : 0u;
        KEEP(off); KEEP(a_w); KEEP(msk);
        PIN();
        v4u a;
        for (unsigned sp = 0; sp < SPIN_MAX; ++sp) {
          asm volatile("" ::: "memory");
          a = ld16(off);
          if (__all(umax4(a) != SENT)) break;
          if (LATE_SLEEP) __builtin_amdgcn_s_sleep(LATE_SLEEP);
        }
        TS(29);
        float v[8];
#pragma unroll
        for (int d = 0; d < 4; ++d) { const unsigned x = a[d] & msk; v[2 * d] = bflo(x); v[2 * d + 1] = bfhi(x); }
        float t0 = rs16(rs32(v[0], v[4]), rs32(v[2], v[6])), t1 = rs16(rs32(v[1], v[5]), rs32(v[3], v[7]));
        t0 += dppf<0x128>(t0); t1 += dppf<0x128>(t1);
        *(LAS f32x2*)(L + a_w) = f32x2{t0, t1};
      }
      BAR();
      if (w == 7 && l < 32) {
        f32x2 v[7];
#pragma unroll
        for (int k = 0; k < 7; ++k) v[k] = *(LAS f32x2*)(L + a_fr + k * 256);
        const float s0 = ((v[0][0] + v[1][0]) + (v[2][0] + v[3][0])) + ((v[4][0] + v[5][0]) + v[6][0]);
        const float s1 = ((v[0][1] + v[1][1]) + (v[2][1] + v[3][1])) + ((v[4][1] + v[5][1]) + v[6][1]);
        *(GAS unsigned*)(p.out + (l >> 4) * DM + 32 * n + 2 * (l & 15)) = pk2(base0 + gb0 * s0, base1 + gb1 * s1);
        if (n == 0 && l == 0) st4(TAG(), OFF_EPOCH);
      }
    }
  }
done: ;
}

extern "C" void kernel_launch(void* const* d_in, const int* in_sizes, int n_in, void* d_out, int out_size, void* d_ws, size_t ws_size, hipStream_t stream) {
  Params p;
  p.x = (const u16*)d_in[0]; p.ckv = (const u16*)d_in[1]; p.wq = (const u16*)d_in[2]; p.wuk = (const u16*)d_in[3]; p.wuv = (const u16*)d_in[4]; p.wo = (const u16*)d_in[5];
  p.wg = (const u16*)d_in[7]; p.wu = (const u16*)d_in[8]; p.wd = (const u16*)d_in[9]; p.c = (const u16*)d_in[10]; p.wmod = (const u16*)d_in[11];
  p.out = (u16*)d_out;
  p.ws = (unsigned char*)d_ws;
  mega<<<256, 512, 0, stream>>>(p);
}
```

```cpp
#include <hip/hip_runtime.h>
#include <stdint.h>
#define E_DELAY 70
#define E_DEPTH 16
#define L_DELAY 40
#define S5_SLEEP 0
#define CODE_PF 6

#define GAS __attribute__((address_space(1)))
#define LAS __attribute__((address_space(3)))
typedef unsigned v4u __attribute__((ext_vector_type(4)));
typedef unsigned v2u __attribute__((ext_vector_type(2)));
typedef float f32x4 __attribute__((ext_vector_type(4)));
typedef float f32x2 __attribute__((ext_vector_type(2)));
typedef __bf16 bf16x8 __attribute__((ext_vector_type(8)));
typedef __bf16 bf16x2 __attribute__((ext_vector_type(2)));
typedef short s16x4 __attribute__((ext_vector_type(4)));
typedef unsigned short u16;
typedef unsigned long long u64;

constexpr int NB = 2, NH = 32, SK = 8192, DH = 128, DM = 640, DC = 512, DF = 1728;
constexpr int OFF_EPOCH = 0;
constexpr int FSTR = 32;
constexpr int OFF_FLAGS = 4096;
constexpr int OFF_EFLAGS = OFF_FLAGS + 256 * FSTR;
constexpr int OFF_L2FLAGS = OFF_FLAGS + 128 * FSTR;
constexpr int OFF_X1 = OFF_EFLAGS + 128 * FSTR;
constexpr int OFF_R = 81920;
constexpr int X2 = OFF_R;
constexpr int X3 = X2 + 131072;
constexpr int X4 = X3 + 65536;
constexpr int X4S = X4 + 4194304;
constexpr int X5 = X4S + 65536;
constexpr int X6 = X5 + 65536;
constexpr int X7 = X6 + 20480;
constexpr int XM = X7 + 138240;
constexpr int R_END = XM + 10240;
static_assert(OFF_X1 + 20480 <= OFF_R, "map");
constexpr int WS_USED = R_END;
#define F_DELAY 30
#define T3_DELAY 0
#define B_ROWS 63
#define Q_SLEEP 0
#define P_SLEEP 0
#define LATE_SLEEP 0
#define T1_EARLY 12
#define CODE_LAST 56
#define WARM_POL " nt"
#define A_DELAY 30
constexpr unsigned SENT = 0xFFFFFFFFu;
constexpr int AUX_SC1 = 16;
constexpr unsigned SPIN_MAX = 1u << 18;
constexpr int L_CKV = 0;
constexpr int L_QIMG = 131072;
constexpr int LDS_BYTES = 163840;
constexpr int L_T1RED = L_QIMG, L_T1MISC = L_QIMG + 8192;
constexpr int L_MX = 0;
constexpr int L_PIMG = 4096;
constexpr int L_RED = 16384;
constexpr int L_RED2 = 32768;
constexpr int L_MISC = 49152;
constexpr int L_UKT = 65536;
constexpr int L_X2W = 81920;
constexpr int L_X1W = 90112;
constexpr int L_X5W = 98304;

#define TS(k) do { } while (0)

struct Params {
  const u16 *x, *ckv, *wq, *wuk, *wuv, *wo, *wg, *wu, *wd, *c, *wmod;
  u16* out;
  unsigned char* ws;
};

#define BAR() do { asm volatile("s_waitcnt lgkmcnt(0)" ::: "memory"); __builtin_amdgcn_s_barrier(); asm volatile("" ::: "memory"); } while (0)
#define DRAIN() asm volatile("s_waitcnt vmcnt(0)" ::: "memory")
#define PIN() __builtin_amdgcn_sched_barrier(0)
#define KEEP(x) asm volatile("" : "+v"(x))
#define KEEP4(a) do { KEEP(a[0]); KEEP(a[1]); KEEP(a[2]); KEEP(a[3]); } while (0)
#define KEEP8(a) do { KEEP(a[0]); KEEP(a[1]); KEEP(a[2]); KEEP(a[3]); KEEP(a[4]); KEEP(a[5]); KEEP(a[6]); KEEP(a[7]); } while (0)

__device__ __forceinline__ float bflo(unsigned d) { return __uint_as_float(d << 16); }
__device__ __forceinline__ float bfhi(unsigned d) { return __uint_as_float(d & 0xffff0000u); }
__device__ __forceinline__ unsigned pk2(float lo, float hi) { f32x2 f = {lo, hi}; return __builtin_bit_cast(unsigned, __builtin_convertvector(f, bf16x2)); }
__device__ __forceinline__ f32x4 mfma16(v4u a, v4u b, f32x4 c) { return __builtin_amdgcn_mfma_f32_16x16x32_bf16(__builtin_bit_cast(bf16x8, a), __builtin_bit_cast(bf16x8, b), c, 0, 0, 0); }
__device__ __forceinline__ float dot2(unsigned a, unsigned b, float c) { return __builtin_amdgcn_fdot2_f32_bf16(__builtin_bit_cast(bf16x2, a), __builtin_bit_cast(bf16x2, b), c, false); }
__device__ __forceinline__ v4u ldnt(const u16* p) { return __builtin_nontemporal_load((const GAS v4u*)p); }
__device__ __forceinline__ unsigned umax3(unsigned a, unsigned b, unsigned c) { unsigned t = a > b ? a : b; return t > c ? t : c; }
__device__ __forceinline__ unsigned umax4(v4u v) { return umax3(umax3(v.x, v.y, v.z), v.w, 0u); }
__device__ __forceinline__ unsigned umax4a(v4u v, unsigned m) { return umax3(umax3(v.x, v.y, v.z), v.w, m); }
__device__ __forceinline__ float x16_max(float x) {
  auto s = __builtin_amdgcn_permlane16_swap(__float_as_uint(x), __float_as_uint(x), false, false);
  x = fmaxf(__uint_as_float(s[0]), __uint_as_float(s[1]));
  auto t = __builtin_amdgcn_permlane32_swap(__float_as_uint(x), __float_as_uint(x), false, false);
  return fmaxf(__uint_as_float(t[0]), __uint_as_float(t[1]));
}
__device__ __forceinline__ float x16_sum(float x) {
  auto s = __builtin_amdgcn_permlane16_swap(__float_as_uint(x), __float_as_uint(x), false, false);
  x = __uint_as_float(s[0]) + __uint_as_float(s[1]);
  auto t = __builtin_amdgcn_permlane32_swap(__float_as_uint(x), __float_as_uint(x), false, false);
  return __uint_as_float(t[0]) + __uint_as_float(t[1]);
}
__device__ __forceinline__ float rs32(float a, float b) { auto t = __builtin_amdgcn_permlane32_swap(__float_as_uint(a), __float_as_uint(b), false, false); return __uint_as_float(t[0]) + __uint_as_float(t[1]); }
__device__ __forceinline__ float rs16(float a, float b) { auto t = __builtin_amdgcn_permlane16_swap(__float_as_uint(a), __float_as_uint(b), false, false); return __uint_as_float(t[0]) + __uint_as_float(t[1]); }
template <int CTRL> __device__ __forceinline__ float dppf(float x) { return __builtin_bit_cast(float, __builtin_amdgcn_mov_dpp(__builtin_bit_cast(int, x), CTRL, 0xf, 0xf, true)); }
__device__ __forceinline__ float row16_sum(float x) { x += dppf<0xB1>(x); x += dppf<0x4E>(x); x += dppf<0x141>(x); x += dppf<0x128>(x); return x; }
__device__ __forceinline__ float wave_sum(float x) { return x16_sum(row16_sum(x)); }
__device__ __forceinline__ float silu(float v) { return v * __builtin_amdgcn_rcpf(1.f + __builtin_amdgcn_exp2f(v * -1.4426950408889634f)); }

__device__ __forceinline__ void tr8(const v4u (&w)[8], v4u (&f)[8]) {
#pragma unroll
  for (int d = 0; d < 4; ++d) {
#pragma unroll
    for (int i2 = 0; i2 < 4; ++i2) {
      f[2 * i2][d] = __builtin_amdgcn_perm(w[2 * d + 1][i2], w[2 * d][i2], 0x05040100u);
      f[2 * i2 + 1][d] = __builtin_amdgcn_perm(w[2 * d + 1][i2], w[2 * d][i2], 0x07060302u);
    }
  }
}
__device__ __forceinline__ v4u silu8(v4u c) {
  v4u r;
#pragma unroll
  for (int d = 0; d < 4; ++d) r[d] = pk2(silu(bflo(c[d])), silu(bfhi(c[d])));
  return r;
}
template <int CTRL> __device__ __forceinline__ unsigned dppu(unsigned x) { return (unsigned)__builtin_amdgcn_mov_dpp((int)x, CTRL, 0xf, 0xf, true); }
__device__ __forceinline__ v4u quad4(unsigned d) { return v4u{dppu<0x00>(d), dppu<0x55>(d), dppu<0xAA>(d), dppu<0xFF>(d)}; }
__device__ __forceinline__ v4u pair2(unsigned a, unsigned b) { return v4u{dppu<0xA0>(a), dppu<0xA0>(b), dppu<0xF5>(a), dppu<0xF5>(b)}; }
template <bool V> struct BoolC { static constexpr bool value = V; };
__device__ __forceinline__ float sum5(float a, float b, float c, float d, float e) { return ((a + b) + (c + d)) + e; }

__global__ void __launch_bounds__(512, 2) mega(Params p) {
  __shared__ __attribute__((aligned(16))) unsigned char smem[LDS_BYTES];
  LAS unsigned char* L = (LAS unsigned char*)smem;
  const int tid = threadIdx.x, l = tid & 63, w = __builtin_amdgcn_readfirstlane(tid >> 6);
  const int bid = blockIdx.x;
  const int g = l >> 4, r16 = l & 15;
  TS(0);
  if (w == CODE_PF) {
    u64 pc; asm volatile("s_getpc_b64 %0" : "=s"(pc));
    const GAS unsigned char* cp = (const GAS unsigned char*)((pc & ~127ull) + (u64)l * 128);
    const unsigned dst = (unsigned)(L_QIMG + 16384) + (unsigned)(size_t)L;
#pragma unroll
    for (int k = 0; k < 4; ++k) {
      const GAS unsigned char* q = cp + (k < 3 || l < CODE_LAST ? k * 8192 : 0);
      unsigned keep;
      asm volatile("s_mov_b32 %0, m0\n\ts_mov_b32 m0, %2\n\ts_nop 0\n\tglobal_load_lds_dword %1, off\n\ts_mov_b32 m0, %0" : "=&s"(keep) : "v"(q), "s"(dst) : "memory");
    }
  }
  {
    const bool e_ = bid < 128;
    const u64 a0 = (u64)(e_ ? p.wmod : p.wq), a1 = (u64)(e_ ? p.c : p.wuk), a2 = (u64)(e_ ? p.x : p.wuv), a3 = e_ ? (u64)p.ckv + (u64)(bid & 127) * (128 * DC * 2) : (u64)p.wo, a4 = (u64)p.ws;
    const int grp = l >> 2;
    unsigned qlo = (unsigned)a4, qhi = (unsigned)(a4 >> 32);
    qlo = grp == 3 ? (unsigned)a3 : qlo; qhi = grp == 3 ? (unsigned)(a3 >> 32) : qhi;
    qlo = grp == 2 ? (unsigned)a2 : qlo; qhi = grp == 2 ? (unsigned)(a2 >> 32) : qhi;
    qlo = grp == 1 ? (unsigned)a1 : qlo; qhi = grp == 1 ? (unsigned)(a1 >> 32) : qhi;
    qlo = grp == 0 ? (unsigned)a0 : qlo; qhi = grp == 0 ? (unsigned)(a0 >> 32) : qhi;
    const unsigned mlo = e_ ? 1u : 0u;
    const unsigned single = (grp >= 1 && grp <= 3) ? mlo : 0u;
    const unsigned po = ((l & 3) == 3 || single) ? 0u : (unsigned)(l & 3) * 1900000u;
    const u64 q = (((u64)qhi << 32) | qlo) + po;
    const unsigned dst = (unsigned)(L_QIMG + 16384) + (unsigned)(size_t)L;
    unsigned keep;
    asm volatile("s_mov_b32 %0, m0\n\ts_mov_b32 m0, %2\n\ts_nop 0\n\tglobal_load_lds_dword %1, off\n\ts_mov_b32 m0, %0" : "=&s"(keep) : "v"(q), "s"(dst) : "memory");
  }
  const __amdgpu_buffer_rsrc_t rs = __builtin_amdgcn_make_buffer_rsrc((void*)p.ws, 0, WS_USED, 0x00020000);
  const bool Ltype = bid >= 128;
  const int idx = bid & 127;
  const int ab = idx >> 6, aj = idx & 63;
  unsigned epoch_raw = 0;
  auto LOAD_EPOCH = [&]() { epoch_raw = __builtin_amdgcn_raw_buffer_load_b32(rs, OFF_EPOCH, 0, AUX_SC1); };
  auto TAG = [&]() { unsigned t = epoch_raw; asm volatile("" : "+v"(t)); return t + 1u; };

  auto ld16 = [&](int off) { return __builtin_amdgcn_raw_buffer_load_b128(rs, off, 0, AUX_SC1); };
  auto ld8 = [&](int off) { return __builtin_amdgcn_raw_buffer_load_b64(rs, off, 0, AUX_SC1); };
  auto ld4 = [&](int off) { return __builtin_amdgcn_raw_buffer_load_b32(rs, off, 0, AUX_SC1); };
  auto st16 = [&](v4u v, int off) { __builtin_amdgcn_raw_buffer_store_b128(v, rs, off, 0, AUX_SC1); };
  auto st8 = [&](v2u v, int off) { __builtin_amdgcn_raw_buffer_store_b64(v, rs, off, 0, AUX_SC1); };
  auto st4 = [&](unsigned v, int off) { __builtin_amdgcn_raw_buffer_store_b32(v, rs, off, 0, AUX_SC1); };
  auto dma_ckv = [&](int row0, int nrows, bool paced = false, int rstride = 1) {
    const u16* base = p.ckv + ((size_t)ab * SK + aj * 128) * DC;
    for (int rr = 0; rr < nrows; ++rr) {
      if (paced) asm volatile("s_waitcnt vmcnt(%0)" :: "i"(E_DEPTH - 1) : "memory");
      const int row = row0 + rr * rstride;
      const int sw = ((row & 3) << 2) | ((row >> 2) & 3);
      const u16* src = base + row * DC + ((l ^ sw) * 8);
      unsigned keep; const unsigned dst = (unsigned)(L_CKV + row * 1024) + (unsigned)(size_t)L;
      asm volatile("s_mov_b32 %0, m0\n\ts_mov_b32 m0, %2\n\ts_nop 0\n\tglobal_load_lds_dwordx4 %1, off nt\n\ts_mov_b32 m0, %0" : "=&s"(keep) : "v"(src), "s"(dst) : "memory");
    }
  };
  const v4u SENT4 = {SENT, SENT, SENT, SENT};
  auto wait_flags = [&](int off) {
    const unsigned tag = TAG();
    for (unsigned sp = 0; sp < SPIN_MAX; ++sp) {
      asm volatile("" ::: "memory");
      if (__all(ld4(off) == tag)) break;
      __builtin_amdgcn_s_sleep(2);
    }
  };

  if (!Ltype) {
    const bool t1role = idx < 40;
    const int t78 = 2 * (8 * (idx >> 4) + (idx & 7)) + ((idx >> 3) & 1);
    const bool role78 = idx < 64 && t78 < 54;
    auto fills = [&]() {
      if (t1role) __builtin_amdgcn_s_sleep(4);
      if (role78) {
#pragma unroll
        for (int k = 0; k < 3; ++k) { const int u = k * 64 + l; if (u < 160) st16(SENT4, X7 + t78 * 2560 + u * 16); }
      }
      DRAIN();
    };
    auto warm_uk = [&](int k) {
      const int ln = 64 * k + l;
      const u16* q = p.wuk + ((size_t)(128 * (idx & 3) + (ln >> 1)) * NH + (idx >> 2)) * DH + 64 * (ln & 1);
      const unsigned dst = (unsigned)(L_QIMG + 16384) + (unsigned)(size_t)L;
      unsigned keep;
      asm volatile("s_mov_b32 %0, m0\n\ts_mov_b32 m0, %2\n\ts_nop 0\n\tglobal_load_lds_dword %1, off" WARM_POL "\n\ts_mov_b32 m0, %0" : "=&s"(keep) : "v"(q), "s"(dst) : "memory");
    };
    if (t1role) {
      const int mg = idx % 10, pp1 = idx / 10;
      if (w < 5) {
        v4u wv[8];
        const int col = (r16 < 8) ? (64 * mg + 8 * r16) : (DM + 64 * mg + 8 * (r16 - 8));
        const int k0 = 160 * pp1 + 32 * w + 8 * g;
        const v4u cv = *(const GAS v4u*)(p.c + (r16 & 1) * DM + k0);
        const u16* src = p.wmod + (size_t)k0 * (6 * DM) + col;
#pragma unroll
        for (int j = 0; j < 8; ++j) wv[j] = ldnt(src + (size_t)j * (6 * DM));
        const unsigned xd = *(const GAS unsigned*)(p.x + (l >> 5) * DM + 64 * mg + 2 * (l & 31));
        LOAD_EPOCH();
        PIN();
        int a_w = L_T1RED + ((w * 2) * 128 + 8 * r16) * 4;
        int a_r = L_T1RED + ((l >> 5) * 128 + 2 * (l & 31)) * 4;
        int o_x1 = OFF_X1 + (((pp1 * 2 + (l >> 5)) * 320) + 32 * mg + (l & 31)) * 8;
        KEEP(a_w); KEEP(a_r); KEEP(o_x1);
        v4u a = silu8(cv);
        KEEP(a);
        PIN();
        v4u f[8]; tr8(wv, f);
        KEEP8(f);
        float xlo = bflo(xd), xhi = bfhi(xd);
        unsigned tag = TAG();
        KEEP(xlo); KEEP(xhi); KEEP(tag);
        TS(1);
        PIN();
        f32x4 acc[8];
#pragma unroll
        for (int i = 0; i < 8; ++i) acc[i] = mfma16(a, f[i], f32x4{0.f, 0.f, 0.f, 0.f});
        if (g == 0) {
#pragma unroll
          for (int b = 0; b < 2; ++b) {
            *(LAS f32x4*)(L + a_w + b * 512) = f32x4{acc[0][b], acc[1][b], acc[2][b], acc[3][b]};
            *(LAS f32x4*)(L + a_w + b * 512 + 16) = f32x4{acc[4][b], acc[5][b], acc[6][b], acc[7][b]};
          }
        }
        TS(2);
        BAR();
        TS(3);
        if (w == 0) {
          f32x2 s[5], c2[5];
#pragma unroll
          for (int ww = 0; ww < 5; ++ww) { s[ww] = *(LAS f32x2*)(L + a_r + ww * 1024); c2[ww] = *(LAS f32x2*)(L + a_r + ww * 1024 + 256); }
          const f32x2 mr = *(LAS f32x2*)(L + L_T1MISC + (l >> 5) * 8);
          const float xn0 = (xlo - mr[0]) * mr[1], xn1 = (xhi - mr[0]) * mr[1];
          const float one = pp1 == 0 ? 1.f : 0.f;
          const float o0 = xn0 * (sum5(c2[0][0], c2[1][0], c2[2][0], c2[3][0], c2[4][0]) + one) + sum5(s[0][0], s[1][0], s[2][0], s[3][0], s[4][0]);
          const float o1 = xn1 * (sum5(c2[0][1], c2[1][1], c2[2][1], c2[3][1], c2[4][1]) + one) + sum5(s[0][1], s[1][1], s[2][1], s[3][1], s[4][1]);
          { const v4u v = pair2(pk2(o0, o1), tag); if ((l & 1) == 0) st16(v, o_x1); }
          TS(4);
        }
        dma_ckv(T1_EARLY * 3 + w, (128 - T1_EARLY * 3 - w + 7) / 8, true, 8);
      } else if (w < 7) {
        const int b = w - 5;
        LOAD_EPOCH();
        float v[10]; float s = 0.f;
#pragma unroll
        for (int k = 0; k < 5; ++k) { const unsigned d = *(const GAS unsigned*)(p.x + b * DM + 2 * (l + 64 * k)); v[2 * k] = bflo(d); v[2 * k + 1] = bfhi(d); s += v[2 * k] + v[2 * k + 1]; }
        const float mean = wave_sum(s) * (1.f / DM);
        float q = 0.f;
#pragma unroll
        for (int k = 0; k < 10; ++k) { const float d = v[k] - mean; q += d * d; }
        const float var = wave_sum(q) * (1.f / DM);
        *(LAS f32x2*)(L + L_T1MISC + b * 8) = f32x2{mean, __builtin_amdgcn_rsqf(var + 1e-5f)};
        __builtin_amdgcn_s_sleep(E_DELAY / 2);
        dma_ckv(b, T1_EARLY, false, 3);
        BAR();
        dma_ckv(T1_EARLY * 3 + w, (128 - T1_EARLY * 3 - w + 7) / 8, true, 8);
      } else {
        __builtin_amdgcn_s_sleep(4);
        LOAD_EPOCH();
        fills();
        if (l == 0) st4(TAG(), OFF_FLAGS + idx * FSTR);
        dma_ckv(2, T1_EARLY, false, 3);
        BAR();
        dma_ckv(T1_EARLY * 3 + w, (128 - T1_EARLY * 3 - w + 7) / 8, true, 8);
      }
    } else if (w < 7) {
      __builtin_amdgcn_s_sleep(E_DELAY);
      LOAD_EPOCH();
      dma_ckv(w, 17, true, 7);
    } else {
      LOAD_EPOCH();
      fills();
      if (l == 0) st4(TAG(), OFF_FLAGS + idx * FSTR);
      dma_ckv(119, 9, true);
    }
    v4u wa0[8], wa1[8], wa2[8], wdn[8];
    {
      const unsigned ef0 = ld4(OFF_EFLAGS + l * FSTR), ef1 = ld4(OFF_EFLAGS + (64 + l) * FSTR);
      int a_qw[2], a_qr[4], a_kr[4];
      const int key = 16 * w + r16;
#pragma unroll
      for (int u = 0; u < 2; ++u) a_qw[u] = L_QIMG + (8 * u + w) * 1024 + ((l ^ (8 * u + w)) * 16);
#pragma unroll
      for (int t = 0; t < 4; ++t) { a_qr[t] = L_QIMG + r16 * 1024 + (((4 * t + g) ^ r16) * 16); a_kr[t] = L_CKV + key * 1024 + (((4 * t + g) ^ (((key & 3) << 2) | ((key >> 2) & 3))) * 16); }
      int a_vr[2];
#pragma unroll
      for (int u = 0; u < 2; ++u) a_vr[u] = L_CKV + (4 * g + (r16 >> 2)) * 1024 + (((8 * w + 4 * u + (l & 3)) ^ (((r16 >> 2) << 2) | g)) * 16);
      int a_mxw = L_MX + r16 * 32 + w * 4, a_mxr = L_MX + r16 * 32;
      int a_pw = L_PIMG + (w >> 1) * 1024 + l * 16 + (w & 1) * 8, a_pr = L_PIMG + l * 16;
      int a_ow[2], a_or[4];
#pragma unroll
      for (int u = 0; u < 2; ++u) a_ow[u] = L_QIMG + r16 * 1024 + (((8 * w + 4 * u + g) ^ r16) * 16);
#pragma unroll
      for (int k = 0; k < 4; ++k) a_or[k] = L_QIMG + (4 * w + k) * 1024 + ((l ^ ((4 * w + k) & 15)) * 16);
      KEEP(a_ow[0]); KEEP(a_ow[1]); KEEP4(a_or);
      int o_st = X4 + ((ab * NH + 4 * w) * 64 + aj) * 1024 + l * 16, o_ss = X4S + ((ab * 64 + aj) * NH + 16 * (w & 1) + r16) * 16;
      int o_q = X3 + ab * 32768 + tid * 16;
      KEEP(a_qw[0]); KEEP(a_qw[1]); KEEP4(a_qr); KEEP4(a_kr); KEEP(a_vr[0]); KEEP(a_vr[1]); KEEP(a_mxw); KEEP(a_mxr); KEEP(a_pw); KEEP(a_pr); KEEP(o_st); KEEP(o_ss); KEEP(o_q);
      DRAIN();
      { const unsigned tag = TAG(); if (!__all((ef0 == tag) & (ef1 == tag))) { wait_flags(OFF_EFLAGS + l * FSTR); wait_flags(OFF_EFLAGS + (64 + l) * FSTR); } }
      BAR();
      TS(11);
      v4u qa[4];
#pragma unroll
      for (int k = 0; k < 4; ++k) qa[k] = ld16(o_q + k * 8192);
      PIN();
      v4u qk[16], pv[16];
#pragma unroll
      for (int s = 0; s < 16; ++s) qk[s] = *(LAS v4u*)(L + a_kr[s & 3] + (s >> 2) * 256);
#pragma unroll
      for (int s = 0; s < 4; ++s)
#pragma unroll
        for (int t = 0; t < 4; ++t) {
          v2u h2[2];
#pragma unroll
          for (int hf = 0; hf < 2; ++hf)
            h2[hf] = __builtin_bit_cast(v2u, __builtin_amdgcn_ds_read_tr16_b64_v4i16((LAS s16x4*)(L + a_vr[t >> 1] + (32 * s + 16 * hf) * 1024 + 8 * (t & 1))));
          pv[s * 4 + t] = v4u{h2[0].x, h2[0].y, h2[1].x, h2[1].y};
        }
#pragma unroll
      for (int s = 0; s < 16; ++s) { KEEP(qk[s]); KEEP(pv[s]); }
      TS(12);
      PIN();
      for (unsigned sp = 0; sp < SPIN_MAX; ++sp) {
        if (__all(umax4a(qa[3], umax4a(qa[2], umax4a(qa[1], umax4(qa[0])))) != SENT)) break;
        if (Q_SLEEP) __builtin_amdgcn_s_sleep(Q_SLEEP);
        asm volatile("" ::: "memory");
#pragma unroll
        for (int k = 0; k < 4; ++k) qa[k] = ld16(o_q + k * 8192);
      }
#pragma unroll
      for (int k = 0; k < 4; ++k) *(LAS v4u*)(L + a_qw[k & 1] + (k >> 1) * 16384) = qa[k];
      TS(13);
      BAR();
      TS(14);
      f32x4 sacc[2][2];
#pragma unroll
      for (int ht = 0; ht < 2; ++ht) { sacc[ht][0] = f32x4{0.f, 0.f, 0.f, 0.f}; sacc[ht][1] = f32x4{0.f, 0.f, 0.f, 0.f}; }
      {
        v4u fa[8], fb[8];
#pragma unroll
        for (int s = 0; s < 8; ++s) fa[s] = *(LAS v4u*)(L + a_qr[s & 3] + (s >> 2) * 256);
#pragma unroll
        for (int s = 0; s < 8; ++s) fb[s] = *(LAS v4u*)(L + a_qr[s & 3] + (2 + (s >> 2)) * 256);
        PIN();
#pragma unroll
        for (int s = 0; s < 8; ++s) sacc[0][s & 1] = mfma16(qk[s], fa[s], sacc[0][s & 1]);
        PIN();
#pragma unroll
        for (int s = 0; s < 8; ++s) fa[s] = *(LAS v4u*)(L + a_qr[s & 3] + 16384 + (s >> 2) * 256);
        PIN();
#pragma unroll
        for (int s = 0; s < 8; ++s) sacc[0][s & 1] = mfma16(qk[8 + s], fb[s], sacc[0][s & 1]);
        PIN();
#pragma unroll
        for (int s = 0; s < 8; ++s) fb[s] = *(LAS v4u*)(L + a_qr[s & 3] + 16384 + (2 + (s >> 2)) * 256);
        PIN();
#pragma unroll
        for (int s = 0; s < 8; ++s) sacc[1][s & 1] = mfma16(qk[s], fa[s], sacc[1][s & 1]);
#pragma unroll
        for (int s = 0; s < 8; ++s) sacc[1][s & 1] = mfma16(qk[8 + s], fb[s], sacc[1][s & 1]);
      }
      PIN();
      unsigned lf[2] = {0u, 0u};
      if (w == 6) lf[0] = ld4(OFF_FLAGS + l * FSTR);
      f32x4 sc[2];
#pragma unroll
      for (int ht = 0; ht < 2; ++ht) {
        sc[ht] = sacc[ht][0] + sacc[ht][1];
        *(LAS float*)(L + a_mxw + ht * 512) = x16_max(fmaxf(fmaxf(sc[ht][0], sc[ht][1]), fmaxf(sc[ht][2], sc[ht][3])));
      }
      TS(15);
      BAR();
      TS(16);
      float m[2];
#pragma unroll
      for (int ht = 0; ht < 2; ++ht) {
        const f32x4 m0 = *(LAS f32x4*)(L + a_mxr + ht * 512), m1 = *(LAS f32x4*)(L + a_mxr + ht * 512 + 16);
        m[ht] = fmaxf(fmaxf(fmaxf(m0[0], m0[1]), fmaxf(m0[2], m0[3])), fmaxf(fmaxf(m1[0], m1[1]), fmaxf(m1[2], m1[3])));
        *(LAS v2u*)(L + a_pw + ht * 4096) = v2u{pk2(__builtin_amdgcn_exp2f(sc[ht][0] - m[ht]), __builtin_amdgcn_exp2f(sc[ht][1] - m[ht])), pk2(__builtin_amdgcn_exp2f(sc[ht][2] - m[ht]), __builtin_amdgcn_exp2f(sc[ht][3] - m[ht]))};
      }
      BAR();
      v4u pf[2][4];
#pragma unroll
      for (int ht = 0; ht < 2; ++ht)
#pragma unroll
        for (int s = 0; s < 4; ++s) pf[ht][s] = *(LAS v4u*)(L + a_pr + ht * 4096 + s * 1024);
      PIN();
#pragma unroll
      for (int ht = 0; ht < 2; ++ht) {
        f32x4 o[4];
#pragma unroll
        for (int t = 0; t < 4; ++t) o[t] = f32x4{0.f, 0.f, 0.f, 0.f};
#pragma unroll
        for (int s = 0; s < 4; ++s)
#pragma unroll
          for (int t = 0; t < 4; ++t) o[t] = mfma16(pv[s * 4 + t], pf[ht][s], o[t]);
        *(LAS v4u*)(L + a_ow[0] + ht * 16384) = v4u{pk2(o[0][0], o[0][1]), pk2(o[0][2], o[0][3]), pk2(o[1][0], o[1][1]), pk2(o[1][2], o[1][3])};
        *(LAS v4u*)(L + a_ow[1] + ht * 16384) = v4u{pk2(o[2][0], o[2][1]), pk2(o[2][2], o[2][3]), pk2(o[3][0], o[3][1]), pk2(o[3][2], o[3][3])};
      }
      float lsum = 0.f;
      if (w < 2) {
        const v4u ones = {0x3F803F80u, 0x3F803F80u, 0x3F803F80u, 0x3F803F80u};
        f32x4 lacc = {0.f, 0.f, 0.f, 0.f};
        if (w == 0) {
#pragma unroll
          for (int s = 0; s < 4; ++s) lacc = mfma16(ones, pf[0][s], lacc);
        } else {
#pragma unroll
          for (int s = 0; s < 4; ++s) lacc = mfma16(ones, pf[1][s], lacc);
        }
        lsum = lacc[0];
      }
      BAR();
#pragma unroll
      for (int k = 0; k < 4; ++k) st16(*(LAS v4u*)(L + a_or[k]), o_st + k * 65536);
      TS(18);
      DRAIN();
      BAR();
      if (w < 2 && g == 0) { const unsigned tag = TAG(); st16(v4u{__float_as_uint(w == 0 ? m[0] : m[1]), tag, __float_as_uint(lsum), tag}, o_ss); }
      PIN();
      if (role78) {
        if (w >= 4) {
          const u16* wsrc78 = (((l & 7) < 4) ? p.wg : p.wu) + 32 * t78 + 8 * (l & 3) + (size_t)(64 * (w & 3) + 32 * ((l >> 3) & 1) + 8 * g) * DF;
#pragma unroll
          for (int j = 0; j < 8; ++j) wa0[j] = ldnt(wsrc78 + (size_t)j * DF);
#pragma unroll
          for (int j = 0; j < 8; ++j) wa1[j] = ldnt(wsrc78 + (size_t)(256 + j) * DF);
          if (w < 6) {
#pragma unroll
            for (int j = 0; j < 8; ++j) wa2[j] = ldnt(wsrc78 + (size_t)(512 + j) * DF);
          }
        }
        if (w >= 3) {
          const u16* src = p.wd + (size_t)(32 * t78 + 8 * g) * DM + 128 * (w - 3) + 8 * r16;
#pragma unroll
          for (int j = 0; j < 8; ++j) wdn[j] = ldnt(src + (size_t)j * DM);
        }
        PIN();
      }
      TS(17);
      if (w == 6) {
        const unsigned tag = TAG();
        if (!__all(lf[0] == tag)) wait_flags(OFF_FLAGS + l * FSTR);
      }
      BAR();
      TS(19);
    }
    if (role78) {
      v4u fa[3][8], fd[8];
      if (w >= 4) { tr8(wa0, fa[0]); tr8(wa1, fa[1]); if (w < 6) tr8(wa2, fa[2]); KEEP8(fa[0]); KEEP8(fa[1]); if (w < 6) KEEP8(fa[2]); }
      if (w >= 3) { tr8(wdn, fd); KEEP8(fd); }
      const int b = w / 3, cc = 32 * (w % 3) + (l & 31), half = l >> 5;
      const bool act_lane = (w < 6) && (cc < 80);
      float x1[4] = {0.f, 0.f, 0.f, 0.f}, cb4[4], sb4[4];
      int a_h2w = L_RED + (b * DM + 8 * cc + 4 * half) * 2;
      if (w < 6) {
        const int ccc = cc < 80 ? cc : 79;
        const int mo = 8 * ccc + 4 * half;
        v2u gav, sbv, cbv;
        for (unsigned sp = 0; sp < SPIN_MAX; ++sp) {
          asm volatile("" ::: "memory");
          gav = ld8(XM + (b * 2560 + mo) * 2); sbv = ld8(XM + (b * 2560 + 640 + mo) * 2); cbv = ld8(XM + (b * 2560 + 1280 + mo) * 2);
          if (__all(umax3(umax3(gav.x, gav.y, sbv.x), umax3(sbv.y, cbv.x, cbv.y), 0u) != SENT)) break;
          __builtin_amdgcn_s_sleep(8);
        }
        const v2u xv = *(const GAS v2u*)(p.x + b * DM + mo);
        float xf[4] = {bflo(xv.x), bfhi(xv.x), bflo(xv.y), bfhi(xv.y)}, ga4[4] = {bflo(gav.x), bfhi(gav.x), bflo(gav.y), bfhi(gav.y)};
        cb4[0] = 1.f + bflo(cbv.x); cb4[1] = 1.f + bfhi(cbv.x); cb4[2] = 1.f + bflo(cbv.y); cb4[3] = 1.f + bfhi(cbv.y);
        sb4[0] = bflo(sbv.x); sb4[1] = bfhi(sbv.x); sb4[2] = bflo(sbv.y); sb4[3] = bfhi(sbv.y);
        if (!act_lane) { ga4[0] = ga4[1] = ga4[2] = ga4[3] = 0.f; xf[0] = xf[1] = xf[2] = xf[3] = 0.f; }
        int off = X6 + ((4 * half * 2 + b) * DM + 8 * ccc) * 2;
        int a_st = L_MISC + w * 8;
        unsigned one_lo = 0x00003F80u, one_hi = 0x3F800000u;
        KEEP(one_lo); KEEP(one_hi);
        KEEP4(xf); KEEP4(ga4); KEEP4(cb4); KEEP4(sb4); KEEP(off); KEEP(a_st); KEEP(a_h2w);
        PIN();
        v4u a[4];
        {
          unsigned pend = 15u;
          for (unsigned sp = 0; sp < SPIN_MAX; ++sp) {
            asm volatile("" ::: "memory");
#pragma unroll
            for (int k = 0; k < 4; ++k) if (pend >> k & 1u) a[k] = ld16(off + k * 2560);
#pragma unroll
            for (int k = 0; k < 4; ++k) if (pend >> k & 1u) { if (__all(umax4(a[k]) != SENT)) pend &= ~(1u << k); }
            if (!pend) break;
          }
        }
        TS(24);
        float acc[8];
#pragma unroll
        for (int d = 0; d < 4; ++d) {
          acc[2 * d] = dot2(a[3][d], one_lo, dot2(a[2][d], one_lo, dot2(a[1][d], one_lo, bflo(a[0][d]))));
          acc[2 * d + 1] = dot2(a[3][d], one_hi, dot2(a[2][d], one_hi, dot2(a[1][d], one_hi, bfhi(a[0][d]))));
        }
#pragma unroll
        for (int e = 0; e < 4; ++e) x1[e] = xf[e] + ga4[e] * rs32(acc[e], acc[e + 4]);
        const float s1 = wave_sum((x1[0] + x1[1]) + (x1[2] + x1[3]));
        const float s2 = wave_sum((x1[0] * x1[0] + x1[1] * x1[1]) + (x1[2] * x1[2] + x1[3] * x1[3]));
        *(LAS f32x2*)(L + a_st) = f32x2{s1, s2};
      }
      int a_st2 = L_MISC + b * 24;
      int a_ar = L_RED + ((r16 & 1) * DM + 64 * (w & 3) + 32 * (r16 >> 3) + 8 * g) * 2;
      int a_rw = L_RED2 + ((((w & 3) * 2 + (g >> 1)) * 2) * 64 + 8 * (r16 & 7)) * 4;
      KEEP(a_st2); KEEP(a_ar); KEEP(a_rw);
      BAR();
      TS(25);
      if (act_lane) {
        const f32x2 t0 = *(LAS f32x2*)(L + a_st2), t1 = *(LAS f32x2*)(L + a_st2 + 8), t2 = *(LAS f32x2*)(L + a_st2 + 16);
        const float mean = (t0[0] + t1[0] + t2[0]) * (1.f / DM);
        const float var = fmaxf((t0[1] + t1[1] + t2[1]) * (1.f / DM) - mean * mean, 0.f);
        const float rstd = __builtin_amdgcn_rsqf(var + 1e-5f);
        float hv[4];
#pragma unroll
        for (int e = 0; e < 4; ++e) hv[e] = (x1[e] - mean) * rstd * cb4[e] + sb4[e];
        *(LAS v2u*)(L + a_h2w) = v2u{pk2(hv[0], hv[1]), pk2(hv[2], hv[3])};
      }
      BAR();
      TS(26);
      if (w >= 4) {
        const v4u a0 = *(LAS v4u*)(L + a_ar), a1 = *(LAS v4u*)(L + a_ar + 512);
        v4u a2 = a0;
        if (w < 6) a2 = *(LAS v4u*)(L + a_ar + 1024);
        f32x4 acc[8];
#pragma unroll
        for (int i = 0; i < 8; ++i) acc[i] = mfma16(a0, fa[0][i], f32x4{0.f, 0.f, 0.f, 0.f});
#pragma unroll
        for (int i = 0; i < 8; ++i) acc[i] = mfma16(a1, fa[1][i], acc[i]);
        if (w < 6) {
#pragma unroll
          for (int i = 0; i < 8; ++i) acc[i] = mfma16(a2, fa[2][i], acc[i]);
        }
        if ((g == 0 && r16 < 8) || (g == 2 && r16 >= 8)) {
#pragma unroll
          for (int b2 = 0; b2 < 2; ++b2) {
            *(LAS f32x4*)(L + a_rw + b2 * 256) = f32x4{acc[0][b2], acc[1][b2], acc[2][b2], acc[3][b2]};
            *(LAS f32x4*)(L + a_rw + b2 * 256 + 16) = f32x4{acc[4][b2], acc[5][b2], acc[6][b2], acc[7][b2]};
          }
        }
      }
      int a_gr = L_RED2 + ((l >> 5) * 64 + (l & 31)) * 4, a_aw = L_MISC + 512 + l * 2, a_acr = L_MISC + 512 + ((r16 & 1) * 32 + 8 * g) * 2;
      int o_x7 = X7 + (t78 * 2 * DM + 128 * (w - 3) + 8 * r16) * 2;
      KEEP(a_gr); KEEP(a_aw); KEEP(a_acr); KEEP(o_x7);
      BAR();
      TS(27);
      if (w == 0) {
        float gv[8], uv[8];
#pragma unroll
        for (int k = 0; k < 8; ++k) { gv[k] = *(LAS float*)(L + a_gr + k * 512); uv[k] = *(LAS float*)(L + a_gr + k * 512 + 128); }
        const float gs = ((gv[0] + gv[1]) + (gv[2] + gv[3])) + ((gv[4] + gv[5]) + (gv[6] + gv[7]));
        const float us = ((uv[0] + uv[1]) + (uv[2] + uv[3])) + ((uv[4] + uv[5]) + (uv[6] + uv[7]));
        *(LAS u16*)(L + a_aw) = (u16)pk2(silu(gs) * us, 0.f);
      }
      BAR();
      TS(28);
      if (w >= 3) {
        const v4u a = *(LAS v4u*)(L + a_acr);
        f32x4 d[8];
#pragma unroll
        for (int i = 0; i < 8; ++i) d[i] = mfma16(a, fd[i], f32x4{0.f, 0.f, 0.f, 0.f});
        if (g == 0) {
#pragma unroll
          for (int b2 = 0; b2 < 2; ++b2)
            st16(v4u{pk2(d[0][b2], d[1][b2]), pk2(d[2][b2], d[3][b2]), pk2(d[4][b2], d[5][b2]), pk2(d[6][b2], d[7][b2])}, o_x7 + b2 * DM * 2);
        }
        TS(29);
      }
    } else if (idx >= 94 && idx < 114) {
      const int n = idx - 94;
      float base0 = 0.f, base1 = 0.f, gb0 = 0.f, gb1 = 0.f;
      if (w == 7 && l < 32) {
        const int b = l >> 4, pr = l & 15, m = 32 * n + 2 * pr;
        unsigned ga = 0, gb = 0, ap[8];
        for (unsigned sp = 0; sp < SPIN_MAX; ++sp) {
          asm volatile("" ::: "memory");
          ga = ld4(XM + (b * 2560 + m) * 2); gb = ld4(XM + (b * 2560 + 1920 + m) * 2);
#pragma unroll
          for (int k = 0; k < 8; ++k) ap[k] = ld4(X6 + ((k * 2 + b) * DM + m) * 2);
          if (__all(umax3(umax3(umax3(ap[0], ap[1], ap[2]), umax3(ap[3], ap[4], ap[5]), umax3(ap[6], ap[7], ga)), gb, 0u) != SENT)) break;
          __builtin_amdgcn_s_sleep(8);
        }
        const unsigned xd = *(const GAS unsigned*)(p.x + b * DM + m);
        float s0 = 0.f, s1 = 0.f;
#pragma unroll
        for (int k = 0; k < 8; ++k) { s0 += bflo(ap[k]); s1 += bfhi(ap[k]); }
        base0 = bflo(xd) + bflo(ga) * s0; base1 = bfhi(xd) + bfhi(ga) * s1; gb0 = bflo(gb); gb1 = bfhi(gb);
        KEEP(base0); KEEP(base1); KEEP(gb0); KEEP(gb1);
      }
      int a_fr = L_RED + ((l >> 4) * 32 + 2 * (l & 15)) * 4;
      KEEP(a_fr);
      if (w < 7) {
        const int task = 8 * w + (l >> 3), b = (l >> 2) & 1, ch = l & 3;
        const bool valid = task < 54;
        int off = X7 + (((valid ? task : 0) * 2 + b) * DM + 32 * n + 8 * ch) * 2;
        int a_w = L_RED + (w * 64 + b * 32 + 8 * ch + 4 * (l >> 5) + 2 * ((l >> 4) & 1)) * 4;
        unsigned msk = valid ? 0xFFFFFFFFu : 0u;
        KEEP(off); KEEP(a_w); KEEP(msk);
        PIN();
        v4u a;
        for (unsigned sp = 0; sp < SPIN_MAX; ++sp) {
          asm volatile("" ::: "memory");
          a = ld16(off);
          if (__all(umax4(a) != SENT)) break;
          if (LATE_SLEEP) __builtin_amdgcn_s_sleep(LATE_SLEEP);
        }
        TS(29);
        float v[8];
#pragma unroll
        for (int d = 0; d < 4; ++d) { const unsigned x = a[d] & msk; v[2 * d] = bflo(x); v[2 * d + 1] = bfhi(x); }
        float t0 = rs16(rs32(v[0], v[4]), rs32(v[2], v[6])), t1 = rs16(rs32(v[1], v[5]), rs32(v[3], v[7]));
        t0 += dppf<0x128>(t0); t1 += dppf<0x128>(t1);
        *(LAS f32x2*)(L + a_w) = f32x2{t0, t1};
      }
      BAR();
      if (w == 7 && l < 32) {
        f32x2 v[7];
#pragma unroll
        for (int k = 0; k < 7; ++k) v[k] = *(LAS f32x2*)(L + a_fr + k * 256);
        const float s0 = ((v[0][0] + v[1][0]) + (v[2][0] + v[3][0])) + ((v[4][0] + v[5][0]) + v[6][0]);
        const float s1 = ((v[0][1] + v[1][1]) + (v[2][1] + v[3][1])) + ((v[4][1] + v[5][1]) + v[6][1]);
        *(GAS unsigned*)(p.out + (l >> 4) * DM + 32 * n + 2 * (l & 15)) = pk2(base0 + gb0 * s0, base1 + gb1 * s1);
        if (n == 0 && l == 0) st4(TAG(), OFF_EPOCH);
      }
    }
  } else {
    const int hh = idx >> 2, sub = idx & 3;
    if (w < 5) {
      v4u wv2[8], uk[8];
      const u16* src2 = p.wq + (size_t)(160 * sub + 32 * w + 8 * g) * (NH * DH) + hh * DH + 8 * r16;
      auto load_t3 = [&]() {
        const u16* src = p.wuk + ((size_t)(128 * sub + 32 * (w & 3) + g) * NH + hh) * DH + 8 * r16;
#pragma unroll
        for (int t = 0; t < 2; ++t)
#pragma unroll
          for (int i = 0; i < 4; ++i) uk[4 * t + i] = ldnt(src + (size_t)(16 * t + 4 * i) * NH * DH);
      };
      auto load_t23 = [&]() {
#pragma unroll
        for (int j = 0; j < 8; ++j) wv2[j] = ldnt(src2 + (size_t)j * (NH * DH));

      };
      __builtin_amdgcn_s_sleep(L_DELAY);
      load_t23();
      LOAD_EPOCH();
      PIN();
      {
        unsigned tag = TAG();
        v4u f[8]; tr8(wv2, f);
        KEEP8(f);

        TS(2);
        int off = OFF_X1 + ((l >> 3) * 320 + (160 * sub + 32 * w) / 2 + 2 * (l & 7)) * 8;
        int a_gw = L_X1W + w * 512 + (l >> 3) * 64 + (l & 7) * 8, a_gr = L_X1W + w * 512 + (r16 & 7) * 64 + g * 16;
        int a_w = L_RED2 + (((w * 2 + (g & 1)) * 2) * 128 + 8 * r16) * 4;
        int a_r = L_RED2 + ((w & 1) * 128 + 2 * l) * 4, o_x2 = X2 + ((hh * 4 + sub) * 2 + (w & 1)) * 512 + l * 8;
        KEEP8(f); KEEP(off); KEEP(a_w); KEEP(tag); KEEP(a_r); KEEP(o_x2); KEEP(a_gw); KEEP(a_gr);
        TS(5);
        PIN();
        v4u u0;
        for (unsigned sp = 0; sp < SPIN_MAX; ++sp) {
          asm volatile("" ::: "memory");
          u0 = ld16(off);
          if (__all(((u0.y ^ tag) | (u0.w ^ tag)) == 0u)) break;
          if (P_SLEEP) __builtin_amdgcn_s_sleep(P_SLEEP);
        }
        *(LAS v2u*)(L + a_gw) = v2u{u0.x, u0.z};
        const v4u a = *(LAS v4u*)(L + a_gr);
        TS(6);
        f32x4 acc[8];
#pragma unroll
        for (int i = 0; i < 8; ++i) acc[i] = mfma16(a, f[i], f32x4{0.f, 0.f, 0.f, 0.f});
        if (g < 2) {
#pragma unroll
          for (int b2 = 0; b2 < 2; ++b2) {
            *(LAS f32x4*)(L + a_w + b2 * 512) = f32x4{acc[0][b2] + acc[0][b2 + 2], acc[1][b2] + acc[1][b2 + 2], acc[2][b2] + acc[2][b2 + 2], acc[3][b2] + acc[3][b2 + 2]};
            *(LAS f32x4*)(L + a_w + b2 * 512 + 16) = f32x4{acc[4][b2] + acc[4][b2 + 2], acc[5][b2] + acc[5][b2 + 2], acc[6][b2] + acc[6][b2 + 2], acc[7][b2] + acc[7][b2 + 2]};
          }
        }
        PIN();
        if (w == 2 || w == 3) load_t3();
        PIN();
        BAR();
        TS(7);
        if (w < 2) {
          f32x2 v[10];
#pragma unroll
          for (int k = 0; k < 10; ++k) v[k] = *(LAS f32x2*)(L + a_r + k * 1024);
          const float s0 = sum5(v[0][0], v[1][0], v[2][0], v[3][0], v[4][0]) + sum5(v[5][0], v[6][0], v[7][0], v[8][0], v[9][0]);
          const float s1 = sum5(v[0][1], v[1][1], v[2][1], v[3][1], v[4][1]) + sum5(v[5][1], v[6][1], v[7][1], v[8][1], v[9][1]);
          { const v4u v = pair2(pk2(s0, s1), tag); if ((l & 1) == 0) st16(v, o_x2); }
          TS(8);
          PIN();
          load_t3();
        }
      }
      {
        int off = X2 + hh * 4096 + l * 16;
        int a_gw[4];
#pragma unroll
        for (int k = 0; k < 4; ++k) { const int row = 2 * k + (l >> 5), pc = l & 31; a_gw[k] = L_X2W + (w & 3) * 2048 + row * 256 + (((pc >> 1) ^ row) * 16) + (pc & 1) * 8; }
        int a_gr[4];
#pragma unroll
        for (int s = 0; s < 4; ++s) a_gr[s] = L_X2W + (w & 3) * 2048 + (r16 & 7) * 256 + (((4 * s + g) ^ (r16 & 7)) * 16);
        int a_w = L_RED + ((g & 1) * 128 + 32 * (w & 3) + r16) * 2;
        int o_x3 = X3 + ((l >> 4) * NH + hh) * 1024 + sub * 256 + (l & 15) * 16;
        unsigned tag = TAG();
        KEEP(off); KEEP(a_w); KEEP(o_x3); KEEP4(a_gw); KEEP4(a_gr); KEEP(tag);
        if (w < 4) {
#pragma unroll
          for (int t = 0; t < 2; ++t) {
#pragma unroll
            for (int i = 0; i < 4; ++i) *(LAS v4u*)(L + L_UKT + w * 4096 + (4 * i + g) * 256 + ((r16 ^ (4 * i + g)) * 16)) = uk[4 * t + i];
#pragma unroll
            for (int s = 0; s < 4; ++s) uk[4 * t + s] = *(LAS v4u*)(L + L_UKT + w * 4096 + r16 * 256 + (((4 * s + g) ^ r16) * 16));
            KEEP(uk[4 * t]); KEEP(uk[4 * t + 1]); KEEP(uk[4 * t + 2]); KEEP(uk[4 * t + 3]);
          }
          TS(1);
        }
        PIN();
        if (w < 4) {
          v4u u[4];
          for (unsigned sp = 0; sp < SPIN_MAX; ++sp) {
            asm volatile("" ::: "memory");
#pragma unroll
            for (int k = 0; k < 4; ++k) u[k] = ld16(off + k * 1024);
            const unsigned bad = (((u[0].y ^ tag) | (u[0].w ^ tag)) | ((u[1].y ^ tag) | (u[1].w ^ tag))) | (((u[2].y ^ tag) | (u[2].w ^ tag)) | ((u[3].y ^ tag) | (u[3].w ^ tag)));
            if (__all(bad == 0u)) break;
            if (P_SLEEP) __builtin_amdgcn_s_sleep(P_SLEEP);
          }
#pragma unroll
          for (int k = 0; k < 4; ++k) *(LAS v2u*)(L + a_gw[k]) = v2u{u[k].x, u[k].z};
          v4u a[4];
#pragma unroll
          for (int s = 0; s < 4; ++s) a[s] = *(LAS v4u*)(L + a_gr[s]);
          TS(9);
          const float sc = 0.08838834764831845f * 1.4426950408889634f;
          f32x4 acc[2][2];
#pragma unroll
          for (int t = 0; t < 2; ++t) { acc[t][0] = mfma16(a[0], uk[4 * t], f32x4{0.f, 0.f, 0.f, 0.f}); acc[t][1] = mfma16(a[1], uk[4 * t + 1], f32x4{0.f, 0.f, 0.f, 0.f}); }
#pragma unroll
          for (int t = 0; t < 2; ++t) { acc[t][0] = mfma16(a[2], uk[4 * t + 2], acc[t][0]); acc[t][1] = mfma16(a[3], uk[4 * t + 3], acc[t][1]); }
#pragma unroll
          for (int t = 0; t < 2; ++t) {
            const f32x4 v = acc[t][0] + acc[t][1];
            const float tot = rs16(v[0] + v[2], v[1] + v[3]) * sc;
            if (g < 2) *(LAS u16*)(L + a_w + 32 * t) = (u16)pk2(tot, tot);
          }
        }
        BAR();
        if (w == 0 && l < 32) {
          st16(*(LAS v4u*)(L + L_RED + l * 16), o_x3);
          TS(10);
        }
      }
    } else {
      if (w == 7) {
        if (l >= 32) st16(SENT4, X3 + (((l - 32) >> 4) * NH + hh) * 1024 + sub * 256 + (l & 15) * 16);
        if (l < 32) st16(SENT4, X5 + idx * 512 + l * 16);
        else if (l < 48 && idx < 80) st16(SENT4, X6 + (((idx / 10) * 2 + ((l - 32) >> 3)) * DM + 64 * (idx % 10)) * 2 + (l & 7) * 16);
        else if (l < 48 && idx < 120) st16(SENT4, XM + (((l - 32) >> 3) * 2560 + 64 * (idx - 80)) * 2 + (l & 7) * 16);
        LOAD_EPOCH();
        DRAIN();
        if (l == 0) st4(TAG(), OFF_EFLAGS + idx * FSTR);
      } else {
        __builtin_amdgcn_s_sleep(20);
        LOAD_EPOCH();
      }
      BAR();
      BAR();
    }
    const int h5 = idx >> 2, cq = idx & 3;
    v4u w5[8], w6[2][8];
    if (w < 4) {
      const u16* src = p.wuv + ((size_t)(128 * cq + 32 * w + 8 * g) * NH + h5) * DH + 8 * r16;
#pragma unroll
      for (int j = 0; j < 8; ++j) w5[j] = ldnt(src + (size_t)j * (NH * DH));
      if (idx < 80) {
        const int kk = (l >> 3) & 1, ch = l & 7, pp = idx / 10, n = idx % 10;
        const u16* src6 = p.wo + (size_t)(512 * pp + 64 * w + 32 * kk + 8 * g) * DM + 64 * n + 8 * ch;
#pragma unroll
        for (int k = 0; k < 2; ++k)
#pragma unroll
          for (int j = 0; j < 8; ++j) w6[k][j] = ldnt(src6 + (size_t)(256 * k + j) * DM);
      }
    }
    PIN();
    if (idx >= 80 && idx < 120) {
      const int tb = idx - 80, wv = w & 3;
      if (w >= 4) {
        v4u wpre[3][8], cv[3];
        const int kk = (l >> 3) & 1, ch = l & 7;
#pragma unroll
        for (int k = 0; k < 3; ++k) cv[k] = *(const GAS v4u*)(p.c + (r16 & 1) * DM + 64 * (k < 2 || wv < 2 ? wv + 4 * k : wv) + 32 * (r16 >> 3) + 8 * g);
        const u16* src = p.wmod + (size_t)(64 * wv + 32 * kk + 8 * g) * (6 * DM) + 2 * DM + 64 * tb + 8 * ch;
#pragma unroll
        for (int k = 0; k < 2; ++k)
#pragma unroll
          for (int j = 0; j < 8; ++j) wpre[k][j] = ldnt(src + (size_t)(256 * k + j) * (6 * DM));
        if (wv < 2) {
#pragma unroll
          for (int j = 0; j < 8; ++j) wpre[2][j] = ldnt(src + (size_t)(512 + j) * (6 * DM));
        }
        PIN();
        v4u a[3];
#pragma unroll
        for (int k = 0; k < 3; ++k) a[k] = silu8(cv[k]);
        f32x4 acc[8];
#pragma unroll
        for (int i = 0; i < 8; ++i) acc[i] = f32x4{0.f, 0.f, 0.f, 0.f};
#pragma unroll
        for (int k = 0; k < 3; ++k) {
          if (k < 2 || wv < 2) {
            v4u f[8];
            tr8(wpre[k], f);
#pragma unroll
            for (int i = 0; i < 8; ++i) acc[i] = mfma16(a[k], f[i], acc[i]);
          }
        }
        if ((g == 0 && r16 < 8) || (g == 2 && r16 >= 8)) {
#pragma unroll
          for (int b2 = 0; b2 < 2; ++b2) {
            LAS float* d = (LAS float*)(L + L_X5W) + ((wv * 2 + (g >> 1)) * 2 + b2) * 64 + 8 * (r16 & 7);
            *(LAS f32x4*)d = f32x4{acc[0][b2], acc[1][b2], acc[2][b2], acc[3][b2]}; *(LAS f32x4*)(d + 4) = f32x4{acc[4][b2], acc[5][b2], acc[6][b2], acc[7][b2]};
          }
        }
      }
      BAR();
      if (w == 4) {
        const int b = l >> 5, pr = l & 31;
        float s0 = 0.f, s1 = 0.f;
#pragma unroll
        for (int k = 0; k < 8; ++k) { const f32x2 v = *(LAS f32x2*)((LAS float*)(L + L_X5W) + (k * 2 + b) * 64 + 2 * pr); s0 += v[0]; s1 += v[1]; }
        { const v4u q = quad4(pk2(s0, s1)); if ((l & 3) == 0) st16(q, XM + (b * 2560 + 64 * tb + 2 * pr) * 2); }
        TS(29);
      }
    }
    wait_flags(OFF_EFLAGS + l * FSTR); wait_flags(OFF_EFLAGS + (64 + l) * FSTR);
    {
      const int b = w >> 2;
      int off = X4 + (((b * NH + h5) * 64 + 16 * (w & 3) + g) * 512 + 128 * cq + 8 * r16) * 2;
      int offs = X4S + ((b * 64 + 16 * (w & 3) + g) * NH + h5) * 16;
      const int cofs = 8 * r16 + 4 * (l >> 5) + 2 * ((l >> 4) & 1);
      int a_w = L_RED + w * 256 + (((cofs >> 3) ^ w) * 16) + (cofs & 7) * 2;
      int a_s = L_MISC + 1024 + w * 8;
      unsigned tag = TAG();
      KEEP(off); KEEP(offs); KEEP(a_w); KEEP(a_s); KEEP(tag);
      PIN();
      v4u a[4], sv[4];
      for (unsigned sp = 0; sp < SPIN_MAX; ++sp) {
        asm volatile("" ::: "memory");
#pragma unroll
        for (int k = 0; k < 4; ++k) sv[k] = ld16(offs + k * 2048);
        if (__all(((((sv[0].y ^ tag) | (sv[0].w ^ tag)) | ((sv[1].y ^ tag) | (sv[1].w ^ tag))) | (((sv[2].y ^ tag) | (sv[2].w ^ tag)) | ((sv[3].y ^ tag) | (sv[3].w ^ tag)))) == 0u)) break;
        __builtin_amdgcn_s_sleep(S5_SLEEP);
      }
      asm volatile("" ::: "memory");
      TS(19);
#pragma unroll
      for (int k = 0; k < 4; ++k) a[k] = ld16(off + k * 4096);
      TS(20);
      float mw = fmaxf(fmaxf(__uint_as_float(sv[0].x), __uint_as_float(sv[1].x)), fmaxf(__uint_as_float(sv[2].x), __uint_as_float(sv[3].x)));
      mw = x16_max(mw);
      float acc[8] = {0.f, 0.f, 0.f, 0.f, 0.f, 0.f, 0.f, 0.f}; float lw = 0.f;
#pragma unroll
      for (int k = 0; k < 4; ++k) {
        const float e = __builtin_amdgcn_exp2f(__uint_as_float(sv[k].x) - mw);
        const unsigned wlo = pk2(e, 0.f), whi = pk2(0.f, e);
        lw += __uint_as_float(whi) * __uint_as_float(sv[k].z);
#pragma unroll
        for (int d = 0; d < 4; ++d) { acc[2 * d] = dot2(a[k][d], wlo, acc[2 * d]); acc[2 * d + 1] = dot2(a[k][d], whi, acc[2 * d + 1]); }
      }
      const float u0 = rs16(rs32(acc[0], acc[4]), rs32(acc[2], acc[6])), u1 = rs16(rs32(acc[1], acc[5]), rs32(acc[3], acc[7]));
      *(LAS unsigned*)(L + a_w) = pk2(u0, u1);
      lw = x16_sum(lw);
      *(LAS f32x2*)(L + a_s) = f32x2{mw, lw};
    }
    {
      v4u f[8];
      if (w < 4) { tr8(w5, f); KEEP8(f); }
      const int row = r16 & 7;
      int a_a = L_RED + row * 256 + (((4 * w + g) ^ row) * 16);
      int a_st = L_MISC + 1024 + (g & 1) * 32;
      int a_w2 = L_RED2 + ((w * 2 + (g & 1)) * 128 + 8 * r16) * 4;
      int a_r2 = L_RED2 + ((w & 1) * 128 + 2 * l) * 4, o_x5 = X5 + (idx * 2 + (w & 1)) * 256 + l * 4;
      KEEP(a_a); KEEP(a_st); KEEP(a_w2); KEEP(a_r2); KEEP(o_x5);
      BAR();
      TS(21);
      if (w < 4) {
        const v4u a = *(LAS v4u*)(L + a_a);
        const f32x4 s01 = *(LAS f32x4*)(L + a_st), s23 = *(LAS f32x4*)(L + a_st + 16);
        f32x4 d[8];
#pragma unroll
        for (int i = 0; i < 8; ++i) d[i] = mfma16(a, f[i], f32x4{0.f, 0.f, 0.f, 0.f});
        const float mb = fmaxf(fmaxf(s01[0], s01[2]), fmaxf(s23[0], s23[2]));
        float f0 = __builtin_amdgcn_exp2f(s01[0] - mb), f1 = __builtin_amdgcn_exp2f(s01[2] - mb), f2 = __builtin_amdgcn_exp2f(s23[0] - mb), f3 = __builtin_amdgcn_exp2f(s23[2] - mb);
        const float inv = __builtin_amdgcn_rcpf((f0 * s01[1] + f1 * s01[3]) + (f2 * s23[1] + f3 * s23[3]));
        f0 *= inv; f1 *= inv; f2 *= inv; f3 *= inv;
        float v[8];
#pragma unroll
        for (int i = 0; i < 8; ++i) v[i] = (d[i][0] * f0 + d[i][1] * f1) + (d[i][2] * f2 + d[i][3] * f3);
        if (g < 2) { *(LAS f32x4*)(L + a_w2) = f32x4{v[0], v[1], v[2], v[3]}; *(LAS f32x4*)(L + a_w2 + 16) = f32x4{v[4], v[5], v[6], v[7]}; }
      }
      BAR();
      TS(22);
      if (w < 2) {
        f32x2 v[4];
#pragma unroll
        for (int k = 0; k < 4; ++k) v[k] = *(LAS f32x2*)(L + a_r2 + k * 1024);
        { const v4u q = quad4(pk2((v[0][0] + v[1][0]) + (v[2][0] + v[3][0]), (v[0][1] + v[1][1]) + (v[2][1] + v[3][1]))); if ((l & 3) == 0) st16(q, o_x5); }
        TS(23);
      }
    }
    if (idx < 80) {
      const int pp = idx / 10, n = idx % 10;
      if (w < 4) {
        v4u f0[8], f1[8]; tr8(w6[0], f0); tr8(w6[1], f1);
        int off = X5 + ((4 * pp + (w >> 1)) * 8 + (l >> 3)) * 256 + 128 * (w & 1) + (l & 7) * 16;
        int a_gw = L_X5W + w * 2048 + (l >> 3) * 128 + (l & 7) * 16, a_gr = L_X5W + w * 2048 + (r16 & 7) * 128 + (r16 >> 3) * 64 + g * 16;
        int a_w = L_RED + (((w * 2 + (g >> 1)) * 2 + (g & 1)) * 64 + 8 * (r16 & 7)) * 4;
        KEEP8(f0); KEEP8(f1); KEEP(off); KEEP(a_w); KEEP(a_gw); KEEP(a_gr);
        PIN();
        v4u u0, u1;
        {
          bool p0 = true, p1 = true;
          for (unsigned sp = 0; sp < SPIN_MAX; ++sp) {
            asm volatile("" ::: "memory");
            if (p0) u0 = ld16(off);
            if (p1) u1 = ld16(off + 2 * 2048);
            if (p0) p0 = !__all(umax4(u0) != SENT);
            if (p1) p1 = !__all(umax4(u1) != SENT);
            if (!p0 && !p1) break;
          }
        }
        *(LAS v4u*)(L + a_gw) = u0; *(LAS v4u*)(L + a_gw + 1024) = u1;
        const v4u a0 = *(LAS v4u*)(L + a_gr), a1 = *(LAS v4u*)(L + a_gr + 1024);
        TS(24);
        f32x4 d[8];
#pragma unroll
        for (int i = 0; i < 8; ++i) d[i] = mfma16(a0, f0[i], f32x4{0.f, 0.f, 0.f, 0.f});
#pragma unroll
        for (int i = 0; i < 8; ++i) d[i] = mfma16(a1, f1[i], d[i]);
        float s[8];
#pragma unroll
        for (int i = 0; i < 8; ++i) s[i] = rs16(d[i][0] + d[i][2], d[i][1] + d[i][3]);
        if ((g >> 1) == (r16 >> 3)) {
          *(LAS f32x4*)(L + a_w) = f32x4{s[0], s[1], s[2], s[3]}; *(LAS f32x4*)(L + a_w + 16) = f32x4{s[4], s[5], s[6], s[7]};
        }
      }
      int a_r = L_RED + ((l >> 5) * 64 + 2 * (l & 31)) * 4, o_x6 = X6 + ((pp * 2 + (l >> 5)) * DM + 64 * n + 2 * (l & 31)) * 2;
      KEEP(a_r); KEEP(o_x6);
      BAR();
      TS(25);
      if (w == 0) {
        f32x2 v[8];
#pragma unroll
        for (int k = 0; k < 8; ++k) v[k] = *(LAS f32x2*)(L + a_r + k * 512);
        const float s0 = ((v[0][0] + v[1][0]) + (v[2][0] + v[3][0])) + ((v[4][0] + v[5][0]) + (v[6][0] + v[7][0]));
        const float s1 = ((v[0][1] + v[1][1]) + (v[2][1] + v[3][1])) + ((v[4][1] + v[5][1]) + (v[6][1] + v[7][1]));
        { const v4u q = quad4(pk2(s0, s1)); if ((l & 3) == 0) st16(q, o_x6); }
        TS(29);
      }
    }
  }
done: ;
}

extern "C" void kernel_launch(void* const* d_in, const int* in_sizes, int n_in, void* d_out, int out_size, void* d_ws, size_t ws_size, hipStream_t stream) {
  Params p;
  p.x = (const u16*)d_in[0]; p.ckv = (const u16*)d_in[1]; p.wq = (const u16*)d_in[2]; p.wuk = (const u16*)d_in[3]; p.wuv = (const u16*)d_in[4]; p.wo = (const u16*)d_in[5];
  p.wg = (const u16*)d_in[7]; p.wu = (const u16*)d_in[8]; p.wd = (const u16*)d_in[9]; p.c = (const u16*)d_in[10]; p.wmod = (const u16*)d_in[11];
  p.out = (u16*)d_out;
  p.ws = (unsigned char*)d_ws;
  mega<<<256, 512, 0, stream>>>(p);
}
```

```cpp
#include <hip/hip_runtime.h>
#include <stdint.h>
#define E_DELAY 50
#define E_DEPTH 17
#define L_DELAY 20
#define S5_SLEEP 0
#define Q_SLEEP 0
#define CODE_PF 6

#define GAS __attribute__((address_space(1)))
#define LAS __attribute__((address_space(3)))
typedef unsigned v4u __attribute__((ext_vector_type(4)));
typedef unsigned v2u __attribute__((ext_vector_type(2)));
typedef float f32x4 __attribute__((ext_vector_type(4)));
typedef float f32x2 __attribute__((ext_vector_type(2)));
typedef __bf16 bf16x8 __attribute__((ext_vector_type(8)));
typedef __bf16 bf16x2 __attribute__((ext_vector_type(2)));
typedef short s16x4 __attribute__((ext_vector_type(4)));
typedef unsigned short u16;
typedef unsigned long long u64;

constexpr int NB = 2, NH = 32, SK = 8192, DH = 128, DM = 640, DC = 512, DF = 1728;
constexpr int OFF_EPOCH = 0;
constexpr int FSTR = 32;
constexpr int OFF_FLAGS = 4096;
constexpr int OFF_EFLAGS = OFF_FLAGS + 256 * FSTR;
constexpr int OFF_L2FLAGS = OFF_FLAGS + 128 * FSTR;
constexpr int OFF_X1 = OFF_EFLAGS + 128 * FSTR;
constexpr int OFF_R = 81920;
constexpr int X2 = OFF_R;
constexpr int X3 = X2 + 131072;
constexpr int X4 = X3 + 65536;
constexpr int X4S = X4 + 4194304;
constexpr int X5 = X4S + 65536;
constexpr int X6 = X5 + 65536;
constexpr int X7 = X6 + 20480;
constexpr int XM = X7 + 138240;
constexpr int R_END = XM + 10240;
static_assert(OFF_X1 + 20480 <= OFF_R, "map");
constexpr int WS_USED = R_END;
#define F_DELAY 30
#define T3_DELAY 0
#define B_ROWS 63
#define P_SLEEP 0
#define LATE_SLEEP 0
#define T1_EARLY 12
#define CODE_LAST 56
#define WARM_POL " nt"
#define T1_CUS 40
#define A_DELAY 30
constexpr unsigned SENT = 0xFFFFFFFFu;
constexpr int AUX_SC1 = 16;
constexpr unsigned SPIN_MAX = 1u << 18;
constexpr int L_CKV = 0;
constexpr int L_QIMG = 131072;
constexpr int LDS_BYTES = 163840;
constexpr int L_T1RED = L_QIMG, L_T1MISC = L_QIMG + 8192;
constexpr int L_MX = 0;
constexpr int L_PIMG = 4096;
constexpr int L_RED = 16384;
constexpr int L_RED2 = 32768;
constexpr int L_MISC = 49152;
constexpr int L_UKT = 65536;
constexpr int L_X2W = 81920;
constexpr int L_X1W = 90112;
constexpr int L_X5W = 98304;

#define TS(k) do { } while (0)

struct Params {
  const u16 *x, *ckv, *wq, *wuk, *wuv, *wo, *wg, *wu, *wd, *c, *wmod;
  u16* out;
  unsigned char* ws;
};

#define BAR() do { asm volatile("s_waitcnt lgkmcnt(0)" ::: "memory"); __builtin_amdgcn_s_barrier(); asm volatile("" ::: "memory"); } while (0)
#define DRAIN() asm volatile("s_waitcnt vmcnt(0)" ::: "memory")
#define PIN() __builtin_amdgcn_sched_barrier(0)
#define KEEP(x) asm volatile("" : "+v"(x))
#define KEEP4(a) do { KEEP(a[0]); KEEP(a[1]); KEEP(a[2]); KEEP(a[3]); } while (0)
#define KEEP8(a) do { KEEP(a[0]); KEEP(a[1]); KEEP(a[2]); KEEP(a[3]); KEEP(a[4]); KEEP(a[5]); KEEP(a[6]); KEEP(a[7]); } while (0)

__device__ __forceinline__ float bflo(unsigned d) { return __uint_as_float(d << 16); }
__device__ __forceinline__ float bfhi(unsigned d) { return __uint_as_float(d & 0xffff0000u); }
__device__ __forceinline__ unsigned pk2(float lo, float hi) { f32x2 f = {lo, hi}; return __builtin_bit_cast(unsigned, __builtin_convertvector(f, bf16x2)); }
__device__ __forceinline__ f32x4 mfma16(v4u a, v4u b, f32x4 c) { return __builtin_amdgcn_mfma_f32_16x16x32_bf16(__builtin_bit_cast(bf16x8, a), __builtin_bit_cast(bf16x8, b), c, 0, 0, 0); }
__device__ __forceinline__ float dot2(unsigned a, unsigned b, float c) { return __builtin_amdgcn_fdot2_f32_bf16(__builtin_bit_cast(bf16x2, a), __builtin_bit_cast(bf16x2, b), c, false); }
__device__ __forceinline__ v4u ldnt(const u16* p) { return __builtin_nontemporal_load((const GAS v4u*)p); }
__device__ __forceinline__ v4u ld78(const u16* p) { return __builtin_nontemporal_load((const GAS v4u*)p); }
__device__ __forceinline__ unsigned umax3(unsigned a, unsigned b, unsigned c) { unsigned t = a > b ? a : b; return t > c ? t : c; }
__device__ __forceinline__ unsigned umax4(v4u v) { return umax3(umax3(v.x, v.y, v.z), v.w, 0u); }
__device__ __forceinline__ unsigned umax4a(v4u v, unsigned m) { return umax3(umax3(v.x, v.y, v.z), v.w, m); }
__device__ __forceinline__ float x16_max(float x) {
  auto s = __builtin_amdgcn_permlane16_swap(__float_as_uint(x), __float_as_uint(x), false, false);
  x = fmaxf(__uint_as_float(s[0]), __uint_as_float(s[1]));
  auto t = __builtin_amdgcn_permlane32_swap(__float_as_uint(x), __float_as_uint(x), false, false);
  return fmaxf(__uint_as_float(t[0]), __uint_as_float(t[1]));
}
__device__ __forceinline__ float x16_sum(float x) {
  auto s = __builtin_amdgcn_permlane16_swap(__float_as_uint(x), __float_as_uint(x), false, false);
  x = __uint_as_float(s[0]) + __uint_as_float(s[1]);
  auto t = __builtin_amdgcn_permlane32_swap(__float_as_uint(x), __float_as_uint(x), false, false);
  return __uint_as_float(t[0]) + __uint_as_float(t[1]);
}
__device__ __forceinline__ float rs32(float a, float b) { auto t = __builtin_amdgcn_permlane32_swap(__float_as_uint(a), __float_as_uint(b), false, false); return __uint_as_float(t[0]) + __uint_as_float(t[1]); }
__device__ __forceinline__ float rs16(float a, float b) { auto t = __builtin_amdgcn_permlane16_swap(__float_as_uint(a), __float_as_uint(b), false, false); return __uint_as_float(t[0]) + __uint_as_float(t[1]); }
template <int CTRL> __device__ __forceinline__ float dppf(float x) { return __builtin_bit_cast(float, __builtin_amdgcn_mov_dpp(__builtin_bit_cast(int, x), CTRL, 0xf, 0xf, true)); }
__device__ __forceinline__ float row16_sum(float x) { x += dppf<0xB1>(x); x += dppf<0x4E>(x); x += dppf<0x141>(x); x += dppf<0x128>(x); return x; }
__device__ __forceinline__ float wave_sum(float x) { return x16_sum(row16_sum(x)); }
__device__ __forceinline__ float silu(float v) { return v * __builtin_amdgcn_rcpf(1.f + __builtin_amdgcn_exp2f(v * -1.4426950408889634f)); }

__device__ __forceinline__ void tr8(const v4u (&w)[8], v4u (&f)[8]) {
#pragma unroll
  for (int d = 0; d < 4; ++d) {
#pragma unroll
    for (int i2 = 0; i2 < 4; ++i2) {
      f[2 * i2][d] = __builtin_amdgcn_perm(w[2 * d + 1][i2], w[2 * d][i2], 0x05040100u);
      f[2 * i2 + 1][d] = __builtin_amdgcn_perm(w[2 * d + 1][i2], w[2 * d][i2], 0x07060302u);
    }
  }
}
__device__ __forceinline__ v4u silu8(v4u c) {
  v4u r;
#pragma unroll
  for (int d = 0; d < 4; ++d) r[d] = pk2(silu(bflo(c[d])), silu(bfhi(c[d])));
  return r;
}
template <int CTRL> __device__ __forceinline__ unsigned dppu(unsigned x) { return (unsigned)__builtin_amdgcn_mov_dpp((int)x, CTRL, 0xf, 0xf, true); }
__device__ __forceinline__ v4u quad4(unsigned d) { return v4u{dppu<0x00>(d), dppu<0x55>(d), dppu<0xAA>(d), dppu<0xFF>(d)}; }
__device__ __forceinline__ v4u pair2(unsigned a, unsigned b) { return v4u{dppu<0xA0>(a), dppu<0xA0>(b), dppu<0xF5>(a), dppu<0xF5>(b)}; }
template <bool V> struct BoolC { static constexpr bool value = V; };
__device__ __forceinline__ float sum5(float a, float b, float c, float d, float e) { return ((a + b) + (c + d)) + e; }

__global__ void __launch_bounds__(512, 2) mega(Params p) {
  __shared__ __attribute__((aligned(16))) unsigned char smem[LDS_BYTES];
  LAS unsigned char* L = (LAS unsigned char*)smem;
  const int tid = threadIdx.x, l = tid & 63, w = __builtin_amdgcn_readfirstlane(tid >> 6);
  const int bid = blockIdx.x;
  const int g = l >> 4, r16 = l & 15;
  TS(0);
  if (w == CODE_PF) {
    u64 pc; asm volatile("s_getpc_b64 %0" : "=s"(pc));
    const GAS unsigned char* cp = (const GAS unsigned char*)((pc & ~127ull) + (u64)l * 128);
    const unsigned dst = (unsigned)(L_QIMG + 16384) + (unsigned)(size_t)L;
#pragma unroll
    for (int k = 0; k < 4; ++k) {
      const GAS unsigned char* q = cp + (k < 3 || l < CODE_LAST ? k * 8192 : 0);
      unsigned keep;
      asm volatile("s_mov_b32 %0, m0\n\ts_mov_b32 m0, %2\n\ts_nop 0\n\tglobal_load_lds_dword %1, off\n\ts_mov_b32 m0, %0" : "=&s"(keep) : "v"(q), "s"(dst) : "memory");
    }
  }
  {
    const bool e_ = bid < 128;
    const u64 a0 = (u64)(e_ ? p.wmod : p.wq), a1 = (u64)(e_ ? p.c : p.wuk), a2 = (u64)(e_ ? p.x : p.wuv), a3 = e_ ? (u64)p.ckv + (u64)(bid & 127) * (128 * DC * 2) : (u64)p.wo, a4 = (u64)p.ws;
    const int grp = l >> 2;
    unsigned qlo = (unsigned)a4, qhi = (unsigned)(a4 >> 32);
    qlo = grp == 3 ? (unsigned)a3 : qlo; qhi = grp == 3 ? (unsigned)(a3 >> 32) : qhi;
    qlo = grp == 2 ? (unsigned)a2 : qlo; qhi = grp == 2 ? (unsigned)(a2 >> 32) : qhi;
    qlo = grp == 1 ? (unsigned)a1 : qlo; qhi = grp == 1 ? (unsigned)(a1 >> 32) : qhi;
    qlo = grp == 0 ? (unsigned)a0 : qlo; qhi = grp == 0 ? (unsigned)(a0 >> 32) : qhi;
    const unsigned mlo = e_ ? 1u : 0u;
    const unsigned single = (grp >= 1 && grp <= 3) ? mlo : 0u;
    const unsigned po = ((l & 3) == 3 || single) ? 0u : (unsigned)(l & 3) * 1900000u;
    const u64 q = (((u64)qhi << 32) | qlo) + po;
    const unsigned dst = (unsigned)(L_QIMG + 16384) + (unsigned)(size_t)L;
    unsigned keep;
    asm volatile("s_mov_b32 %0, m0\n\ts_mov_b32 m0, %2\n\ts_nop 0\n\tglobal_load_lds_dword %1, off\n\ts_mov_b32 m0, %0" : "=&s"(keep) : "v"(q), "s"(dst) : "memory");
  }
  const __amdgpu_buffer_rsrc_t rs = __builtin_amdgcn_make_buffer_rsrc((void*)p.ws, 0, WS_USED, 0x00020000);
  const bool Ltype = bid >= 128;
  const int idx = bid & 127;
  const int ab = idx >> 6, aj = idx & 63;
  unsigned epoch_raw = 0;
  auto LOAD_EPOCH = [&]() { epoch_raw = __builtin_amdgcn_raw_buffer_load_b32(rs, OFF_EPOCH, 0, AUX_SC1); };
  auto TAG = [&]() { unsigned t = epoch_raw; asm volatile("" : "+v"(t)); return t + 1u; };

  auto ld16 = [&](int off) { return __builtin_amdgcn_raw_buffer_load_b128(rs, off, 0, AUX_SC1); };
  auto ld8 = [&](int off) { return __builtin_amdgcn_raw_buffer_load_b64(rs, off, 0, AUX_SC1); };
  auto ld4 = [&](int off) { return __builtin_amdgcn_raw_buffer_load_b32(rs, off, 0, AUX_SC1); };
  auto st16 = [&](v4u v, int off) { __builtin_amdgcn_raw_buffer_store_b128(v, rs, off, 0, AUX_SC1); };
  auto st8 = [&](v2u v, int off) { __builtin_amdgcn_raw_buffer_store_b64(v, rs, off, 0, AUX_SC1); };
  auto st4 = [&](unsigned v, int off) { __builtin_amdgcn_raw_buffer_store_b32(v, rs, off, 0, AUX_SC1); };
  auto dma_ckv = [&](int row0, int nrows, bool paced = false, int rstride = 1) {
    const u16* base = p.ckv + ((size_t)ab * SK + aj * 128) * DC;
    for (int rr = 0; rr < nrows; ++rr) {
      if (paced) asm volatile("s_waitcnt vmcnt(%0)" :: "i"(E_DEPTH - 1) : "memory");
      const int row = row0 + rr * rstride;
      const int sw = ((row & 3) << 2) | ((row >> 2) & 3);
      const u16* src = base + row * DC + ((l ^ sw) * 8);
      unsigned keep; const unsigned dst = (unsigned)(L_CKV + row * 1024) + (unsigned)(size_t)L;
      asm volatile("s_mov_b32 %0, m0\n\ts_mov_b32 m0, %2\n\ts_nop 0\n\tglobal_load_lds_dwordx4 %1, off nt\n\ts_mov_b32 m0, %0" : "=&s"(keep) : "v"(src), "s"(dst) : "memory");
    }
  };
  const v4u SENT4 = {SENT, SENT, SENT, SENT};
  auto wait_flags = [&](int off) {
    const unsigned tag = TAG();
    for (unsigned sp = 0; sp < SPIN_MAX; ++sp) {
      asm volatile("" ::: "memory");
      if (__all(ld4(off) == tag)) break;
      __builtin_amdgcn_s_sleep(2);
    }
  };

  if (!Ltype) {
    const bool t1role = idx < T1_CUS;
    const int t1task = idx < 40 ? idx : (idx - 40 + 3) % 40;
    const int t78 = 2 * (8 * (idx >> 4) + (idx & 7)) + ((idx >> 3) & 1);
    const bool role78 = idx < 64 && t78 < 54;
    auto fills = [&]() {
      if (t1role) __builtin_amdgcn_s_sleep(4);
      if (role78) {
#pragma unroll
        for (int k = 0; k < 3; ++k) { const int u = k * 64 + l; if (u < 160) st16(SENT4, X7 + t78 * 2560 + u * 16); }
      }
      DRAIN();
    };
    auto warm_uk = [&](int k) {
      const int ln = 64 * k + l;
      const u16* q = p.wuk + ((size_t)(128 * (idx & 3) + (ln >> 1)) * NH + (idx >> 2)) * DH + 64 * (ln & 1);
      const unsigned dst = (unsigned)(L_QIMG + 16384) + (unsigned)(size_t)L;
      unsigned keep;
      asm volatile("s_mov_b32 %0, m0\n\ts_mov_b32 m0, %2\n\ts_nop 0\n\tglobal_load_lds_dword %1, off" WARM_POL "\n\ts_mov_b32 m0, %0" : "=&s"(keep) : "v"(q), "s"(dst) : "memory");
    };
    if (t1role) {
      const int mg = t1task % 10, pp1 = t1task / 10;
      if (w < 5) {
        v4u wv[8];
        const int col = (r16 < 8) ? (64 * mg + 8 * r16) : (DM + 64 * mg + 8 * (r16 - 8));
        const int k0 = 160 * pp1 + 32 * w + 8 * g;
        const v4u cv = *(const GAS v4u*)(p.c + (r16 & 1) * DM + k0);
        const u16* src = p.wmod + (size_t)k0 * (6 * DM) + col;
#pragma unroll
        for (int j = 0; j < 8; ++j) wv[j] = ldnt(src + (size_t)j * (6 * DM));
        const unsigned xd = *(const GAS unsigned*)(p.x + (l >> 5) * DM + 64 * mg + 2 * (l & 31));
        LOAD_EPOCH();
        PIN();
        int a_w = L_T1RED + ((w * 2) * 128 + 8 * r16) * 4;
        int a_r = L_T1RED + ((l >> 5) * 128 + 2 * (l & 31)) * 4;
        int o_x1 = OFF_X1 + (((pp1 * 2 + (l >> 5)) * 320) + 32 * mg + (l & 31)) * 8;
        KEEP(a_w); KEEP(a_r); KEEP(o_x1);
        v4u a = silu8(cv);
        KEEP(a);
        PIN();
        v4u f[8]; tr8(wv, f);
        KEEP8(f);
        float xlo = bflo(xd), xhi = bfhi(xd);
        unsigned tag = TAG();
        KEEP(xlo); KEEP(xhi); KEEP(tag);
        TS(1);
        PIN();
        f32x4 acc[8];
#pragma unroll
        for (int i = 0; i < 8; ++i) acc[i] = mfma16(a, f[i], f32x4{0.f, 0.f, 0.f, 0.f});
        if (g == 0) {
#pragma unroll
          for (int b = 0; b < 2; ++b) {
            *(LAS f32x4*)(L + a_w + b * 512) = f32x4{acc[0][b], acc[1][b], acc[2][b], acc[3][b]};
            *(LAS f32x4*)(L + a_w + b * 512 + 16) = f32x4{acc[4][b], acc[5][b], acc[6][b], acc[7][b]};
          }
        }
        TS(2);
        BAR();
        TS(3);
        if (w == 0) {
          f32x2 s[5], c2[5];
#pragma unroll
          for (int ww = 0; ww < 5; ++ww) { s[ww] = *(LAS f32x2*)(L + a_r + ww * 1024); c2[ww] = *(LAS f32x2*)(L + a_r + ww * 1024 + 256); }
          const f32x2 mr = *(LAS f32x2*)(L + L_T1MISC + (l >> 5) * 8);
          const float xn0 = (xlo - mr[0]) * mr[1], xn1 = (xhi - mr[0]) * mr[1];
          const float one = pp1 == 0 ? 1.f : 0.f;
          const float o0 = xn0 * (sum5(c2[0][0], c2[1][0], c2[2][0], c2[3][0], c2[4][0]) + one) + sum5(s[0][0], s[1][0], s[2][0], s[3][0], s[4][0]);
          const float o1 = xn1 * (sum5(c2[0][1], c2[1][1], c2[2][1], c2[3][1], c2[4][1]) + one) + sum5(s[0][1], s[1][1], s[2][1], s[3][1], s[4][1]);
          { const v4u v = pair2(pk2(o0, o1), tag); if ((l & 1) == 0) st16(v, o_x1); }
          TS(4);
        }
        dma_ckv(T1_EARLY * 3 + w, (128 - T1_EARLY * 3 - w + 7) / 8, true, 8);
      } else if (w < 7) {
        const int b = w - 5;
        LOAD_EPOCH();
        float v[10]; float s = 0.f;
#pragma unroll
        for (int k = 0; k < 5; ++k) { const unsigned d = *(const GAS unsigned*)(p.x + b * DM + 2 * (l + 64 * k)); v[2 * k] = bflo(d); v[2 * k + 1] = bfhi(d); s += v[2 * k] + v[2 * k + 1]; }
        const float mean = wave_sum(s) * (1.f / DM);
        float q = 0.f;
#pragma unroll
        for (int k = 0; k < 10; ++k) { const float d = v[k] - mean; q += d * d; }
        const float var = wave_sum(q) * (1.f / DM);
        *(LAS f32x2*)(L + L_T1MISC + b * 8) = f32x2{mean, __builtin_amdgcn_rsqf(var + 1e-5f)};
        __builtin_amdgcn_s_sleep(E_DELAY / 2);
        dma_ckv(b, T1_EARLY, false, 3);
        BAR();
        dma_ckv(T1_EARLY * 3 + w, (128 - T1_EARLY * 3 - w + 7) / 8, true, 8);
      } else {
        __builtin_amdgcn_s_sleep(4);
        LOAD_EPOCH();
        fills();
        if (l == 0) st4(TAG(), OFF_FLAGS + idx * FSTR);
        dma_ckv(2, T1_EARLY, false, 3);
        BAR();
        dma_ckv(T1_EARLY * 3 + w, (128 - T1_EARLY * 3 - w + 7) / 8, true, 8);
      }
    } else if (w < 7) {
      __builtin_amdgcn_s_sleep(E_DELAY);
      LOAD_EPOCH();
      dma_ckv(w, 17, true, 7);
    } else {
      LOAD_EPOCH();
      fills();
      if (l == 0) st4(TAG(), OFF_FLAGS + idx * FSTR);
      dma_ckv(119, 9, true);
    }
    v4u wa0[8], wa1[8], wa2[8], wdn[8];
    {
      const unsigned ef0 = ld4(OFF_EFLAGS + l * FSTR), ef1 = ld4(OFF_EFLAGS + (64 + l) * FSTR);
      int a_qw[2], a_qr[4], a_kr[4];
      const int key = 16 * w + r16;
#pragma unroll
      for (int u = 0; u < 2; ++u) a_qw[u] = L_QIMG + (8 * u + w) * 1024 + ((l ^ (8 * u + w)) * 16);
#pragma unroll
      for (int t = 0; t < 4; ++t) { a_qr[t] = L_QIMG + r16 * 1024 + (((4 * t + g) ^ r16) * 16); a_kr[t] = L_CKV + key * 1024 + (((4 * t + g) ^ (((key & 3) << 2) | ((key >> 2) & 3))) * 16); }
      int a_vr[2];
#pragma unroll
      for (int u = 0; u < 2; ++u) a_vr[u] = L_CKV + (4 * g + (r16 >> 2)) * 1024 + (((8 * w + 4 * u + (l & 3)) ^ (((r16 >> 2) << 2) | g)) * 16);
      int a_mxw = L_MX + r16 * 32 + w * 4, a_mxr = L_MX + r16 * 32;
      int a_pw = L_PIMG + (w >> 1) * 1024 + l * 16 + (w & 1) * 8, a_pr = L_PIMG + l * 16;
      int a_ow[2], a_or[4];
#pragma unroll
      for (int u = 0; u < 2; ++u) a_ow[u] = L_QIMG + r16 * 1024 + (((8 * w + 4 * u + g) ^ r16) * 16);
#pragma unroll
      for (int k = 0; k < 4; ++k) a_or[k] = L_QIMG + (4 * w + k) * 1024 + ((l ^ ((4 * w + k) & 15)) * 16);
      KEEP(a_ow[0]); KEEP(a_ow[1]); KEEP4(a_or);
      int o_st = X4 + ((ab * NH + 4 * w) * 64 + aj) * 1024 + l * 16, o_ss = X4S + ((ab * 64 + aj) * NH + 16 * (w & 1) + r16) * 16;
      int o_q = X3 + ab * 32768 + tid * 16;
      KEEP(a_qw[0]); KEEP(a_qw[1]); KEEP4(a_qr); KEEP4(a_kr); KEEP(a_vr[0]); KEEP(a_vr[1]); KEEP(a_mxw); KEEP(a_mxr); KEEP(a_pw); KEEP(a_pr); KEEP(o_st); KEEP(o_ss); KEEP(o_q);
      DRAIN();
      { const unsigned tag = TAG(); if (!__all((ef0 == tag) & (ef1 == tag))) { wait_flags(OFF_EFLAGS + l * FSTR); wait_flags(OFF_EFLAGS + (64 + l) * FSTR); } }
      BAR();
      TS(11);
      v4u qa[4];
#pragma unroll
      for (int k = 0; k < 4; ++k) qa[k] = ld16(o_q + k * 8192);
      PIN();
      v4u qk[16], pv[16];
#pragma unroll
      for (int s = 0; s < 16; ++s) qk[s] = *(LAS v4u*)(L + a_kr[s & 3] + (s >> 2) * 256);
#pragma unroll
      for (int s = 0; s < 4; ++s)
#pragma unroll
        for (int t = 0; t < 4; ++t) {
          v2u h2[2];
#pragma unroll
          for (int hf = 0; hf < 2; ++hf)
            h2[hf] = __builtin_bit_cast(v2u, __builtin_amdgcn_ds_read_tr16_b64_v4i16((LAS s16x4*)(L + a_vr[t >> 1] + (32 * s + 16 * hf) * 1024 + 8 * (t & 1))));
          pv[s * 4 + t] = v4u{h2[0].x, h2[0].y, h2[1].x, h2[1].y};
        }
#pragma unroll
      for (int s = 0; s < 16; ++s) { KEEP(qk[s]); KEEP(pv[s]); }
      TS(12);
      PIN();
      {
        unsigned pend = 15u;
        for (unsigned sp = 0; sp < SPIN_MAX; ++sp) {
#pragma unroll
          for (int k = 0; k < 4; ++k) if (pend >> k & 1u) { if (__all(umax4(qa[k]) != SENT)) pend &= ~(1u << k); }
          if (!pend) break;
          if (Q_SLEEP) __builtin_amdgcn_s_sleep(Q_SLEEP);
          asm volatile("" ::: "memory");
#pragma unroll
          for (int k = 0; k < 4; ++k) if (pend >> k & 1u) qa[k] = ld16(o_q + k * 8192);
        }
      }
#pragma unroll
      for (int k = 0; k < 4; ++k) *(LAS v4u*)(L + a_qw[k & 1] + (k >> 1) * 16384) = qa[k];
      TS(13);
      BAR();
      TS(14);
      f32x4 sacc[2][2];
#pragma unroll
      for (int ht = 0; ht < 2; ++ht) { sacc[ht][0] = f32x4{0.f, 0.f, 0.f, 0.f}; sacc[ht][1] = f32x4{0.f, 0.f, 0.f, 0.f}; }
      {
        v4u fa[8], fb[8];
#pragma unroll
        for (int s = 0; s < 8; ++s) fa[s] = *(LAS v4u*)(L + a_qr[s & 3] + (s >> 2) * 256);
#pragma unroll
        for (int s = 0; s < 8; ++s) fb[s] = *(LAS v4u*)(L + a_qr[s & 3] + (2 + (s >> 2)) * 256);
        PIN();
#pragma unroll
        for (int s = 0; s < 8; ++s) sacc[0][s & 1] = mfma16(qk[s], fa[s], sacc[0][s & 1]);
        PIN();
#pragma unroll
        for (int s = 0; s < 8; ++s) fa[s] = *(LAS v4u*)(L + a_qr[s & 3] + 16384 + (s >> 2) * 256);
        PIN();
#pragma unroll
        for (int s = 0; s < 8; ++s) sacc[0][s & 1] = mfma16(qk[8 + s], fb[s], sacc[0][s & 1]);
        PIN();
#pragma unroll
        for (int s = 0; s < 8; ++s) fb[s] = *(LAS v4u*)(L + a_qr[s & 3] + 16384 + (2 + (s >> 2)) * 256);
        PIN();
#pragma unroll
        for (int s = 0; s < 8; ++s) sacc[1][s & 1] = mfma16(qk[s], fa[s], sacc[1][s & 1]);
#pragma unroll
        for (int s = 0; s < 8; ++s) sacc[1][s & 1] = mfma16(qk[8 + s], fb[s], sacc[1][s & 1]);
      }
      PIN();
      unsigned lf[2] = {0u, 0u};
      if (w == 6) lf[0] = ld4(OFF_FLAGS + l * FSTR);
      f32x4 sc[2];
#pragma unroll
      for (int ht = 0; ht < 2; ++ht) {
        sc[ht] = sacc[ht][0] + sacc[ht][1];
        *(LAS float*)(L + a_mxw + ht * 512) = x16_max(fmaxf(fmaxf(sc[ht][0], sc[ht][1]), fmaxf(sc[ht][2], sc[ht][3])));
      }
      TS(15);
      BAR();
      TS(16);
      float m[2];
#pragma unroll
      for (int ht = 0; ht < 2; ++ht) {
        const f32x4 m0 = *(LAS f32x4*)(L + a_mxr + ht * 512), m1 = *(LAS f32x4*)(L + a_mxr + ht * 512 + 16);
        m[ht] = fmaxf(fmaxf(fmaxf(m0[0], m0[1]), fmaxf(m0[2], m0[3])), fmaxf(fmaxf(m1[0], m1[1]), fmaxf(m1[2], m1[3])));
        *(LAS v2u*)(L + a_pw + ht * 4096) = v2u{pk2(__builtin_amdgcn_exp2f(sc[ht][0] - m[ht]), __builtin_amdgcn_exp2f(sc[ht][1] - m[ht])), pk2(__builtin_amdgcn_exp2f(sc[ht][2] - m[ht]), __builtin_amdgcn_exp2f(sc[ht][3] - m[ht]))};
      }
      BAR();
      v4u pf[2][4];
#pragma unroll
      for (int ht = 0; ht < 2; ++ht)
#pragma unroll
        for (int s = 0; s < 4; ++s) pf[ht][s] = *(LAS v4u*)(L + a_pr + ht * 4096 + s * 1024);
      PIN();
#pragma unroll
      for (int ht = 0; ht < 2; ++ht) {
        f32x4 o[4];
#pragma unroll
        for (int t = 0; t < 4; ++t) o[t] = f32x4{0.f, 0.f, 0.f, 0.f};
#pragma unroll
        for (int s = 0; s < 4; ++s)
#pragma unroll
          for (int t = 0; t < 4; ++t) o[t] = mfma16(pv[s * 4 + t], pf[ht][s], o[t]);
        *(LAS v4u*)(L + a_ow[0] + ht * 16384) = v4u{pk2(o[0][0], o[0][1]), pk2(o[0][2], o[0][3]), pk2(o[1][0], o[1][1]), pk2(o[1][2], o[1][3])};
        *(LAS v4u*)(L + a_ow[1] + ht * 16384) = v4u{pk2(o[2][0], o[2][1]), pk2(o[2][2], o[2][3]), pk2(o[3][0], o[3][1]), pk2(o[3][2], o[3][3])};
      }
      float lsum = 0.f;
      if (w < 2) {
        const v4u ones = {0x3F803F80u, 0x3F803F80u, 0x3F803F80u, 0x3F803F80u};
        f32x4 lacc = {0.f, 0.f, 0.f, 0.f};
        if (w == 0) {
#pragma unroll
          for (int s = 0; s < 4; ++s) lacc = mfma16(ones, pf[0][s], lacc);
        } else {
#pragma unroll
          for (int s = 0; s < 4; ++s) lacc = mfma16(ones, pf[1][s], lacc);
        }
        lsum = lacc[0];
      }
      BAR();
#pragma unroll
      for (int k = 0; k < 4; ++k) st16(*(LAS v4u*)(L + a_or[k]), o_st + k * 65536);
      TS(18);
      DRAIN();
      BAR();
      if (w < 2 && g == 0) { const unsigned tag = TAG(); st16(v4u{__float_as_uint(w == 0 ? m[0] : m[1]), tag, __float_as_uint(lsum), tag}, o_ss); }
      PIN();
      if (role78) {
        if (w >= 4) {
          const u16* wsrc78 = (((l & 7) < 4) ? p.wg : p.wu) + 32 * t78 + 8 * (l & 3) + (size_t)(64 * (w & 3) + 32 * ((l >> 3) & 1) + 8 * g) * DF;
#pragma unroll
          for (int j = 0; j < 8; ++j) wa0[j] = ld78(wsrc78 + (size_t)j * DF);
#pragma unroll
          for (int j = 0; j < 8; ++j) wa1[j] = ld78(wsrc78 + (size_t)(256 + j) * DF);
          if (w < 6) {
#pragma unroll
            for (int j = 0; j < 8; ++j) wa2[j] = ld78(wsrc78 + (size_t)(512 + j) * DF);
          }
        }
        if (w >= 3) {
          const u16* src = p.wd + (size_t)(32 * t78 + 8 * g) * DM + 128 * (w - 3) + 8 * r16;
#pragma unroll
          for (int j = 0; j < 8; ++j) wdn[j] = ldnt(src + (size_t)j * DM);
        }
        PIN();
      }
      TS(17);
      if (w == 6) {
        const unsigned tag = TAG();
        if (!__all(lf[0] == tag)) wait_flags(OFF_FLAGS + l * FSTR);
      }
      BAR();
      TS(19);
    }
    if (role78) {
      v4u fa[3][8], fd[8];
      if (w >= 4) { tr8(wa0, fa[0]); tr8(wa1, fa[1]); if (w < 6) tr8(wa2, fa[2]); KEEP8(fa[0]); KEEP8(fa[1]); if (w < 6) KEEP8(fa[2]); }
      if (w >= 3) { tr8(wdn, fd); KEEP8(fd); }
      const int b = w / 3, cc = 32 * (w % 3) + (l & 31), half = l >> 5;
      const bool act_lane = (w < 6) && (cc < 80);
      float x1[4] = {0.f, 0.f, 0.f, 0.f}, cb4[4], sb4[4];
      int a_h2w = L_RED + (b * DM + 8 * cc + 4 * half) * 2;
      if (w < 6) {
        const int ccc = cc < 80 ? cc : 79;
        const int mo = 8 * ccc + 4 * half;
        v2u gav, sbv, cbv;
        for (unsigned sp = 0; sp < SPIN_MAX; ++sp) {
          asm volatile("" ::: "memory");
          gav = ld8(XM + (b * 2560 + mo) * 2); sbv = ld8(XM + (b * 2560 + 640 + mo) * 2); cbv = ld8(XM + (b * 2560 + 1280 + mo) * 2);
          if (__all(umax3(umax3(gav.x, gav.y, sbv.x), umax3(sbv.y, cbv.x, cbv.y), 0u) != SENT)) break;
          __builtin_amdgcn_s_sleep(8);
        }
        const v2u xv = *(const GAS v2u*)(p.x + b * DM + mo);
        float xf[4] = {bflo(xv.x), bfhi(xv.x), bflo(xv.y), bfhi(xv.y)}, ga4[4] = {bflo(gav.x), bfhi(gav.x), bflo(gav.y), bfhi(gav.y)};
        cb4[0] = 1.f + bflo(cbv.x); cb4[1] = 1.f + bfhi(cbv.x); cb4[2] = 1.f + bflo(cbv.y); cb4[3] = 1.f + bfhi(cbv.y);
        sb4[0] = bflo(sbv.x); sb4[1] = bfhi(sbv.x); sb4[2] = bflo(sbv.y); sb4[3] = bfhi(sbv.y);
        if (!act_lane) { ga4[0] = ga4[1] = ga4[2] = ga4[3] = 0.f; xf[0] = xf[1] = xf[2] = xf[3] = 0.f; }
        int off = X6 + ((4 * half * 2 + b) * DM + 8 * ccc) * 2;
        int a_st = L_MISC + w * 8;
        unsigned one_lo = 0x00003F80u, one_hi = 0x3F800000u;
        KEEP(one_lo); KEEP(one_hi);
        KEEP4(xf); KEEP4(ga4); KEEP4(cb4); KEEP4(sb4); KEEP(off); KEEP(a_st); KEEP(a_h2w);
        PIN();
        v4u a[4];
        {
          unsigned pend = 15u;
          for (unsigned sp = 0; sp < SPIN_MAX; ++sp) {
            asm volatile("" ::: "memory");
#pragma unroll
            for (int k = 0; k < 4; ++k) if (pend >> k & 1u) a[k] = ld16(off + k * 2560);
#pragma unroll
            for (int k = 0; k < 4; ++k) if (pend >> k & 1u) { if (__all(umax4(a[k]) != SENT)) pend &= ~(1u << k); }
            if (!pend) break;
          }
        }
        TS(24);
        float acc[8];
#pragma unroll
        for (int d = 0; d < 4; ++d) {
          acc[2 * d] = dot2(a[3][d], one_lo, dot2(a[2][d], one_lo, dot2(a[1][d], one_lo, bflo(a[0][d]))));
          acc[2 * d + 1] = dot2(a[3][d], one_hi, dot2(a[2][d], one_hi, dot2(a[1][d], one_hi, bfhi(a[0][d]))));
        }
#pragma unroll
        for (int e = 0; e < 4; ++e) x1[e] = xf[e] + ga4[e] * rs32(acc[e], acc[e + 4]);
        const float s1 = wave_sum((x1[0] + x1[1]) + (x1[2] + x1[3]));
        const float s2 = wave_sum((x1[0] * x1[0] + x1[1] * x1[1]) + (x1[2] * x1[2] + x1[3] * x1[3]));
        *(LAS f32x2*)(L + a_st) = f32x2{s1, s2};
      }
      int a_st2 = L_MISC + b * 24;
      int a_ar = L_RED + ((r16 & 1) * DM + 64 * (w & 3) + 32 * (r16 >> 3) + 8 * g) * 2;
      int a_rw = L_RED2 + ((((w & 3) * 2 + (g >> 1)) * 2) * 64 + 8 * (r16 & 7)) * 4;
      KEEP(a_st2); KEEP(a_ar); KEEP(a_rw);
      BAR();
      TS(25);
      if (act_lane) {
        const f32x2 t0 = *(LAS f32x2*)(L + a_st2), t1 = *(LAS f32x2*)(L + a_st2 + 8), t2 = *(LAS f32x2*)(L + a_st2 + 16);
        const float mean = (t0[0] + t1[0] + t2[0]) * (1.f / DM);
        const float var = fmaxf((t0[1] + t1[1] + t2[1]) * (1.f / DM) - mean * mean, 0.f);
        const float rstd = __builtin_amdgcn_rsqf(var + 1e-5f);
        float hv[4];
#pragma unroll
        for (int e = 0; e < 4; ++e) hv[e] = (x1[e] - mean) * rstd * cb4[e] + sb4[e];
        *(LAS v2u*)(L + a_h2w) = v2u{pk2(hv[0], hv[1]), pk2(hv[2], hv[3])};
      }
      BAR();
      TS(26);
      if (w >= 4) {
        const v4u a0 = *(LAS v4u*)(L + a_ar), a1 = *(LAS v4u*)(L + a_ar + 512);
        v4u a2 = a0;
        if (w < 6) a2 = *(LAS v4u*)(L + a_ar + 1024);
        f32x4 acc[8];
#pragma unroll
        for (int i = 0; i < 8; ++i) acc[i] = mfma16(a0, fa[0][i], f32x4{0.f, 0.f, 0.f, 0.f});
#pragma unroll
        for (int i = 0; i < 8; ++i) acc[i] = mfma16(a1, fa[1][i], acc[i]);
        if (w < 6) {
#pragma unroll
          for (int i = 0; i < 8; ++i) acc[i] = mfma16(a2, fa[2][i], acc[i]);
        }
        if ((g == 0 && r16 < 8) || (g == 2 && r16 >= 8)) {
#pragma unroll
          for (int b2 = 0; b2 < 2; ++b2) {
            *(LAS f32x4*)(L + a_rw + b2 * 256) = f32x4{acc[0][b2], acc[1][b2], acc[2][b2], acc[3][b2]};
            *(LAS f32x4*)(L + a_rw + b2 * 256 + 16) = f32x4{acc[4][b2], acc[5][b2], acc[6][b2], acc[7][b2]};
          }
        }
      }
      int a_gr = L_RED2 + ((l >> 5) * 64 + (l & 31)) * 4, a_aw = L_MISC + 512 + l * 2, a_acr = L_MISC + 512 + ((r16 & 1) * 32 + 8 * g) * 2;
      int o_x7 = X7 + (t78 * 2 * DM + 128 * (w - 3) + 8 * r16) * 2;
      KEEP(a_gr); KEEP(a_aw); KEEP(a_acr); KEEP(o_x7);
      BAR();
      TS(27);
      if (w == 0) {
        float gv[8], uv[8];
#pragma unroll
        for (int k = 0; k < 8; ++k) { gv[k] = *(LAS float*)(L + a_gr + k * 512); uv[k] = *(LAS float*)(L + a_gr + k * 512 + 128); }
        const float gs = ((gv[0] + gv[1]) + (gv[2] + gv[3])) + ((gv[4] + gv[5]) + (gv[6] + gv[7]));
        const float us = ((uv[0] + uv[1]) + (uv[2] + uv[3])) + ((uv[4] + uv[5]) + (uv[6] + uv[7]));
        *(LAS u16*)(L + a_aw) = (u16)pk2(silu(gs) * us, 0.f);
      }
      BAR();
      TS(28);
      if (w >= 3) {
        const v4u a = *(LAS v4u*)(L + a_acr);
        f32x4 d[8];
#pragma unroll
        for (int i = 0; i < 8; ++i) d[i] = mfma16(a, fd[i], f32x4{0.f, 0.f, 0.f, 0.f});
        if (g == 0) {
#pragma unroll
          for (int b2 = 0; b2 < 2; ++b2)
            st16(v4u{pk2(d[0][b2], d[1][b2]), pk2(d[2][b2], d[3][b2]), pk2(d[4][b2], d[5][b2]), pk2(d[6][b2], d[7][b2])}, o_x7 + b2 * DM * 2);
        }
        TS(29);
      }
    } else if (idx >= 94 && idx < 114) {
      const int n = idx - 94;
      float base0 = 0.f, base1 = 0.f, gb0 = 0.f, gb1 = 0.f;
      if (w == 7 && l < 32) {
        const int b = l >> 4, pr = l & 15, m = 32 * n + 2 * pr;
        unsigned ga = 0, gb = 0, ap[8];
        for (unsigned sp = 0; sp < SPIN_MAX; ++sp) {
          asm volatile("" ::: "memory");
          ga = ld4(XM + (b * 2560 + m) * 2); gb = ld4(XM + (b * 2560 + 1920 + m) * 2);
#pragma unroll
          for (int k = 0; k < 8; ++k) ap[k] = ld4(X6 + ((k * 2 + b) * DM + m) * 2);
          if (__all(umax3(umax3(umax3(ap[0], ap[1], ap[2]), umax3(ap[3], ap[4], ap[5]), umax3(ap[6], ap[7], ga)), gb, 0u) != SENT)) break;
          __builtin_amdgcn_s_sleep(8);
        }
        const unsigned xd = *(const GAS unsigned*)(p.x + b * DM + m);
        float s0 = 0.f, s1 = 0.f;
#pragma unroll
        for (int k = 0; k < 8; ++k) { s0 += bflo(ap[k]); s1 += bfhi(ap[k]); }
        base0 = bflo(xd) + bflo(ga) * s0; base1 = bfhi(xd) + bfhi(ga) * s1; gb0 = bflo(gb); gb1 = bfhi(gb);
        KEEP(base0); KEEP(base1); KEEP(gb0); KEEP(gb1);
      }
      int a_fr = L_RED + ((l >> 4) * 32 + 2 * (l & 15)) * 4;
      KEEP(a_fr);
      if (w < 7) {
        const int task = 8 * w + (l >> 3), b = (l >> 2) & 1, ch = l & 3;
        const bool valid = task < 54;
        int off = X7 + (((valid ? task : 0) * 2 + b) * DM + 32 * n + 8 * ch) * 2;
        int a_w = L_RED + (w * 64 + b * 32 + 8 * ch + 4 * (l >> 5) + 2 * ((l >> 4) & 1)) * 4;
        unsigned msk = valid ? 0xFFFFFFFFu : 0u;
        KEEP(off); KEEP(a_w); KEEP(msk);
        PIN();
        v4u a;
        for (unsigned sp = 0; sp < SPIN_MAX; ++sp) {
          asm volatile("" ::: "memory");
          a = ld16(off);
          if (__all(umax4(a) != SENT)) break;
          if (LATE_SLEEP) __builtin_amdgcn_s_sleep(LATE_SLEEP);
        }
        TS(29);
        float v[8];
#pragma unroll
        for (int d = 0; d < 4; ++d) { const unsigned x = a[d] & msk; v[2 * d] = bflo(x); v[2 * d + 1] = bfhi(x); }
        float t0 = rs16(rs32(v[0], v[4]), rs32(v[2], v[6])), t1 = rs16(rs32(v[1], v[5]), rs32(v[3], v[7]));
        t0 += dppf<0x128>(t0); t1 += dppf<0x128>(t1);
        *(LAS f32x2*)(L + a_w) = f32x2{t0, t1};
      }
      BAR();
      if (w == 7 && l < 32) {
        f32x2 v[7];
#pragma unroll
        for (int k = 0; k < 7; ++k) v[k] = *(LAS f32x2*)(L + a_fr + k * 256);
        const float s0 = ((v[0][0] + v[1][0]) + (v[2][0] + v[3][0])) + ((v[4][0] + v[5][0]) + v[6][0]);
        const float s1 = ((v[0][1] + v[1][1]) + (v[2][1] + v[3][1])) + ((v[4][1] + v[5][1]) + v[6][1]);
        *(GAS unsigned*)(p.out + (l >> 4) * DM + 32 * n + 2 * (l & 15)) = pk2(base0 + gb0 * s0, base1 + gb1 * s1);
        if (n == 0 && l == 0) st4(TAG(), OFF_EPOCH);
      }
    }
  } else {
    const int hh = idx >> 2, sub = idx & 3;
    if (w < 5) {
      v4u wv2[8], uk[8];
      const u16* src2 = p.wq + (size_t)(160 * sub + 32 * w + 8 * g) * (NH * DH) + hh * DH + 8 * r16;
      auto load_t3 = [&]() {
        const u16* src = p.wuk + ((size_t)(128 * sub + 32 * (w & 3) + g) * NH + hh) * DH + 8 * r16;
#pragma unroll
        for (int t = 0; t < 2; ++t)
#pragma unroll
          for (int i = 0; i < 4; ++i) uk[4 * t + i] = ldnt(src + (size_t)(16 * t + 4 * i) * NH * DH);
      };
      auto load_t23 = [&]() {
#pragma unroll
        for (int j = 0; j < 8; ++j) wv2[j] = ldnt(src2 + (size_t)j * (NH * DH));

      };
      __builtin_amdgcn_s_sleep(L_DELAY);
      load_t23();
      LOAD_EPOCH();
      PIN();
      {
        unsigned tag = TAG();
        v4u f[8]; tr8(wv2, f);
        KEEP8(f);

        TS(2);
        int off = OFF_X1 + ((l >> 3) * 320 + (160 * sub + 32 * w) / 2 + 2 * (l & 7)) * 8;
        int a_gw = L_X1W + w * 512 + (l >> 3) * 64 + (l & 7) * 8, a_gr = L_X1W + w * 512 + (r16 & 7) * 64 + g * 16;
        int a_w = L_RED2 + (((w * 2 + (g & 1)) * 2) * 128 + 8 * r16) * 4;
        int a_r = L_RED2 + ((w & 1) * 128 + 2 * l) * 4, o_x2 = X2 + ((hh * 4 + sub) * 2 + (w & 1)) * 512 + l * 8;
        KEEP8(f); KEEP(off); KEEP(a_w); KEEP(tag); KEEP(a_r); KEEP(o_x2); KEEP(a_gw); KEEP(a_gr);
        TS(5);
        PIN();
        v4u u0;
        for (unsigned sp = 0; sp < SPIN_MAX; ++sp) {
          asm volatile("" ::: "memory");
          u0 = ld16(off);
          if (__all(((u0.y ^ tag) | (u0.w ^ tag)) == 0u)) break;
          if (P_SLEEP) __builtin_amdgcn_s_sleep(P_SLEEP);
        }
        *(LAS v2u*)(L + a_gw) = v2u{u0.x, u0.z};
        const v4u a = *(LAS v4u*)(L + a_gr);
        TS(6);
        f32x4 acc[8];
#pragma unroll
        for (int i = 0; i < 8; ++i) acc[i] = mfma16(a, f[i], f32x4{0.f, 0.f, 0.f, 0.f});
        if (g < 2) {
#pragma unroll
          for (int b2 = 0; b2 < 2; ++b2) {
            *(LAS f32x4*)(L + a_w + b2 * 512) = f32x4{acc[0][b2] + acc[0][b2 + 2], acc[1][b2] + acc[1][b2 + 2], acc[2][b2] + acc[2][b2 + 2], acc[3][b2] + acc[3][b2 + 2]};
            *(LAS f32x4*)(L + a_w + b2 * 512 + 16) = f32x4{acc[4][b2] + acc[4][b2 + 2], acc[5][b2] + acc[5][b2 + 2], acc[6][b2] + acc[6][b2 + 2], acc[7][b2] + acc[7][b2 + 2]};
          }
        }
        PIN();
        if (w == 2 || w == 3) load_t3();
        PIN();
        BAR();
        TS(7);
        if (w < 2) {
          f32x2 v[10];
#pragma unroll
          for (int k = 0; k < 10; ++k) v[k] = *(LAS f32x2*)(L + a_r + k * 1024);
          const float s0 = sum5(v[0][0], v[1][0], v[2][0], v[3][0], v[4][0]) + sum5(v[5][0], v[6][0], v[7][0], v[8][0], v[9][0]);
          const float s1 = sum5(v[0][1], v[1][1], v[2][1], v[3][1], v[4][1]) + sum5(v[5][1], v[6][1], v[7][1], v[8][1], v[9][1]);
          { const v4u v = pair2(pk2(s0, s1), tag); if ((l & 1) == 0) st16(v, o_x2); }
          TS(8);
          PIN();
          load_t3();
        }
      }
      {
        int off = X2 + hh * 4096 + l * 16;
        int a_gw[4];
#pragma unroll
        for (int k = 0; k < 4; ++k) { const int row = 2 * k + (l >> 5), pc = l & 31; a_gw[k] = L_X2W + (w & 3) * 2048 + row * 256 + (((pc >> 1) ^ row) * 16) + (pc & 1) * 8; }
        int a_gr[4];
#pragma unroll
        for (int s = 0; s < 4; ++s) a_gr[s] = L_X2W + (w & 3) * 2048 + (r16 & 7) * 256 + (((4 * s + g) ^ (r16 & 7)) * 16);
        int a_w = L_RED + ((g & 1) * 128 + 32 * (w & 3) + r16) * 2;
        int o_x3 = X3 + ((l >> 4) * NH + hh) * 1024 + sub * 256 + (l & 15) * 16;
        unsigned tag = TAG();
        KEEP(off); KEEP(a_w); KEEP(o_x3); KEEP4(a_gw); KEEP4(a_gr); KEEP(tag);
        if (w < 4) {
#pragma unroll
          for (int t = 0; t < 2; ++t) {
#pragma unroll
            for (int i = 0; i < 4; ++i) *(LAS v4u*)(L + L_UKT + w * 4096 + (4 * i + g) * 256 + ((r16 ^ (4 * i + g)) * 16)) = uk[4 * t + i];
#pragma unroll
            for (int s = 0; s < 4; ++s) uk[4 * t + s] = *(LAS v4u*)(L + L_UKT + w * 4096 + r16 * 256 + (((4 * s + g) ^ r16) * 16));
            KEEP(uk[4 * t]); KEEP(uk[4 * t + 1]); KEEP(uk[4 * t + 2]); KEEP(uk[4 * t + 3]);
          }
          TS(1);
        }
        PIN();
        if (w < 4) {
          v4u u[4];
          {
            unsigned pend = 15u;
            for (unsigned sp = 0; sp < SPIN_MAX; ++sp) {
              asm volatile("" ::: "memory");
#pragma unroll
              for (int k = 0; k < 4; ++k) if (pend >> k & 1u) u[k] = ld16(off + k * 1024);
#pragma unroll
              for (int k = 0; k < 4; ++k) if (pend >> k & 1u) { if (__all(((u[k].y ^ tag) | (u[k].w ^ tag)) == 0u)) pend &= ~(1u << k); }
              if (!pend) break;
            }
          }
#pragma unroll
          for (int k = 0; k < 4; ++k) *(LAS v2u*)(L + a_gw[k]) = v2u{u[k].x, u[k].z};
          v4u a[4];
#pragma unroll
          for (int s = 0; s < 4; ++s) a[s] = *(LAS v4u*)(L + a_gr[s]);
          TS(9);
          const float sc = 0.08838834764831845f * 1.4426950408889634f;
          f32x4 acc[2][2];
#pragma unroll
          for (int t = 0; t < 2; ++t) { acc[t][0] = mfma16(a[0], uk[4 * t], f32x4{0.f, 0.f, 0.f, 0.f}); acc[t][1] = mfma16(a[1], uk[4 * t + 1], f32x4{0.f, 0.f, 0.f, 0.f}); }
#pragma unroll
          for (int t = 0; t < 2; ++t) { acc[t][0] = mfma16(a[2], uk[4 * t + 2], acc[t][0]); acc[t][1] = mfma16(a[3], uk[4 * t + 3], acc[t][1]); }
#pragma unroll
          for (int t = 0; t < 2; ++t) {
            const f32x4 v = acc[t][0] + acc[t][1];
            const float tot = rs16(v[0] + v[2], v[1] + v[3]) * sc;
            if (g < 2) *(LAS u16*)(L + a_w + 32 * t) = (u16)pk2(tot, tot);
          }
        }
        BAR();
        if (w == 0 && l < 32) {
          st16(*(LAS v4u*)(L + L_RED + l * 16), o_x3);
          TS(10);
        }
      }
    } else {
      if (w == 7) {
        if (l >= 32) st16(SENT4, X3 + (((l - 32) >> 4) * NH + hh) * 1024 + sub * 256 + (l & 15) * 16);
        if (l < 32) st16(SENT4, X5 + idx * 512 + l * 16);
        else if (l < 48 && idx < 80) st16(SENT4, X6 + (((idx / 10) * 2 + ((l - 32) >> 3)) * DM + 64 * (idx % 10)) * 2 + (l & 7) * 16);
        else if (l < 48 && idx < 120) st16(SENT4, XM + (((l - 32) >> 3) * 2560 + 64 * (idx - 80)) * 2 + (l & 7) * 16);
        LOAD_EPOCH();
        DRAIN();
        if (l == 0) st4(TAG(), OFF_EFLAGS + idx * FSTR);
      } else {
        __builtin_amdgcn_s_sleep(20);
        LOAD_EPOCH();
      }
      BAR();
      BAR();
    }
    const int h5 = idx >> 2, cq = idx & 3;
    v4u w5[8], w6[2][8];
    if (w < 4) {
      const u16* src = p.wuv + ((size_t)(128 * cq + 32 * w + 8 * g) * NH + h5) * DH + 8 * r16;
#pragma unroll
      for (int j = 0; j < 8; ++j) w5[j] = ldnt(src + (size_t)j * (NH * DH));
      if (idx < 80) {
        const int kk = (l >> 3) & 1, ch = l & 7, pp = idx / 10, n = idx % 10;
        const u16* src6 = p.wo + (size_t)(512 * pp + 64 * w + 32 * kk + 8 * g) * DM + 64 * n + 8 * ch;
#pragma unroll
        for (int k = 0; k < 2; ++k)
#pragma unroll
          for (int j = 0; j < 8; ++j) w6[k][j] = ldnt(src6 + (size_t)(256 * k + j) * DM);
      }
    }
    PIN();
    if (idx >= 80 && idx < 120) {
      const int tb = idx - 80, wv = w & 3;
      if (w >= 4) {
        v4u wpre[3][8], cv[3];
        const int kk = (l >> 3) & 1, ch = l & 7;
#pragma unroll
        for (int k = 0; k < 3; ++k) cv[k] = *(const GAS v4u*)(p.c + (r16 & 1) * DM + 64 * (k < 2 || wv < 2 ? wv + 4 * k : wv) + 32 * (r16 >> 3) + 8 * g);
        const u16* src = p.wmod + (size_t)(64 * wv + 32 * kk + 8 * g) * (6 * DM) + 2 * DM + 64 * tb + 8 * ch;
#pragma unroll
        for (int k = 0; k < 2; ++k)
#pragma unroll
          for (int j = 0; j < 8; ++j) wpre[k][j] = ldnt(src + (size_t)(256 * k + j) * (6 * DM));
        if (wv < 2) {
#pragma unroll
          for (int j = 0; j < 8; ++j) wpre[2][j] = ldnt(src + (size_t)(512 + j) * (6 * DM));
        }
        PIN();
        v4u a[3];
#pragma unroll
        for (int k = 0; k < 3; ++k) a[k] = silu8(cv[k]);
        f32x4 acc[8];
#pragma unroll
        for (int i = 0; i < 8; ++i) acc[i] = f32x4{0.f, 0.f, 0.f, 0.f};
#pragma unroll
        for (int k = 0; k < 3; ++k) {
          if (k < 2 || wv < 2) {
            v4u f[8];
            tr8(wpre[k], f);
#pragma unroll
            for (int i = 0; i < 8; ++i) acc[i] = mfma16(a[k], f[i], acc[i]);
          }
        }
        if ((g == 0 && r16 < 8) || (g == 2 && r16 >= 8)) {
#pragma unroll
          for (int b2 = 0; b2 < 2; ++b2) {
            LAS float* d = (LAS float*)(L + L_X5W) + ((wv * 2 + (g >> 1)) * 2 + b2) * 64 + 8 * (r16 & 7);
            *(LAS f32x4*)d = f32x4{acc[0][b2], acc[1][b2], acc[2][b2], acc[3][b2]}; *(LAS f32x4*)(d + 4) = f32x4{acc[4][b2], acc[5][b2], acc[6][b2], acc[7][b2]};
          }
        }
      }
      BAR();
      if (w == 4) {
        const int b = l >> 5, pr = l & 31;
        float s0 = 0.f, s1 = 0.f;
#pragma unroll
        for (int k = 0; k < 8; ++k) { const f32x2 v = *(LAS f32x2*)((LAS float*)(L + L_X5W) + (k * 2 + b) * 64 + 2 * pr); s0 += v[0]; s1 += v[1]; }
        { const v4u q = quad4(pk2(s0, s1)); if ((l & 3) == 0) st16(q, XM + (b * 2560 + 64 * tb + 2 * pr) * 2); }
        TS(29);
      }
    }
    wait_flags(OFF_EFLAGS + l * FSTR); wait_flags(OFF_EFLAGS + (64 + l) * FSTR);
    {
      const int b = w >> 2;
      int off = X4 + (((b * NH + h5) * 64 + 16 * (w & 3) + g) * 512 + 128 * cq + 8 * r16) * 2;
      int offs = X4S + ((b * 64 + 16 * (w & 3) + g) * NH + h5) * 16;
      const int cofs = 8 * r16 + 4 * (l >> 5) + 2 * ((l >> 4) & 1);
      int a_w = L_RED + w * 256 + (((cofs >> 3) ^ w) * 16) + (cofs & 7) * 2;
      int a_s = L_MISC + 1024 + w * 8;
      unsigned tag = TAG();
      KEEP(off); KEEP(offs); KEEP(a_w); KEEP(a_s); KEEP(tag);
      PIN();
      v4u a[4], sv[4];
      {
        unsigned pend = 15u;
        for (unsigned sp = 0; sp < SPIN_MAX; ++sp) {
          asm volatile("" ::: "memory");
#pragma unroll
          for (int k = 0; k < 4; ++k) if (pend >> k & 1u) sv[k] = ld16(offs + k * 2048);
#pragma unroll
          for (int k = 0; k < 4; ++k) if (pend >> k & 1u) { if (__all(((sv[k].y ^ tag) | (sv[k].w ^ tag)) == 0u)) pend &= ~(1u << k); }
          if (!pend) break;
          if (S5_SLEEP) __builtin_amdgcn_s_sleep(S5_SLEEP);
        }
      }
      asm volatile("" ::: "memory");
      TS(19);
#pragma unroll
      for (int k = 0; k < 4; ++k) a[k] = ld16(off + k * 4096);
      TS(20);
      float mw = fmaxf(fmaxf(__uint_as_float(sv[0].x), __uint_as_float(sv[1].x)), fmaxf(__uint_as_float(sv[2].x), __uint_as_float(sv[3].x)));
      mw = x16_max(mw);
      float acc[8] = {0.f, 0.f, 0.f, 0.f, 0.f, 0.f, 0.f, 0.f}; float lw = 0.f;
#pragma unroll
      for (int k = 0; k < 4; ++k) {
        const float e = __builtin_amdgcn_exp2f(__uint_as_float(sv[k].x) - mw);
        const unsigned wlo = pk2(e, 0.f), whi = pk2(0.f, e);
        lw += __uint_as_float(whi) * __uint_as_float(sv[k].z);
#pragma unroll
        for (int d = 0; d < 4; ++d) { acc[2 * d] = dot2(a[k][d], wlo, acc[2 * d]); acc[2 * d + 1] = dot2(a[k][d], whi, acc[2 * d + 1]); }
      }
      const float u0 = rs16(rs32(acc[0], acc[4]), rs32(acc[2], acc[6])), u1 = rs16(rs32(acc[1], acc[5]), rs32(acc[3], acc[7]));
      *(LAS unsigned*)(L + a_w) = pk2(u0, u1);
      lw = x16_sum(lw);
      *(LAS f32x2*)(L + a_s) = f32x2{mw, lw};
    }
    {
      v4u f[8];
      if (w < 4) { tr8(w5, f); KEEP8(f); }
      const int row = r16 & 7;
      int a_a = L_RED + row * 256 + (((4 * w + g) ^ row) * 16);
      int a_st = L_MISC + 1024 + (g & 1) * 32;
      int a_w2 = L_RED2 + ((w * 2 + (g & 1)) * 128 + 8 * r16) * 4;
      int a_r2 = L_RED2 + ((w & 1) * 128 + 2 * l) * 4, o_x5 = X5 + (idx * 2 + (w & 1)) * 256 + l * 4;
      KEEP(a_a); KEEP(a_st); KEEP(a_w2); KEEP(a_r2); KEEP(o_x5);
      BAR();
      TS(21);
      if (w < 4) {
        const v4u a = *(LAS v4u*)(L + a_a);
        const f32x4 s01 = *(LAS f32x4*)(L + a_st), s23 = *(LAS f32x4*)(L + a_st + 16);
        f32x4 d[8];
#pragma unroll
        for (int i = 0; i < 8; ++i) d[i] = mfma16(a, f[i], f32x4{0.f, 0.f, 0.f, 0.f});
        const float mb = fmaxf(fmaxf(s01[0], s01[2]), fmaxf(s23[0], s23[2]));
        float f0 = __builtin_amdgcn_exp2f(s01[0] - mb), f1 = __builtin_amdgcn_exp2f(s01[2] - mb), f2 = __builtin_amdgcn_exp2f(s23[0] - mb), f3 = __builtin_amdgcn_exp2f(s23[2] - mb);
        const float inv = __builtin_amdgcn_rcpf((f0 * s01[1] + f1 * s01[3]) + (f2 * s23[1] + f3 * s23[3]));
        f0 *= inv; f1 *= inv; f2 *= inv; f3 *= inv;
        float v[8];
#pragma unroll
        for (int i = 0; i < 8; ++i) v[i] = (d[i][0] * f0 + d[i][1] * f1) + (d[i][2] * f2 + d[i][3] * f3);
        if (g < 2) { *(LAS f32x4*)(L + a_w2) = f32x4{v[0], v[1], v[2], v[3]}; *(LAS f32x4*)(L + a_w2 + 16) = f32x4{v[4], v[5], v[6], v[7]}; }
      }
      BAR();
      TS(22);
      if (w < 2) {
        f32x2 v[4];
#pragma unroll
        for (int k = 0; k < 4; ++k) v[k] = *(LAS f32x2*)(L + a_r2 + k * 1024);
        { const v4u q = quad4(pk2((v[0][0] + v[1][0]) + (v[2][0] + v[3][0]), (v[0][1] + v[1][1]) + (v[2][1] + v[3][1]))); if ((l & 3) == 0) st16(q, o_x5); }
        TS(23);
      }
    }
    if (idx < 80) {
      const int pp = idx / 10, n = idx % 10;
      if (w < 4) {
        v4u f0[8], f1[8]; tr8(w6[0], f0); tr8(w6[1], f1);
        int off = X5 + ((4 * pp + (w >> 1)) * 8 + (l >> 3)) * 256 + 128 * (w & 1) + (l & 7) * 16;
        int a_gw = L_X5W + w * 2048 + (l >> 3) * 128 + (l & 7) * 16, a_gr = L_X5W + w * 2048 + (r16 & 7) * 128 + (r16 >> 3) * 64 + g * 16;
        int a_w = L_RED + (((w * 2 + (g >> 1)) * 2 + (g & 1)) * 64 + 8 * (r16 & 7)) * 4;
        KEEP8(f0); KEEP8(f1); KEEP(off); KEEP(a_w); KEEP(a_gw); KEEP(a_gr);
        PIN();
        v4u u0, u1;
        {
          bool p0 = true, p1 = true;
          for (unsigned sp = 0; sp < SPIN_MAX; ++sp) {
            asm volatile("" ::: "memory");
            if (p0) u0 = ld16(off);
            if (p1) u1 = ld16(off + 2 * 2048);
            if (p0) p0 = !__all(umax4(u0) != SENT);
            if (p1) p1 = !__all(umax4(u1) != SENT);
            if (!p0 && !p1) break;
          }
        }
        *(LAS v4u*)(L + a_gw) = u0; *(LAS v4u*)(L + a_gw + 1024) = u1;
        const v4u a0 = *(LAS v4u*)(L + a_gr), a1 = *(LAS v4u*)(L + a_gr + 1024);
        TS(24);
        f32x4 d[8];
#pragma unroll
        for (int i = 0; i < 8; ++i) d[i] = mfma16(a0, f0[i], f32x4{0.f, 0.f, 0.f, 0.f});
#pragma unroll
        for (int i = 0; i < 8; ++i) d[i] = mfma16(a1, f1[i], d[i]);
        float s[8];
#pragma unroll
        for (int i = 0; i < 8; ++i) s[i] = rs16(d[i][0] + d[i][2], d[i][1] + d[i][3]);
        if ((g >> 1) == (r16 >> 3)) {
          *(LAS f32x4*)(L + a_w) = f32x4{s[0], s[1], s[2], s[3]}; *(LAS f32x4*)(L + a_w + 16) = f32x4{s[4], s[5], s[6], s[7]};
        }
      }
      int a_r = L_RED + ((l >> 5) * 64 + 2 * (l & 31)) * 4, o_x6 = X6 + ((pp * 2 + (l >> 5)) * DM + 64 * n + 2 * (l & 31)) * 2;
      KEEP(a_r); KEEP(o_x6);
      BAR();
      TS(25);
      if (w == 0) {
        f32x2 v[8];
#pragma unroll
        for (int k = 0; k < 8; ++k) v[k] = *(LAS f32x2*)(L + a_r + k * 512);
        const float s0 = ((v[0][0] + v[1][0]) + (v[2][0] + v[3][0])) + ((v[4][0] + v[5][0]) + (v[6][0] + v[7][0]));
        const float s1 = ((v[0][1] + v[1][1]) + (v[2][1] + v[3][1])) + ((v[4][1] + v[5][1]) + (v[6][1] + v[7][1]));
        { const v4u q = quad4(pk2(s0, s1)); if ((l & 3) == 0) st16(q, o_x6); }
        TS(29);
      }
    }
  }
done: ;
}

extern "C" void kernel_launch(void* const* d_in, const int* in_sizes, int n_in, void* d_out, int out_size, void* d_ws, size_t ws_size, hipStream_t stream) {
  Params p;
  p.x = (const u16*)d_in[0]; p.ckv = (const u16*)d_in[1]; p.wq = (const u16*)d_in[2]; p.wuk = (const u16*)d_in[3]; p.wuv = (const u16*)d_in[4]; p.wo = (const u16*)d_in[5];
  p.wg = (const u16*)d_in[7]; p.wu = (const u16*)d_in[8]; p.wd = (const u16*)d_in[9]; p.c = (const u16*)d_in[10]; p.wmod = (const u16*)d_in[11];
  p.out = (u16*)d_out;
  p.ws = (unsigned char*)d_ws;
  mega<<<256, 512, 0, stream>>>(p);
}
```

```cpp
#include <hip/hip_runtime.h>
#include <stdint.h>

#define GAS __attribute__((address_space(1)))
#define LAS __attribute__((address_space(3)))
typedef unsigned v4u __attribute__((ext_vector_type(4)));
typedef unsigned v2u __attribute__((ext_vector_type(2)));
typedef float f32x4 __attribute__((ext_vector_type(4)));
typedef float f32x2 __attribute__((ext_vector_type(2)));
typedef __bf16 bf16x8 __attribute__((ext_vector_type(8)));
typedef __bf16 bf16x2 __attribute__((ext_vector_type(2)));
typedef short s16x4 __attribute__((ext_vector_type(4)));
typedef unsigned short u16;
typedef unsigned long long u64;

constexpr int NH = 32, SK = 8192, DH = 128, DM = 640, DC = 512, DF = 1728;
constexpr int OFF_EPOCH = 0;
constexpr int FSTR = 32;
constexpr int OFF_FLAGS = 4096;
constexpr int OFF_EFLAGS = OFF_FLAGS + 256 * FSTR;
constexpr int OFF_X1 = OFF_EFLAGS + 128 * FSTR;
constexpr int OFF_R = 81920;
constexpr int X2 = OFF_R;
constexpr int X3 = X2 + 131072;
constexpr int X4 = X3 + 65536;
constexpr int X4S = X4 + 4194304;
constexpr int X5 = X4S + 65536;
constexpr int X6 = X5 + 65536;
constexpr int X7 = X6 + 20480;
constexpr int XM = X7 + 138240;
constexpr int R_END = XM + 10240;
static_assert(OFF_X1 + 20480 <= OFF_R, "map");
constexpr int WS_USED = R_END;
constexpr int E_DELAY = 65;
constexpr int E_DEPTH = 17;
constexpr int T1_EARLY = 12;
constexpr int L_DELAY = 10;
constexpr int T3_EARLY = 1;
constexpr int CODE_LINES = 232;
constexpr unsigned SENT = 0xFFFFFFFFu;
constexpr int AUX_SC1 = 16;
constexpr unsigned SPIN_MAX = 1u << 18;
constexpr int L_CKV = 0;
constexpr int L_QIMG = 131072;
constexpr int LDS_BYTES = 163840;
constexpr int L_T1RED = L_QIMG, L_T1MISC = L_QIMG + 8192;
constexpr int L_MX = 0;
constexpr int L_PIMG = 4096;
constexpr int L_RED = 16384;
constexpr int L_RED2 = 32768;
constexpr int L_MISC = 49152;
constexpr int L_UKT = 65536;
constexpr int L_X2W = 81920;
constexpr int L_X1W = 90112;
constexpr int L_X5W = 98304;

struct Params {
  const u16 *x, *ckv, *wq, *wuk, *wuv, *wo, *wg, *wu, *wd, *c, *wmod;
  u16* out;
  unsigned char* ws;
};

#define BAR() do { asm volatile("s_waitcnt lgkmcnt(0)" ::: "memory"); __builtin_amdgcn_s_barrier(); asm volatile("" ::: "memory"); } while (0)
#define DRAIN() asm volatile("s_waitcnt vmcnt(0)" ::: "memory")
#define PIN() __builtin_amdgcn_sched_barrier(0)
#define KEEP(x) asm volatile("" : "+v"(x))
#define KEEP4(a) do { KEEP(a[0]); KEEP(a[1]); KEEP(a[2]); KEEP(a[3]); } while (0)
#define KEEP8(a) do { KEEP(a[0]); KEEP(a[1]); KEEP(a[2]); KEEP(a[3]); KEEP(a[4]); KEEP(a[5]); KEEP(a[6]); KEEP(a[7]); } while (0)

__device__ __forceinline__ float bflo(unsigned d) { return __uint_as_float(d << 16); }
__device__ __forceinline__ float bfhi(unsigned d) { return __uint_as_float(d & 0xffff0000u); }
__device__ __forceinline__ unsigned pk2(float lo, float hi) { f32x2 f = {lo, hi}; return __builtin_bit_cast(unsigned, __builtin_convertvector(f, bf16x2)); }
__device__ __forceinline__ f32x4 mfma16(v4u a, v4u b, f32x4 c) { return __builtin_amdgcn_mfma_f32_16x16x32_bf16(__builtin_bit_cast(bf16x8, a), __builtin_bit_cast(bf16x8, b), c, 0, 0, 0); }
__device__ __forceinline__ float dot2(unsigned a, unsigned b, float c) { return __builtin_amdgcn_fdot2_f32_bf16(__builtin_bit_cast(bf16x2, a), __builtin_bit_cast(bf16x2, b), c, false); }
__device__ __forceinline__ v4u ldnt(const u16* p) { return __builtin_nontemporal_load((const GAS v4u*)p); }
__device__ __forceinline__ unsigned umax3(unsigned a, unsigned b, unsigned c) { unsigned t = a > b ? a : b; return t > c ? t : c; }
__device__ __forceinline__ unsigned umax4(v4u v) { return umax3(umax3(v.x, v.y, v.z), v.w, 0u); }
__device__ __forceinline__ unsigned umax4a(v4u v, unsigned m) { return umax3(umax3(v.x, v.y, v.z), v.w, m); }
__device__ __forceinline__ float x16_max(float x) {
  auto s = __builtin_amdgcn_permlane16_swap(__float_as_uint(x), __float_as_uint(x), false, false);
  x = fmaxf(__uint_as_float(s[0]), __uint_as_float(s[1]));
  auto t = __builtin_amdgcn_permlane32_swap(__float_as_uint(x), __float_as_uint(x), false, false);
  return fmaxf(__uint_as_float(t[0]), __uint_as_float(t[1]));
}
__device__ __forceinline__ float x16_sum(float x) {
  auto s = __builtin_amdgcn_permlane16_swap(__float_as_uint(x), __float_as_uint(x), false, false);
  x = __uint_as_float(s[0]) + __uint_as_float(s[1]);
  auto t = __builtin_amdgcn_permlane32_swap(__float_as_uint(x), __float_as_uint(x), false, false);
  return __uint_as_float(t[0]) + __uint_as_float(t[1]);
}
__device__ __forceinline__ float rs32(float a, float b) { auto t = __builtin_amdgcn_permlane32_swap(__float_as_uint(a), __float_as_uint(b), false, false); return __uint_as_float(t[0]) + __uint_as_float(t[1]); }
__device__ __forceinline__ float rs16(float a, float b) { auto t = __builtin_amdgcn_permlane16_swap(__float_as_uint(a), __float_as_uint(b), false, false); return __uint_as_float(t[0]) + __uint_as_float(t[1]); }
template <int CTRL> __device__ __forceinline__ float dppf(float x) { return __builtin_bit_cast(float, __builtin_amdgcn_mov_dpp(__builtin_bit_cast(int, x), CTRL, 0xf, 0xf, true)); }
__device__ __forceinline__ float row16_sum(float x) { x += dppf<0xB1>(x); x += dppf<0x4E>(x); x += dppf<0x141>(x); x += dppf<0x128>(x); return x; }
__device__ __forceinline__ float wave_sum(float x) { return x16_sum(row16_sum(x)); }
__device__ __forceinline__ float silu(float v) { return v * __builtin_amdgcn_rcpf(1.f + __builtin_amdgcn_exp2f(v * -1.4426950408889634f)); }

__device__ __forceinline__ void tr8(const v4u (&w)[8], v4u (&f)[8]) {
#pragma unroll
  for (int d = 0; d < 4; ++d) {
#pragma unroll
    for (int i2 = 0; i2 < 4; ++i2) {
      f[2 * i2][d] = __builtin_amdgcn_perm(w[2 * d + 1][i2], w[2 * d][i2], 0x05040100u);
      f[2 * i2 + 1][d] = __builtin_amdgcn_perm(w[2 * d + 1][i2], w[2 * d][i2], 0x07060302u);
    }
  }
}
__device__ __forceinline__ v4u silu8(v4u c) {
  v4u r;
#pragma unroll
  for (int d = 0; d < 4; ++d) r[d] = pk2(silu(bflo(c[d])), silu(bfhi(c[d])));
  return r;
}
template <int CTRL> __device__ __forceinline__ unsigned dppu(unsigned x) { return (unsigned)__builtin_amdgcn_mov_dpp((int)x, CTRL, 0xf, 0xf, true); }
__device__ __forceinline__ v4u quad4(unsigned d) { return v4u{dppu<0x00>(d), dppu<0x55>(d), dppu<0xAA>(d), dppu<0xFF>(d)}; }
__device__ __forceinline__ v4u pair2(unsigned a, unsigned b) { return v4u{dppu<0xA0>(a), dppu<0xA0>(b), dppu<0xF5>(a), dppu<0xF5>(b)}; }
__device__ __forceinline__ float sum5(float a, float b, float c, float d, float e) { return ((a + b) + (c + d)) + e; }

__global__ void __launch_bounds__(512, 2) mega(Params p) {
  __shared__ __attribute__((aligned(16))) unsigned char smem[LDS_BYTES];
  LAS unsigned char* L = (LAS unsigned char*)smem;
  const int tid = threadIdx.x, l = tid & 63, w = __builtin_amdgcn_readfirstlane(tid >> 6);
  const int bid = blockIdx.x;
  const int g = l >> 4, r16 = l & 15;
  if (w == 6) {
    u64 pc; asm volatile("s_getpc_b64 %0" : "=s"(pc));
    const GAS unsigned char* cp = (const GAS unsigned char*)((pc & ~127ull) + (u64)l * 128);
    const unsigned dst = (unsigned)(L_QIMG + 16384) + (unsigned)(size_t)L;
#pragma unroll
    for (int k = 0; k < 4; ++k) {
      const GAS unsigned char* q = cp + (64 * k + l < CODE_LINES ? k * 8192 : 0);
      unsigned keep;
      asm volatile("s_mov_b32 %0, m0\n\ts_mov_b32 m0, %2\n\ts_nop 0\n\tglobal_load_lds_dword %1, off\n\ts_mov_b32 m0, %0" : "=&s"(keep) : "v"(q), "s"(dst) : "memory");
    }
  }
  {
    const bool e_ = bid < 128;
    const u64 a0 = (u64)(e_ ? p.wmod : p.wq), a1 = (u64)(e_ ? p.c : p.wuk), a2 = (u64)(e_ ? p.x : p.wuv), a3 = e_ ? (u64)p.ckv + (u64)(bid & 127) * (128 * DC * 2) : (u64)p.wo, a4 = (u64)p.ws;
    const int grp = l >> 2;
    unsigned qlo = (unsigned)a4, qhi = (unsigned)(a4 >> 32);
    qlo = grp == 3 ? (unsigned)a3 : qlo; qhi = grp == 3 ? (unsigned)(a3 >> 32) : qhi;
    qlo = grp == 2 ? (unsigned)a2 : qlo; qhi = grp == 2 ? (unsigned)(a2 >> 32) : qhi;
    qlo = grp == 1 ? (unsigned)a1 : qlo; qhi = grp == 1 ? (unsigned)(a1 >> 32) : qhi;
    qlo = grp == 0 ? (unsigned)a0 : qlo; qhi = grp == 0 ? (unsigned)(a0 >> 32) : qhi;
    const unsigned mlo = e_ ? 1u : 0u;
    const unsigned single = (grp >= 1 && grp <= 3) ? mlo : 0u;
    const unsigned po = ((l & 3) == 3 || single) ? 0u : (unsigned)(l & 3) * 1900000u;
    const u64 q = (((u64)qhi << 32) | qlo) + po;
    const unsigned dst = (unsigned)(L_QIMG + 16384) + (unsigned)(size_t)L;
    unsigned keep;
    asm volatile("s_mov_b32 %0, m0\n\ts_mov_b32 m0, %2\n\ts_nop 0\n\tglobal_load_lds_dword %1, off\n\ts_mov_b32 m0, %0" : "=&s"(keep) : "v"(q), "s"(dst) : "memory");
  }
  const __amdgpu_buffer_rsrc_t rs = __builtin_amdgcn_make_buffer_rsrc((void*)p.ws, 0, WS_USED, 0x00020000);
  const bool Ltype = bid >= 128;
  const int idx = bid & 127;
  const int ab = idx >> 6, aj = idx & 63;
  unsigned epoch_raw = 0;
  auto LOAD_EPOCH = [&]() { epoch_raw = __builtin_amdgcn_raw_buffer_load_b32(rs, OFF_EPOCH, 0, AUX_SC1); };
  auto TAG = [&]() { unsigned t = epoch_raw; asm volatile("" : "+v"(t)); return t + 1u; };

  auto ld16 = [&](int off) { return __builtin_amdgcn_raw_buffer_load_b128(rs, off, 0, AUX_SC1); };
  auto ld8 = [&](int off) { return __builtin_amdgcn_raw_buffer_load_b64(rs, off, 0, AUX_SC1); };
  auto ld4 = [&](int off) { return __builtin_amdgcn_raw_buffer_load_b32(rs, off, 0, AUX_SC1); };
  auto st16 = [&](v4u v, int off) { __builtin_amdgcn_raw_buffer_store_b128(v, rs, off, 0, AUX_SC1); };
  auto st4 = [&](unsigned v, int off) { __builtin_amdgcn_raw_buffer_store_b32(v, rs, off, 0, AUX_SC1); };
  auto dma_ckv = [&](int row0, int nrows, bool paced = false, int rstride = 1) {
    const u16* base = p.ckv + ((size_t)ab * SK + aj * 128) * DC;
    for (int rr = 0; rr < nrows; ++rr) {
      if (paced) asm volatile("s_waitcnt vmcnt(%0)" :: "i"(E_DEPTH - 1) : "memory");
      const int row = row0 + rr * rstride;
      const int sw = ((row & 3) << 2) | ((row >> 2) & 3);
      const u16* src = base + row * DC + ((l ^ sw) * 8);
      unsigned keep; const unsigned dst = (unsigned)(L_CKV + row * 1024) + (unsigned)(size_t)L;
      asm volatile("s_mov_b32 %0, m0\n\ts_mov_b32 m0, %2\n\ts_nop 0\n\tglobal_load_lds_dwordx4 %1, off nt\n\ts_mov_b32 m0, %0" : "=&s"(keep) : "v"(src), "s"(dst) : "memory");
    }
  };
  const v4u SENT4 = {SENT, SENT, SENT, SENT};
  auto wait_flags = [&](int off) {
    const unsigned tag = TAG();
    for (unsigned sp = 0; sp < SPIN_MAX; ++sp) {
      asm volatile("" ::: "memory");
      if (__all(ld4(off) == tag)) break;
      __builtin_amdgcn_s_sleep(2);
    }
  };

  if (!Ltype) {
    const bool t1role = idx < 40;
    const int t78 = 2 * (8 * (idx >> 4) + (idx & 7)) + ((idx >> 3) & 1);
    const bool role78 = idx < 64 && t78 < 54;
    auto fills = [&]() {
      if (t1role) __builtin_amdgcn_s_sleep(4);
      if (role78) {
#pragma unroll
        for (int k = 0; k < 3; ++k) { const int u = k * 64 + l; if (u < 160) st16(SENT4, X7 + t78 * 2560 + u * 16); }
      }
      DRAIN();
    };
    if (t1role) {
      const int mg = idx % 10, pp1 = idx / 10;
      if (w < 5) {
        v4u wv[8];
        const int col = (r16 < 8) ? (64 * mg + 8 * r16) : (DM + 64 * mg + 8 * (r16 - 8));
        const int k0 = 160 * pp1 + 32 * w + 8 * g;
        const v4u cv = *(const GAS v4u*)(p.c + (r16 & 1) * DM + k0);
        const u16* src = p.wmod + (size_t)k0 * (6 * DM) + col;
#pragma unroll
        for (int j = 0; j < 8; ++j) wv[j] = ldnt(src + (size_t)j * (6 * DM));
        const unsigned xd = *(const GAS unsigned*)(p.x + (l >> 5) * DM + 64 * mg + 2 * (l & 31));
        LOAD_EPOCH();
        PIN();
        int a_w = L_T1RED + ((w * 2) * 128 + 8 * r16) * 4;
        int a_r = L_T1RED + ((l >> 5) * 128 + 2 * (l & 31)) * 4;
        int o_x1 = OFF_X1 + (((pp1 * 2 + (l >> 5)) * 320) + 32 * mg + (l & 31)) * 8;
        KEEP(a_w); KEEP(a_r); KEEP(o_x1);
        v4u a = silu8(cv);
        KEEP(a);
        PIN();
        v4u f[8]; tr8(wv, f);
        KEEP8(f);
        float xlo = bflo(xd), xhi = bfhi(xd);
        unsigned tag = TAG();
        KEEP(xlo); KEEP(xhi); KEEP(tag);
        PIN();
        f32x4 acc[8];
#pragma unroll
        for (int i = 0; i < 8; ++i) acc[i] = mfma16(a, f[i], f32x4{0.f, 0.f, 0.f, 0.f});
        if (g == 0) {
#pragma unroll
          for (int b = 0; b < 2; ++b) {
            *(LAS f32x4*)(L + a_w + b * 512) = f32x4{acc[0][b], acc[1][b], acc[2][b], acc[3][b]};
            *(LAS f32x4*)(L + a_w + b * 512 + 16) = f32x4{acc[4][b], acc[5][b], acc[6][b], acc[7][b]};
          }
        }
        BAR();
        if (w == 0) {
          f32x2 s[5], c2[5];
#pragma unroll
          for (int ww = 0; ww < 5; ++ww) { s[ww] = *(LAS f32x2*)(L + a_r + ww * 1024); c2[ww] = *(LAS f32x2*)(L + a_r + ww * 1024 + 256); }
          const f32x2 mr = *(LAS f32x2*)(L + L_T1MISC + (l >> 5) * 8);
          const float xn0 = (xlo - mr[0]) * mr[1], xn1 = (xhi - mr[0]) * mr[1];
          const float one = pp1 == 0 ? 1.f : 0.f;
          const float o0 = xn0 * (sum5(c2[0][0], c2[1][0], c2[2][0], c2[3][0], c2[4][0]) + one) + sum5(s[0][0], s[1][0], s[2][0], s[3][0], s[4][0]);
          const float o1 = xn1 * (sum5(c2[0][1], c2[1][1], c2[2][1], c2[3][1], c2[4][1]) + one) + sum5(s[0][1], s[1][1], s[2][1], s[3][1], s[4][1]);
          { const v4u v = pair2(pk2(o0, o1), tag); if ((l & 1) == 0) st16(v, o_x1); }
        }
        dma_ckv(T1_EARLY * 3 + w, (128 - T1_EARLY * 3 - w + 7) / 8, true, 8);
      } else if (w < 7) {
        const int b = w - 5;
        LOAD_EPOCH();
        float v[10]; float s = 0.f;
#pragma unroll
        for (int k = 0; k < 5; ++k) { const unsigned d = *(const GAS unsigned*)(p.x + b * DM + 2 * (l + 64 * k)); v[2 * k] = bflo(d); v[2 * k + 1] = bfhi(d); s += v[2 * k] + v[2 * k + 1]; }
        const float mean = wave_sum(s) * (1.f / DM);
        float q = 0.f;
#pragma unroll
        for (int k = 0; k < 10; ++k) { const float d = v[k] - mean; q += d * d; }
        const float var = wave_sum(q) * (1.f / DM);
        *(LAS f32x2*)(L + L_T1MISC + b * 8) = f32x2{mean, __builtin_amdgcn_rsqf(var + 1e-5f)};
        __builtin_amdgcn_s_sleep(E_DELAY / 2);
        dma_ckv(b, T1_EARLY, false, 3);
        BAR();
        dma_ckv(T1_EARLY * 3 + w, (128 - T1_EARLY * 3 - w + 7) / 8, true, 8);
      } else {
        LOAD_EPOCH();
        fills();
        if (l == 0) st4(TAG(), OFF_FLAGS + idx * FSTR);
        dma_ckv(2, T1_EARLY, false, 3);
        BAR();
        dma_ckv(T1_EARLY * 3 + w, (128 - T1_EARLY * 3 - w + 7) / 8, true, 8);
      }
    } else if (w < 7) {
      __builtin_amdgcn_s_sleep(E_DELAY);
      LOAD_EPOCH();
      dma_ckv(w, 17, true, 7);
    } else {
      LOAD_EPOCH();
      fills();
      if (l == 0) st4(TAG(), OFF_FLAGS + idx * FSTR);
      dma_ckv(119, 9, true);
    }
    v4u wa0[8], wa1[8], wa2[8], wdn[8];
    {
      const unsigned ef0 = ld4(OFF_EFLAGS + l * FSTR), ef1 = ld4(OFF_EFLAGS + (64 + l) * FSTR);
      int a_qw[2], a_qr[4], a_kr[4];
      const int key = 16 * w + r16;
#pragma unroll
      for (int u = 0; u < 2; ++u) a_qw[u] = L_QIMG + (8 * u + w) * 1024 + ((l ^ (8 * u + w)) * 16);
#pragma unroll
      for (int t = 0; t < 4; ++t) { a_qr[t] = L_QIMG + r16 * 1024 + (((4 * t + g) ^ r16) * 16); a_kr[t] = L_CKV + key * 1024 + (((4 * t + g) ^ (((key & 3) << 2) | ((key >> 2) & 3))) * 16); }
      int a_vr[2];
#pragma unroll
      for (int u = 0; u < 2; ++u) a_vr[u] = L_CKV + (4 * g + (r16 >> 2)) * 1024 + (((8 * w + 4 * u + (l & 3)) ^ (((r16 >> 2) << 2) | g)) * 16);
      int a_mxw = L_MX + r16 * 32 + w * 4, a_mxr = L_MX + r16 * 32;
      int a_pw = L_PIMG + (w >> 1) * 1024 + l * 16 + (w & 1) * 8, a_pr = L_PIMG + l * 16;
      int a_ow[2], a_or[4];
#pragma unroll
      for (int u = 0; u < 2; ++u) a_ow[u] = L_QIMG + r16 * 1024 + (((8 * w + 4 * u + g) ^ r16) * 16);
#pragma unroll
      for (int k = 0; k < 4; ++k) a_or[k] = L_QIMG + (4 * w + k) * 1024 + ((l ^ ((4 * w + k) & 15)) * 16);
      KEEP(a_ow[0]); KEEP(a_ow[1]); KEEP4(a_or);
      int o_st = X4 + ((ab * NH + 4 * w) * 64 + aj) * 1024 + l * 16, o_ss = X4S + ((ab * 64 + aj) * NH + 16 * (w & 1) + r16) * 16;
      int o_q = X3 + ab * 32768 + tid * 16;
      KEEP(a_qw[0]); KEEP(a_qw[1]); KEEP4(a_qr); KEEP4(a_kr); KEEP(a_vr[0]); KEEP(a_vr[1]); KEEP(a_mxw); KEEP(a_mxr); KEEP(a_pw); KEEP(a_pr); KEEP(o_st); KEEP(o_ss); KEEP(o_q);
      DRAIN();
      { const unsigned tag = TAG(); if (!__all((ef0 == tag) & (ef1 == tag))) { wait_flags(OFF_EFLAGS + l * FSTR); wait_flags(OFF_EFLAGS + (64 + l) * FSTR); } }
      BAR();
      v4u qa[4];
#pragma unroll
      for (int k = 0; k < 4; ++k) qa[k] = ld16(o_q + k * 8192);
      PIN();
      v4u qk[16], pv[16];
#pragma unroll
      for (int s = 0; s < 16; ++s) qk[s] = *(LAS v4u*)(L + a_kr[s & 3] + (s >> 2) * 256);
#pragma unroll
      for (int s = 0; s < 4; ++s)
#pragma unroll
        for (int t = 0; t < 4; ++t) {
          v2u h2[2];
#pragma unroll
          for (int hf = 0; hf < 2; ++hf)
            h2[hf] = __builtin_bit_cast(v2u, __builtin_amdgcn_ds_read_tr16_b64_v4i16((LAS s16x4*)(L + a_vr[t >> 1] + (32 * s + 16 * hf) * 1024 + 8 * (t & 1))));
          pv[s * 4 + t] = v4u{h2[0].x, h2[0].y, h2[1].x, h2[1].y};
        }
#pragma unroll
      for (int s = 0; s < 16; ++s) { KEEP(qk[s]); KEEP(pv[s]); }
      PIN();
      {
        unsigned pend = 15u;
        for (unsigned sp = 0; sp < SPIN_MAX; ++sp) {
#pragma unroll
          for (int k = 0; k < 4; ++k) if (pend >> k & 1u) { if (__all(umax4(qa[k]) != SENT)) pend &= ~(1u << k); }
          if (!pend) break;
          asm volatile("" ::: "memory");
#pragma unroll
          for (int k = 0; k < 4; ++k) if (pend >> k & 1u) qa[k] = ld16(o_q + k * 8192);
        }
      }
#pragma unroll
      for (int k = 0; k < 4; ++k) *(LAS v4u*)(L + a_qw[k & 1] + (k >> 1) * 16384) = qa[k];
      BAR();
      f32x4 sacc[2][2];
#pragma unroll
      for (int ht = 0; ht < 2; ++ht) { sacc[ht][0] = f32x4{0.f, 0.f, 0.f, 0.f}; sacc[ht][1] = f32x4{0.f, 0.f, 0.f, 0.f}; }
      {
        v4u fa[8], fb[8];
#pragma unroll
        for (int s = 0; s < 8; ++s) fa[s] = *(LAS v4u*)(L + a_qr[s & 3] + (s >> 2) * 256);
#pragma unroll
        for (int s = 0; s < 8; ++s) fb[s] = *(LAS v4u*)(L + a_qr[s & 3] + (2 + (s >> 2)) * 256);
        PIN();
#pragma unroll
        for (int s = 0; s < 8; ++s) sacc[0][s & 1] = mfma16(qk[s], fa[s], sacc[0][s & 1]);
        PIN();
#pragma unroll
        for (int s = 0; s < 8; ++s) fa[s] = *(LAS v4u*)(L + a_qr[s & 3] + 16384 + (s >> 2) * 256);
        PIN();
#pragma unroll
        for (int s = 0; s < 8; ++s) sacc[0][s & 1] = mfma16(qk[8 + s], fb[s], sacc[0][s & 1]);
        PIN();
#pragma unroll
        for (int s = 0; s < 8; ++s) fb[s] = *(LAS v4u*)(L + a_qr[s & 3] + 16384 + (2 + (s >> 2)) * 256);
        PIN();
#pragma unroll
        for (int s = 0; s < 8; ++s) sacc[1][s & 1] = mfma16(qk[s], fa[s], sacc[1][s & 1]);
#pragma unroll
        for (int s = 0; s < 8; ++s) sacc[1][s & 1] = mfma16(qk[8 + s], fb[s], sacc[1][s & 1]);
      }
      PIN();
      unsigned lf[2] = {0u, 0u};
      if (w == 6) lf[0] = ld4(OFF_FLAGS + l * FSTR);
      f32x4 sc[2];
#pragma unroll
      for (int ht = 0; ht < 2; ++ht) {
        sc[ht] = sacc[ht][0] + sacc[ht][1];
        *(LAS float*)(L + a_mxw + ht * 512) = x16_max(fmaxf(fmaxf(sc[ht][0], sc[ht][1]), fmaxf(sc[ht][2], sc[ht][3])));
      }
      BAR();
      float m[2];
#pragma unroll
      for (int ht = 0; ht < 2; ++ht) {
        const f32x4 m0 = *(LAS f32x4*)(L + a_mxr + ht * 512), m1 = *(LAS f32x4*)(L + a_mxr + ht * 512 + 16);
        m[ht] = fmaxf(fmaxf(fmaxf(m0[0], m0[1]), fmaxf(m0[2], m0[3])), fmaxf(fmaxf(m1[0], m1[1]), fmaxf(m1[2], m1[3])));
        *(LAS v2u*)(L + a_pw + ht * 4096) = v2u{pk2(__builtin_amdgcn_exp2f(sc[ht][0] - m[ht]), __builtin_amdgcn_exp2f(sc[ht][1] - m[ht])), pk2(__builtin_amdgcn_exp2f(sc[ht][2] - m[ht]), __builtin_amdgcn_exp2f(sc[ht][3] - m[ht]))};
      }
      BAR();
      v4u pf[2][4];
#pragma unroll
      for (int ht = 0; ht < 2; ++ht)
#pragma unroll
        for (int s = 0; s < 4; ++s) pf[ht][s] = *(LAS v4u*)(L + a_pr + ht * 4096 + s * 1024);
      PIN();
#pragma unroll
      for (int ht = 0; ht < 2; ++ht) {
        f32x4 o[4];
#pragma unroll
        for (int t = 0; t < 4; ++t) o[t] = f32x4{0.f, 0.f, 0.f, 0.f};
#pragma unroll
        for (int s = 0; s < 4; ++s)
#pragma unroll
          for (int t = 0; t < 4; ++t) o[t] = mfma16(pv[s * 4 + t], pf[ht][s], o[t]);
        *(LAS v4u*)(L + a_ow[0] + ht * 16384) = v4u{pk2(o[0][0], o[0][1]), pk2(o[0][2], o[0][3]), pk2(o[1][0], o[1][1]), pk2(o[1][2], o[1][3])};
        *(LAS v4u*)(L + a_ow[1] + ht * 16384) = v4u{pk2(o[2][0], o[2][1]), pk2(o[2][2], o[2][3]), pk2(o[3][0], o[3][1]), pk2(o[3][2], o[3][3])};
      }
      float lsum = 0.f;
      if (w < 2) {
        const v4u ones = {0x3F803F80u, 0x3F803F80u, 0x3F803F80u, 0x3F803F80u};
        f32x4 lacc = {0.f, 0.f, 0.f, 0.f};
        if (w == 0) {
#pragma unroll
          for (int s = 0; s < 4; ++s) lacc = mfma16(ones, pf[0][s], lacc);
        } else {
#pragma unroll
          for (int s = 0; s < 4; ++s) lacc = mfma16(ones, pf[1][s], lacc);
        }
        lsum = lacc[0];
      }
      BAR();
#pragma unroll
      for (int k = 0; k < 4; ++k) st16(*(LAS v4u*)(L + a_or[k]), o_st + k * 65536);
      DRAIN();
      BAR();
      if (w < 2 && g == 0) { const unsigned tag = TAG(); st16(v4u{__float_as_uint(w == 0 ? m[0] : m[1]), tag, __float_as_uint(lsum), tag}, o_ss); }
      PIN();
      if (role78) {
        if (w >= 4) {
          const u16* wsrc78 = (((l & 7) < 4) ? p.wg : p.wu) + 32 * t78 + 8 * (l & 3) + (size_t)(64 * (w & 3) + 32 * ((l >> 3) & 1) + 8 * g) * DF;
#pragma unroll
          for (int j = 0; j < 8; ++j) wa0[j] = ldnt(wsrc78 + (size_t)j * DF);
#pragma unroll
          for (int j = 0; j < 8; ++j) wa1[j] = ldnt(wsrc78 + (size_t)(256 + j) * DF);
          if (w < 6) {
#pragma unroll
            for (int j = 0; j < 8; ++j) wa2[j] = ldnt(wsrc78 + (size_t)(512 + j) * DF);
          }
        }
        if (w >= 3) {
          const u16* src = p.wd + (size_t)(32 * t78 + 8 * g) * DM + 128 * (w - 3) + 8 * r16;
#pragma unroll
          for (int j = 0; j < 8; ++j) wdn[j] = ldnt(src + (size_t)j * DM);
        }
        PIN();
      }
      if (w == 6) {
        const unsigned tag = TAG();
        if (!__all(lf[0] == tag)) wait_flags(OFF_FLAGS + l * FSTR);
      }
      BAR();
    }
    if (role78) {
      v4u fa[3][8], fd[8];
      if (w >= 4) { tr8(wa0, fa[0]); tr8(wa1, fa[1]); if (w < 6) tr8(wa2, fa[2]); KEEP8(fa[0]); KEEP8(fa[1]); if (w < 6) KEEP8(fa[2]); }
      if (w >= 3) { tr8(wdn, fd); KEEP8(fd); }
      const int b = w / 3, cc = 32 * (w % 3) + (l & 31), half = l >> 5;
      const bool act_lane = (w < 6) && (cc < 80);
      float x1[4] = {0.f, 0.f, 0.f, 0.f}, cb4[4], sb4[4];
      int a_h2w = L_RED + (b * DM + 8 * cc + 4 * half) * 2;
      if (w < 6) {
        const int ccc = cc < 80 ? cc : 79;
        const int mo = 8 * ccc + 4 * half;
        v2u gav, sbv, cbv;
        for (unsigned sp = 0; sp < SPIN_MAX; ++sp) {
          asm volatile("" ::: "memory");
          gav = ld8(XM + (b * 2560 + mo) * 2); sbv = ld8(XM + (b * 2560 + 640 + mo) * 2); cbv = ld8(XM + (b * 2560 + 1280 + mo) * 2);
          if (__all(umax3(umax3(gav.x, gav.y, sbv.x), umax3(sbv.y, cbv.x, cbv.y), 0u) != SENT)) break;
          __builtin_amdgcn_s_sleep(8);
        }
        const v2u xv = *(const GAS v2u*)(p.x + b * DM + mo);
        float xf[4] = {bflo(xv.x), bfhi(xv.x), bflo(xv.y), bfhi(xv.y)}, ga4[4] = {bflo(gav.x), bfhi(gav.x), bflo(gav.y), bfhi(gav.y)};
        cb4[0] = 1.f + bflo(cbv.x); cb4[1] = 1.f + bfhi(cbv.x); cb4[2] = 1.f + bflo(cbv.y); cb4[3] = 1.f + bfhi(cbv.y);
        sb4[0] = bflo(sbv.x); sb4[1] = bfhi(sbv.x); sb4[2] = bflo(sbv.y); sb4[3] = bfhi(sbv.y);
        if (!act_lane) { ga4[0] = ga4[1] = ga4[2] = ga4[3] = 0.f; xf[0] = xf[1] = xf[2] = xf[3] = 0.f; }
        int off = X6 + ((4 * half * 2 + b) * DM + 8 * ccc) * 2;
        int a_st = L_MISC + w * 8;
        unsigned one_lo = 0x00003F80u, one_hi = 0x3F800000u;
        KEEP(one_lo); KEEP(one_hi);
        KEEP4(xf); KEEP4(ga4); KEEP4(cb4); KEEP4(sb4); KEEP(off); KEEP(a_st); KEEP(a_h2w);
        PIN();
        v4u a[4];
        {
          unsigned pend = 15u;
          for (unsigned sp = 0; sp < SPIN_MAX; ++sp) {
            asm volatile("" ::: "memory");
#pragma unroll
            for (int k = 0; k < 4; ++k) if (pend >> k & 1u) a[k] = ld16(off + k * 2560);
#pragma unroll
            for (int k = 0; k < 4; ++k) if (pend >> k & 1u) { if (__all(umax4(a[k]) != SENT)) pend &= ~(1u << k); }
            if (!pend) break;
          }
        }
        float acc[8];
#pragma unroll
        for (int d = 0; d < 4; ++d) {
          acc[2 * d] = dot2(a[3][d], one_lo, dot2(a[2][d], one_lo, dot2(a[1][d], one_lo, bflo(a[0][d]))));
          acc[2 * d + 1] = dot2(a[3][d], one_hi, dot2(a[2][d], one_hi, dot2(a[1][d], one_hi, bfhi(a[0][d]))));
        }
#pragma unroll
        for (int e = 0; e < 4; ++e) x1[e] = xf[e] + ga4[e] * rs32(acc[e], acc[e + 4]);
        const float s1 = wave_sum((x1[0] + x1[1]) + (x1[2] + x1[3]));
        const float s2 = wave_sum((x1[0] * x1[0] + x1[1] * x1[1]) + (x1[2] * x1[2] + x1[3] * x1[3]));
        *(LAS f32x2*)(L + a_st) = f32x2{s1, s2};
      }
      int a_st2 = L_MISC + b * 24;
      int a_ar = L_RED + ((r16 & 1) * DM + 64 * (w & 3) + 32 * (r16 >> 3) + 8 * g) * 2;
      int a_rw = L_RED2 + ((((w & 3) * 2 + (g >> 1)) * 2) * 64 + 8 * (r16 & 7)) * 4;
      KEEP(a_st2); KEEP(a_ar); KEEP(a_rw);
      BAR();
      if (act_lane) {
        const f32x2 t0 = *(LAS f32x2*)(L + a_st2), t1 = *(LAS f32x2*)(L + a_st2 + 8), t2 = *(LAS f32x2*)(L + a_st2 + 16);
        const float mean = (t0[0] + t1[0] + t2[0]) * (1.f / DM);
        const float var = fmaxf((t0[1] + t1[1] + t2[1]) * (1.f / DM) - mean * mean, 0.f);
        const float rstd = __builtin_amdgcn_rsqf(var + 1e-5f);
        float hv[4];
#pragma unroll
        for (int e = 0; e < 4; ++e) hv[e] = (x1[e] - mean) * rstd * cb4[e] + sb4[e];
        *(LAS v2u*)(L + a_h2w) = v2u{pk2(hv[0], hv[1]), pk2(hv[2], hv[3])};
      }
      BAR();
      if (w >= 4) {
        const v4u a0 = *(LAS v4u*)(L + a_ar), a1 = *(LAS v4u*)(L + a_ar + 512);
        v4u a2 = a0;
        if (w < 6) a2 = *(LAS v4u*)(L + a_ar + 1024);
        f32x4 acc[8];
#pragma unroll
        for (int i = 0; i < 8; ++i) acc[i] = mfma16(a0, fa[0][i], f32x4{0.f, 0.f, 0.f, 0.f});
#pragma unroll
        for (int i = 0; i < 8; ++i) acc[i] = mfma16(a1, fa[1][i], acc[i]);
        if (w < 6) {
#pragma unroll
          for (int i = 0; i < 8; ++i) acc[i] = mfma16(a2, fa[2][i], acc[i]);
        }
        if ((g == 0 && r16 < 8) || (g == 2 && r16 >= 8)) {
#pragma unroll
          for (int b2 = 0; b2 < 2; ++b2) {
            *(LAS f32x4*)(L + a_rw + b2 * 256) = f32x4{acc[0][b2], acc[1][b2], acc[2][b2], acc[3][b2]};
            *(LAS f32x4*)(L + a_rw + b2 * 256 + 16) = f32x4{acc[4][b2], acc[5][b2], acc[6][b2], acc[7][b2]};
          }
        }
      }
      int a_gr = L_RED2 + ((l >> 5) * 64 + (l & 31)) * 4, a_aw = L_MISC + 512 + l * 2, a_acr = L_MISC + 512 + ((r16 & 1) * 32 + 8 * g) * 2;
      int o_x7 = X7 + (t78 * 2 * DM + 128 * (w - 3) + 8 * r16) * 2;
      KEEP(a_gr); KEEP(a_aw); KEEP(a_acr); KEEP(o_x7);
      BAR();
      if (w == 0) {
        float gv[8], uv[8];
#pragma unroll
        for (int k = 0; k < 8; ++k) { gv[k] = *(LAS float*)(L + a_gr + k * 512); uv[k] = *(LAS float*)(L + a_gr + k * 512 + 128); }
        const float gs = ((gv[0] + gv[1]) + (gv[2] + gv[3])) + ((gv[4] + gv[5]) + (gv[6] + gv[7]));
        const float us = ((uv[0] + uv[1]) + (uv[2] + uv[3])) + ((uv[4] + uv[5]) + (uv[6] + uv[7]));
        *(LAS u16*)(L + a_aw) = (u16)pk2(silu(gs) * us, 0.f);
      }
      BAR();
      if (w >= 3) {
        const v4u a = *(LAS v4u*)(L + a_acr);
        f32x4 d[8];
#pragma unroll
        for (int i = 0; i < 8; ++i) d[i] = mfma16(a, fd[i], f32x4{0.f, 0.f, 0.f, 0.f});
        if (g == 0) {
#pragma unroll
          for (int b2 = 0; b2 < 2; ++b2)
            st16(v4u{pk2(d[0][b2], d[1][b2]), pk2(d[2][b2], d[3][b2]), pk2(d[4][b2], d[5][b2]), pk2(d[6][b2], d[7][b2])}, o_x7 + b2 * DM * 2);
        }
      }
    } else if (idx >= 94 && idx < 114) {
      const int n = idx - 94;
      float base0 = 0.f, base1 = 0.f, gb0 = 0.f, gb1 = 0.f;
      if (w == 7 && l < 32) {
        const int b = l >> 4, pr = l & 15, m = 32 * n + 2 * pr;
        unsigned ga = 0, gb = 0, ap[8];
        for (unsigned sp = 0; sp < SPIN_MAX; ++sp) {
          asm volatile("" ::: "memory");
          ga = ld4(XM + (b * 2560 + m) * 2); gb = ld4(XM + (b * 2560 + 1920 + m) * 2);
#pragma unroll
          for (int k = 0; k < 8; ++k) ap[k] = ld4(X6 + ((k * 2 + b) * DM + m) * 2);
          if (__all(umax3(umax3(umax3(ap[0], ap[1], ap[2]), umax3(ap[3], ap[4], ap[5]), umax3(ap[6], ap[7], ga)), gb, 0u) != SENT)) break;
          __builtin_amdgcn_s_sleep(8);
        }
        const unsigned xd = *(const GAS unsigned*)(p.x + b * DM + m);
        float s0 = 0.f, s1 = 0.f;
#pragma unroll
        for (int k = 0; k < 8; ++k) { s0 += bflo(ap[k]); s1 += bfhi(ap[k]); }
        base0 = bflo(xd) + bflo(ga) * s0; base1 = bfhi(xd) + bfhi(ga) * s1; gb0 = bflo(gb); gb1 = bfhi(gb);
        KEEP(base0); KEEP(base1); KEEP(gb0); KEEP(gb1);
      }
      int a_fr = L_RED + ((l >> 4) * 32 + 2 * (l & 15)) * 4;
      KEEP(a_fr);
      if (w < 7) {
        const int task = 8 * w + (l >> 3), b = (l >> 2) & 1, ch = l & 3;
        const bool valid = task < 54;
        int off = X7 + (((valid ? task : 0) * 2 + b) * DM + 32 * n + 8 * ch) * 2;
        int a_w = L_RED + (w * 64 + b * 32 + 8 * ch + 4 * (l >> 5) + 2 * ((l >> 4) & 1)) * 4;
        unsigned msk = valid ? 0xFFFFFFFFu : 0u;
        KEEP(off); KEEP(a_w); KEEP(msk);
        PIN();
        v4u a;
        for (unsigned sp = 0; sp < SPIN_MAX; ++sp) {
          asm volatile("" ::: "memory");
          a = ld16(off);
          if (__all(umax4(a) != SENT)) break;
        }
        float v[8];
#pragma unroll
        for (int d = 0; d < 4; ++d) { const unsigned x = a[d] & msk; v[2 * d] = bflo(x); v[2 * d + 1] = bfhi(x); }
        float t0 = rs16(rs32(v[0], v[4]), rs32(v[2], v[6])), t1 = rs16(rs32(v[1], v[5]), rs32(v[3], v[7]));
        t0 += dppf<0x128>(t0); t1 += dppf<0x128>(t1);
        *(LAS f32x2*)(L + a_w) = f32x2{t0, t1};
      }
      BAR();
      if (w == 7 && l < 32) {
        f32x2 v[7];
#pragma unroll
        for (int k = 0; k < 7; ++k) v[k] = *(LAS f32x2*)(L + a_fr + k * 256);
        const float s0 = ((v[0][0] + v[1][0]) + (v[2][0] + v[3][0])) + ((v[4][0] + v[5][0]) + v[6][0]);
        const float s1 = ((v[0][1] + v[1][1]) + (v[2][1] + v[3][1])) + ((v[4][1] + v[5][1]) + v[6][1]);
        *(GAS unsigned*)(p.out + (l >> 4) * DM + 32 * n + 2 * (l & 15)) = pk2(base0 + gb0 * s0, base1 + gb1 * s1);
        if (n == 0 && l == 0) st4(TAG(), OFF_EPOCH);
      }
    }
  } else {
    const int hh = idx >> 2, sub = idx & 3;
    if (w < 5) {
      v4u wv2[8], uk[8];
      const u16* src2 = p.wq + (size_t)(160 * sub + 32 * w + 8 * g) * (NH * DH) + hh * DH + 8 * r16;
      auto load_t3 = [&](int t0, int t1) {
        const u16* src = p.wuk + ((size_t)(128 * sub + 32 * (w & 3) + g) * NH + hh) * DH + 8 * r16;
#pragma unroll
        for (int t = 0; t < 2; ++t)
          if (t >= t0 && t < t1) {
#pragma unroll
            for (int i = 0; i < 4; ++i) uk[4 * t + i] = ldnt(src + (size_t)(16 * t + 4 * i) * NH * DH);
          }
      };
      auto load_t23 = [&]() {
#pragma unroll
        for (int j = 0; j < 8; ++j) wv2[j] = ldnt(src2 + (size_t)j * (NH * DH));
        if (w < 4) load_t3(0, T3_EARLY);
      };
      __builtin_amdgcn_s_sleep(L_DELAY);
      load_t23();
      LOAD_EPOCH();
      PIN();
      {
        unsigned tag = TAG();
        v4u f[8]; tr8(wv2, f);
        int off = OFF_X1 + ((l >> 3) * 320 + (160 * sub + 32 * w) / 2 + 2 * (l & 7)) * 8;
        int a_gw = L_X1W + w * 512 + (l >> 3) * 64 + (l & 7) * 8, a_gr = L_X1W + w * 512 + (r16 & 7) * 64 + g * 16;
        int a_w = L_RED2 + (((w * 2 + (g & 1)) * 2) * 128 + 8 * r16) * 4;
        int a_r = L_RED2 + ((w & 1) * 128 + 2 * l) * 4, o_x2 = X2 + ((hh * 4 + sub) * 2 + (w & 1)) * 512 + l * 8;
        KEEP8(f); KEEP(off); KEEP(a_w); KEEP(tag); KEEP(a_r); KEEP(o_x2); KEEP(a_gw); KEEP(a_gr);
        PIN();
        v4u u0;
        for (unsigned sp = 0; sp < SPIN_MAX; ++sp) {
          asm volatile("" ::: "memory");
          u0 = ld16(off);
          if (__all(((u0.y ^ tag) | (u0.w ^ tag)) == 0u)) break;
        }
        *(LAS v2u*)(L + a_gw) = v2u{u0.x, u0.z};
        const v4u a = *(LAS v4u*)(L + a_gr);
        f32x4 acc[8];
#pragma unroll
        for (int i = 0; i < 8; ++i) acc[i] = mfma16(a, f[i], f32x4{0.f, 0.f, 0.f, 0.f});
        if (g < 2) {
#pragma unroll
          for (int b2 = 0; b2 < 2; ++b2) {
            *(LAS f32x4*)(L + a_w + b2 * 512) = f32x4{acc[0][b2] + acc[0][b2 + 2], acc[1][b2] + acc[1][b2 + 2], acc[2][b2] + acc[2][b2 + 2], acc[3][b2] + acc[3][b2 + 2]};
            *(LAS f32x4*)(L + a_w + b2 * 512 + 16) = f32x4{acc[4][b2] + acc[4][b2 + 2], acc[5][b2] + acc[5][b2 + 2], acc[6][b2] + acc[6][b2 + 2], acc[7][b2] + acc[7][b2 + 2]};
          }
        }
        PIN();
        if (w == 2 || w == 3) load_t3(T3_EARLY, 2);
        PIN();
        BAR();
        if (w < 2) {
          f32x2 v[10];
#pragma unroll
          for (int k = 0; k < 10; ++k) v[k] = *(LAS f32x2*)(L + a_r + k * 1024);
          const float s0 = sum5(v[0][0], v[1][0], v[2][0], v[3][0], v[4][0]) + sum5(v[5][0], v[6][0], v[7][0], v[8][0], v[9][0]);
          const float s1 = sum5(v[0][1], v[1][1], v[2][1], v[3][1], v[4][1]) + sum5(v[5][1], v[6][1], v[7][1], v[8][1], v[9][1]);
          { const v4u v = pair2(pk2(s0, s1), tag); if ((l & 1) == 0) st16(v, o_x2); }
          PIN();
          load_t3(T3_EARLY, 2);
        }
      }
      {
        int off = X2 + hh * 4096 + l * 16;
        int a_gw[4];
#pragma unroll
        for (int k = 0; k < 4; ++k) { const int row = 2 * k + (l >> 5), pc = l & 31; a_gw[k] = L_X2W + (w & 3) * 2048 + row * 256 + (((pc >> 1) ^ row) * 16) + (pc & 1) * 8; }
        int a_gr[4];
#pragma unroll
        for (int s = 0; s < 4; ++s) a_gr[s] = L_X2W + (w & 3) * 2048 + (r16 & 7) * 256 + (((4 * s + g) ^ (r16 & 7)) * 16);
        int a_w = L_RED + ((g & 1) * 128 + 32 * (w & 3) + r16) * 2;
        int o_x3 = X3 + ((l >> 4) * NH + hh) * 1024 + sub * 256 + (l & 15) * 16;
        unsigned tag = TAG();
        KEEP(off); KEEP(a_w); KEEP(o_x3); KEEP4(a_gw); KEEP4(a_gr); KEEP(tag);
        if (w < 4) {
#pragma unroll
          for (int t = 0; t < 2; ++t) {
#pragma unroll
            for (int i = 0; i < 4; ++i) *(LAS v4u*)(L + L_UKT + w * 4096 + (4 * i + g) * 256 + ((r16 ^ (4 * i + g)) * 16)) = uk[4 * t + i];
#pragma unroll
            for (int s = 0; s < 4; ++s) uk[4 * t + s] = *(LAS v4u*)(L + L_UKT + w * 4096 + r16 * 256 + (((4 * s + g) ^ r16) * 16));
            KEEP(uk[4 * t]); KEEP(uk[4 * t + 1]); KEEP(uk[4 * t + 2]); KEEP(uk[4 * t + 3]);
          }
        }
        PIN();
        if (w < 4) {
          v4u u[4];
          {
            unsigned pend = 15u;
            for (unsigned sp = 0; sp < SPIN_MAX; ++sp) {
              asm volatile("" ::: "memory");
#pragma unroll
              for (int k = 0; k < 4; ++k) if (pend >> k & 1u) u[k] = ld16(off + k * 1024);
#pragma unroll
              for (int k = 0; k < 4; ++k) if (pend >> k & 1u) { if (__all(((u[k].y ^ tag) | (u[k].w ^ tag)) == 0u)) pend &= ~(1u << k); }
              if (!pend) break;
            }
          }
#pragma unroll
          for (int k = 0; k < 4; ++k) *(LAS v2u*)(L + a_gw[k]) = v2u{u[k].x, u[k].z};
          v4u a[4];
#pragma unroll
          for (int s = 0; s < 4; ++s) a[s] = *(LAS v4u*)(L + a_gr[s]);
          const float sc = 0.08838834764831845f * 1.4426950408889634f;
          f32x4 acc[2][2];
#pragma unroll
          for (int t = 0; t < 2; ++t) { acc[t][0] = mfma16(a[0], uk[4 * t], f32x4{0.f, 0.f, 0.f, 0.f}); acc[t][1] = mfma16(a[1], uk[4 * t + 1], f32x4{0.f, 0.f, 0.f, 0.f}); }
#pragma unroll
          for (int t = 0; t < 2; ++t) { acc[t][0] = mfma16(a[2], uk[4 * t + 2], acc[t][0]); acc[t][1] = mfma16(a[3], uk[4 * t + 3], acc[t][1]); }
#pragma unroll
          for (int t = 0; t < 2; ++t) {
            const f32x4 v = acc[t][0] + acc[t][1];
            const float tot = rs16(v[0] + v[2], v[1] + v[3]) * sc;
            if (g < 2) *(LAS u16*)(L + a_w + 32 * t) = (u16)pk2(tot, tot);
          }
        }
        BAR();
        if (w == 0 && l < 32) {
          st16(*(LAS v4u*)(L + L_RED + l * 16), o_x3);
        }
      }
    } else {
      if (w == 7) {
        if (l >= 32) st16(SENT4, X3 + (((l - 32) >> 4) * NH + hh) * 1024 + sub * 256 + (l & 15) * 16);
        if (l < 32) st16(SENT4, X5 + idx * 512 + l * 16);
        else if (l < 48 && idx < 80) st16(SENT4, X6 + (((idx / 10) * 2 + ((l - 32) >> 3)) * DM + 64 * (idx % 10)) * 2 + (l & 7) * 16);
        else if (l < 48 && idx < 120) st16(SENT4, XM + (((l - 32) >> 3) * 2560 + 64 * (idx - 80)) * 2 + (l & 7) * 16);
        LOAD_EPOCH();
        DRAIN();
        if (l == 0) st4(TAG(), OFF_EFLAGS + idx * FSTR);
      } else {
        __builtin_amdgcn_s_sleep(20);
        LOAD_EPOCH();
      }
      BAR();
      BAR();
    }
    const int h5 = idx >> 2, cq = idx & 3;
    v4u w5[8], w6[2][8];
    if (w < 4) {
      const u16* src = p.wuv + ((size_t)(128 * cq + 32 * w + 8 * g) * NH + h5) * DH + 8 * r16;
#pragma unroll
      for (int j = 0; j < 8; ++j) w5[j] = ldnt(src + (size_t)j * (NH * DH));
      if (idx < 80) {
        const int kk = (l >> 3) & 1, ch = l & 7, pp = idx / 10, n = idx % 10;
        const u16* src6 = p.wo + (size_t)(512 * pp + 64 * w + 32 * kk + 8 * g) * DM + 64 * n + 8 * ch;
#pragma unroll
        for (int k = 0; k < 2; ++k)
#pragma unroll
          for (int j = 0; j < 8; ++j) w6[k][j] = ldnt(src6 + (size_t)(256 * k + j) * DM);
      }
    }
    PIN();
    if (idx >= 80 && idx < 120) {
      const int tb = idx - 80, wv = w & 3;
      if (w >= 4) {
        v4u wpre[3][8], cv[3];
        const int kk = (l >> 3) & 1, ch = l & 7;
#pragma unroll
        for (int k = 0; k < 3; ++k) cv[k] = *(const GAS v4u*)(p.c + (r16 & 1) * DM + 64 * (k < 2 || wv < 2 ? wv + 4 * k : wv) + 32 * (r16 >> 3) + 8 * g);
        const u16* src = p.wmod + (size_t)(64 * wv + 32 * kk + 8 * g) * (6 * DM) + 2 * DM + 64 * tb + 8 * ch;
#pragma unroll
        for (int k = 0; k < 2; ++k)
#pragma unroll
          for (int j = 0; j < 8; ++j) wpre[k][j] = ldnt(src + (size_t)(256 * k + j) * (6 * DM));
        if (wv < 2) {
#pragma unroll
          for (int j = 0; j < 8; ++j) wpre[2][j] = ldnt(src + (size_t)(512 + j) * (6 * DM));
        }
        PIN();
        v4u a[3];
#pragma unroll
        for (int k = 0; k < 3; ++k) a[k] = silu8(cv[k]);
        f32x4 acc[8];
#pragma unroll
        for (int i = 0; i < 8; ++i) acc[i] = f32x4{0.f, 0.f, 0.f, 0.f};
#pragma unroll
        for (int k = 0; k < 3; ++k) {
          if (k < 2 || wv < 2) {
            v4u f[8];
            tr8(wpre[k], f);
#pragma unroll
            for (int i = 0; i < 8; ++i) acc[i] = mfma16(a[k], f[i], acc[i]);
          }
        }
        if ((g == 0 && r16 < 8) || (g == 2 && r16 >= 8)) {
#pragma unroll
          for (int b2 = 0; b2 < 2; ++b2) {
            LAS float* d = (LAS float*)(L + L_X5W) + ((wv * 2 + (g >> 1)) * 2 + b2) * 64 + 8 * (r16 & 7);
            *(LAS f32x4*)d = f32x4{acc[0][b2], acc[1][b2], acc[2][b2], acc[3][b2]}; *(LAS f32x4*)(d + 4) = f32x4{acc[4][b2], acc[5][b2], acc[6][b2], acc[7][b2]};
          }
        }
      }
      BAR();
      if (w == 4) {
        const int b = l >> 5, pr = l & 31;
        float s0 = 0.f, s1 = 0.f;
#pragma unroll
        for (int k = 0; k < 8; ++k) { const f32x2 v = *(LAS f32x2*)((LAS float*)(L + L_X5W) + (k * 2 + b) * 64 + 2 * pr); s0 += v[0]; s1 += v[1]; }
        { const v4u q = quad4(pk2(s0, s1)); if ((l & 3) == 0) st16(q, XM + (b * 2560 + 64 * tb + 2 * pr) * 2); }
      }
    }
    wait_flags(OFF_EFLAGS + l * FSTR); wait_flags(OFF_EFLAGS + (64 + l) * FSTR);
    {
      const int b = w >> 2;
      int off = X4 + (((b * NH + h5) * 64 + 16 * (w & 3) + g) * 512 + 128 * cq + 8 * r16) * 2;
      int offs = X4S + ((b * 64 + 16 * (w & 3) + g) * NH + h5) * 16;
      const int cofs = 8 * r16 + 4 * (l >> 5) + 2 * ((l >> 4) & 1);
      int a_w = L_RED + w * 256 + (((cofs >> 3) ^ w) * 16) + (cofs & 7) * 2;
      int a_s = L_MISC + 1024 + w * 8;
      unsigned tag = TAG();
      KEEP(off); KEEP(offs); KEEP(a_w); KEEP(a_s); KEEP(tag);
      PIN();
      v4u a[4], sv[4];
      {
        unsigned pend = 15u;
        for (unsigned sp = 0; sp < SPIN_MAX; ++sp) {
          asm volatile("" ::: "memory");
#pragma unroll
          for (int k = 0; k < 4; ++k) if (pend >> k & 1u) sv[k] = ld16(offs + k * 2048);
#pragma unroll
          for (int k = 0; k < 4; ++k) if (pend >> k & 1u) { if (__all(((sv[k].y ^ tag) | (sv[k].w ^ tag)) == 0u)) pend &= ~(1u << k); }
          if (!pend) break;
        }
      }
      asm volatile("" ::: "memory");
#pragma unroll
      for (int k = 0; k < 4; ++k) a[k] = ld16(off + k * 4096);
      float mw = fmaxf(fmaxf(__uint_as_float(sv[0].x), __uint_as_float(sv[1].x)), fmaxf(__uint_as_float(sv[2].x), __uint_as_float(sv[3].x)));
      mw = x16_max(mw);
      float acc[8] = {0.f, 0.f, 0.f, 0.f, 0.f, 0.f, 0.f, 0.f}; float lw = 0.f;
#pragma unroll
      for (int k = 0; k < 4; ++k) {
        const float e = __builtin_amdgcn_exp2f(__uint_as_float(sv[k].x) - mw);
        const unsigned wlo = pk2(e, 0.f), whi = pk2(0.f, e);
        lw += __uint_as_float(whi) * __uint_as_float(sv[k].z);
#pragma unroll
        for (int d = 0; d < 4; ++d) { acc[2 * d] = dot2(a[k][d], wlo, acc[2 * d]); acc[2 * d + 1] = dot2(a[k][d], whi, acc[2 * d + 1]); }
      }
      const float u0 = rs16(rs32(acc[0], acc[4]), rs32(acc[2], acc[6])), u1 = rs16(rs32(acc[1], acc[5]), rs32(acc[3], acc[7]));
      *(LAS unsigned*)(L + a_w) = pk2(u0, u1);
      lw = x16_sum(lw);
      *(LAS f32x2*)(L + a_s) = f32x2{mw, lw};
    }
    {
      v4u f[8];
      if (w < 4) { tr8(w5, f); KEEP8(f); }
      const int row = r16 & 7;
      int a_a = L_RED + row * 256 + (((4 * w + g) ^ row) * 16);
      int a_st = L_MISC + 1024 + (g & 1) * 32;
      int a_w2 = L_RED2 + ((w * 2 + (g & 1)) * 128 + 8 * r16) * 4;
      int a_r2 = L_RED2 + ((w & 1) * 128 + 2 * l) * 4, o_x5 = X5 + (idx * 2 + (w & 1)) * 256 + l * 4;
      KEEP(a_a); KEEP(a_st); KEEP(a_w2); KEEP(a_r2); KEEP(o_x5);
      BAR();
      if (w < 4) {
        const v4u a = *(LAS v4u*)(L + a_a);
        const f32x4 s01 = *(LAS f32x4*)(L + a_st), s23 = *(LAS f32x4*)(L + a_st + 16);
        f32x4 d[8];
#pragma unroll
        for (int i = 0; i < 8; ++i) d[i] = mfma16(a, f[i], f32x4{0.f, 0.f, 0.f, 0.f});
        const float mb = fmaxf(fmaxf(s01[0], s01[2]), fmaxf(s23[0], s23[2]));
        float f0 = __builtin_amdgcn_exp2f(s01[0] - mb), f1 = __builtin_amdgcn_exp2f(s01[2] - mb), f2 = __builtin_amdgcn_exp2f(s23[0] - mb), f3 = __builtin_amdgcn_exp2f(s23[2] - mb);
        const float inv = __builtin_amdgcn_rcpf((f0 * s01[1] + f1 * s01[3]) + (f2 * s23[1] + f3 * s23[3]));
        f0 *= inv; f1 *= inv; f2 *= inv; f3 *= inv;
        float v[8];
#pragma unroll
        for (int i = 0; i < 8; ++i) v[i] = (d[i][0] * f0 + d[i][1] * f1) + (d[i][2] * f2 + d[i][3] * f3);
        if (g < 2) { *(LAS f32x4*)(L + a_w2) = f32x4{v[0], v[1], v[2], v[3]}; *(LAS f32x4*)(L + a_w2 + 16) = f32x4{v[4], v[5], v[6], v[7]}; }
      }
      BAR();
      if (w < 2) {
        f32x2 v[4];
#pragma unroll
        for (int k = 0; k < 4; ++k) v[k] = *(LAS f32x2*)(L + a_r2 + k * 1024);
        { const v4u q = quad4(pk2((v[0][0] + v[1][0]) + (v[2][0] + v[3][0]), (v[0][1] + v[1][1]) + (v[2][1] + v[3][1]))); if ((l & 3) == 0) st16(q, o_x5); }
      }
    }
    if (idx < 80) {
      const int pp = idx / 10, n = idx % 10;
      if (w < 4) {
        v4u f0[8], f1[8]; tr8(w6[0], f0); tr8(w6[1], f1);
        int off = X5 + ((4 * pp + (w >> 1)) * 8 + (l >> 3)) * 256 + 128 * (w & 1) + (l & 7) * 16;
        int a_gw = L_X5W + w * 2048 + (l >> 3) * 128 + (l & 7) * 16, a_gr = L_X5W + w * 2048 + (r16 & 7) * 128 + (r16 >> 3) * 64 + g * 16;
        int a_w = L_RED + (((w * 2 + (g >> 1)) * 2 + (g & 1)) * 64 + 8 * (r16 & 7)) * 4;
        KEEP8(f0); KEEP8(f1); KEEP(off); KEEP(a_w); KEEP(a_gw); KEEP(a_gr);
        PIN();
        v4u u0, u1;
        {
          bool p0 = true, p1 = true;
          for (unsigned sp = 0; sp < SPIN_MAX; ++sp) {
            asm volatile("" ::: "memory");
            if (p0) u0 = ld16(off);
            if (p1) u1 = ld16(off + 2 * 2048);
            if (p0) p0 = !__all(umax4(u0) != SENT);
            if (p1) p1 = !__all(umax4(u1) != SENT);
            if (!p0 && !p1) break;
          }
        }
        *(LAS v4u*)(L + a_gw) = u0; *(LAS v4u*)(L + a_gw + 1024) = u1;
        const v4u a0 = *(LAS v4u*)(L + a_gr), a1 = *(LAS v4u*)(L + a_gr + 1024);
        f32x4 d[8];
#pragma unroll
        for (int i = 0; i < 8; ++i) d[i] = mfma16(a0, f0[i], f32x4{0.f, 0.f, 0.f, 0.f});
#pragma unroll
        for (int i = 0; i < 8; ++i) d[i] = mfma16(a1, f1[i], d[i]);
        float s[8];
#pragma unroll
        for (int i = 0; i < 8; ++i) s[i] = rs16(d[i][0] + d[i][2], d[i][1] + d[i][3]);
        if ((g >> 1) == (r16 >> 3)) {
          *(LAS f32x4*)(L + a_w) = f32x4{s[0], s[1], s[2], s[3]}; *(LAS f32x4*)(L + a_w + 16) = f32x4{s[4], s[5], s[6], s[7]};
        }
      }
      int a_r = L_RED + ((l >> 5) * 64 + 2 * (l & 31)) * 4, o_x6 = X6 + ((pp * 2 + (l >> 5)) * DM + 64 * n + 2 * (l & 31)) * 2;
      KEEP(a_r); KEEP(o_x6);
      BAR();
      if (w == 0) {
        f32x2 v[8];
#pragma unroll
        for (int k = 0; k < 8; ++k) v[k] = *(LAS f32x2*)(L + a_r + k * 512);
        const float s0 = ((v[0][0] + v[1][0]) + (v[2][0] + v[3][0])) + ((v[4][0] + v[5][0]) + (v[6][0] + v[7][0]));
        const float s1 = ((v[0][1] + v[1][1]) + (v[2][1] + v[3][1])) + ((v[4][1] + v[5][1]) + (v[6][1] + v[7][1]));
        { const v4u q = quad4(pk2(s0, s1)); if ((l & 3) == 0) st16(q, o_x6); }
      }
    }
  }
}

extern "C" void kernel_launch(void* const* d_in, const int* in_sizes, int n_in, void* d_out, int out_size, void* d_ws, size_t ws_size, hipStream_t stream) {
  Params p;
  p.x = (const u16*)d_in[0]; p.ckv = (const u16*)d_in[1]; p.wq = (const u16*)d_in[2]; p.wuk = (const u16*)d_in[3]; p.wuv = (const u16*)d_in[4]; p.wo = (const u16*)d_in[5];
  p.wg = (const u16*)d_in[7]; p.wu = (const u16*)d_in[8]; p.wd = (const u16*)d_in[9]; p.c = (const u16*)d_in[10]; p.wmod = (const u16*)d_in[11];
  p.out = (u16*)d_out;
  p.ws = (unsigned char*)d_ws;
  mega<<<256, 512, 0, stream>>>(p);
}
```
